```python
import torch

def _softmax(x, axis=-1):
    return torch.softmax(x, dim=axis)
def _gelu(x):
    return torch.nn.functional.gelu(x, approximate="tanh")
def _silu(x):
    return torch.nn.functional.silu(x)
def _sigmoid(x):
    return torch.sigmoid(x)
def _layernorm(x, axis=-1, eps=1e-5):
    m = x.mean(dim=axis, keepdim=True)
    v = x.var(dim=axis, keepdim=True, unbiased=False)
    return (x - m) / torch.sqrt(v + eps)
def _scatter_add(tgt, idx, src, axis=0):
    return tgt.clone().index_add_(axis, idx, src)
def _segment_sum(src, seg, n):
    out = torch.zeros((n,) + src.shape[1:], dtype=src.dtype)
    return out.index_add_(0, seg, src)
def _pad(x, pw):
    flat = tuple(v for pair in reversed(pw) for v in pair)
    return torch.nn.functional.pad(x, flat)

kernel_name = 'gated_mix__sliding_local_dilated__dense_activation_relu__prompt'

def setup_inputs():

    _w = (lambda a: (lambda t: t.to(torch.bfloat16) if t.is_floating_point() else t)(torch.as_tensor(a)))
    def _A_setup():
        B, S, D = 1, 2048, 1024
        return {
            "x": _w((lambda *s: torch.randn(*s, dtype=torch.bfloat16) * 0.05)(B, S, D)),
            "wqkv": _w((lambda *s: torch.randn(*s, dtype=torch.bfloat16) * 0.8)(D, 3 * D)),
            "wo": _w((lambda *s: torch.randn(*s, dtype=torch.bfloat16) * 0.05)(D, D)),
        }
    _A_in = _A_setup()

    def _B_setup():
        B, S, D, Dff = 1, 2048, 1024, 3584
        return {
            "x": _w((lambda *s: torch.randn(*s, dtype=torch.bfloat16) * 0.05)(B, S, D)),
            "w1": _w((lambda *s: torch.randn(*s, dtype=torch.bfloat16) * 0.2)(D, Dff)),
            "w2": _w((lambda *s: torch.randn(*s, dtype=torch.bfloat16) * 0.2)(Dff, D)),
        }
    _B_in = _B_setup()

    return {
        "A_x": _A_in["x"],
        "A_wqkv": _A_in["wqkv"],
        "A_wo": _A_in["wo"],
        "B_x": _B_in["x"],
        "B_w1": _B_in["w1"],
        "B_w2": _B_in["w2"],
        "gate_w": _w((lambda *s: torch.randn(*s, dtype=torch.bfloat16) * 0.05)(1024) * 16.0),
    }

def reference(A_x, A_wqkv, A_wo, B_x, B_w1, B_w2, gate_w):
    def _A_ref(x, wqkv, wo):
        B, S, D = x.shape
        H = 32; Dh = D // H
        W, dil = 8, 2
        qkv = (lambda x, *d: x.permute(*d))((x @ wqkv).reshape(B, S, 3, H, Dh), 2, 0, 3, 1, 4)
        q, k, v = qkv[0], qkv[1], qkv[2]
        scale = Dh ** -0.5
        pos = torch.arange(S)
        off = torch.arange(W) * dil
        idx = pos[:, None] - off[None, :]
        mask = idx >= 0
        idx_c = torch.where(mask, idx, 0).reshape(-1)
        kg = k[:, :, idx_c, :].reshape(B, H, S, W, Dh)
        vg = v[:, :, idx_c, :].reshape(B, H, S, W, Dh)
        logits = torch.einsum("bhsd,bhswd->bhsw", q, kg) * scale
        logits = torch.where(mask[None, None, :, :], logits, -1e9)
        attn = _softmax(logits, axis=-1)
        o = torch.einsum("bhsw,bhswd->bhsd", attn, vg)
        return (lambda x, *d: x.permute(*d))(o, 0, 2, 1, 3).reshape(B, S, D) @ wo

    def _B_ref(x, w1, w2):
        h = torch.einsum("bsd,df->bsf", x, w1)
        zero = torch.zeros_like(h)
        h = torch.maximum(h, zero)
        return torch.einsum("bsf,fd->bsd", h, w2)

    x = A_x
    g = _sigmoid((x * gate_w).sum(-1, keepdims=True))
    a = _A_ref(x, A_wqkv, A_wo)
    b = _B_ref(x, B_w1, B_w2)
    return g * a + (1 - g) * b
```

```cpp
#include <hip/hip_runtime.h>
#include <hip/hip_bf16.h>
typedef unsigned short u16;
__device__ __forceinline__ float bf2f(u16 x){ return __uint_as_float(((unsigned)x)<<16); }
__device__ __forceinline__ u16 f2bf(float f){ unsigned u=__float_as_uint(f); u += 0x7fffu + ((u>>16)&1u); return (u16)(u>>16); }
template<int MODE>
__global__ void gemm_naive(const u16* A, const u16* B, u16* C, float* Cf, int M, int N, int K){
  __shared__ float As[16][17], Bs[16][17];
  int tx=threadIdx.x&15, ty=threadIdx.x>>4;
  int row=blockIdx.y*16+ty, col=blockIdx.x*16+tx;
  float acc=0;
  for(int k0=0;k0<K;k0+=16){
    As[ty][tx]=bf2f(A[(size_t)row*K+k0+tx]);
    Bs[ty][tx]=bf2f(B[(size_t)(k0+ty)*N+col]);
    __syncthreads();
    for(int k=0;k<16;k++) acc+=As[ty][k]*Bs[k][tx];
    __syncthreads();
  }
  if(MODE==1) acc=fmaxf(acc,0.f);
  if(MODE==2) Cf[(size_t)row*N+col]=acc; else C[(size_t)row*N+col]=f2bf(acc);
}
__global__ void attn_naive(const u16* qkv, u16* o){
  int idx=blockIdx.x*blockDim.x+threadIdx.x;
  int s=idx>>5,h=idx&31;
  float q[32]; for(int d=0;d<32;d++) q[d]=bf2f(qkv[(size_t)s*3072+h*32+d]);
  float lg[8]; float mx=-1e30f;
  for(int w=0;w<8;w++){ int p=s-2*w; float a=-1e9f; if(p>=0){ a=0; for(int d=0;d<32;d++) a+=q[d]*bf2f(qkv[(size_t)p*3072+1024+h*32+d]); a*=0.17677669529663687f;} lg[w]=a; mx=fmaxf(mx,a);}
  float sum=0; for(int w=0;w<8;w++){ lg[w]=__expf(lg[w]-mx); sum+=lg[w]; }
  for(int d=0;d<32;d++){ float a=0; for(int w=0;w<8;w++){ int p=s-2*w; if(p>=0) a+=lg[w]*bf2f(qkv[(size_t)p*3072+2048+h*32+d]); } o[(size_t)s*1024+h*32+d]=f2bf(a/sum);}
}
__global__ void final_mix(const u16* x,const u16* gw,const float* a,const float* b,u16* out){
  int s=blockIdx.x; __shared__ float red[256];
  float t=0; for(int d=threadIdx.x;d<1024;d+=256) t+=bf2f(x[(size_t)s*1024+d])*bf2f(gw[d]);
  red[threadIdx.x]=t; __syncthreads();
  for(int o=128;o>0;o>>=1){ if(threadIdx.x<o) red[threadIdx.x]+=red[threadIdx.x+o]; __syncthreads(); }
  float g=1.f/(1.f+__expf(-red[0]));
  for(int d=threadIdx.x;d<1024;d+=256) out[(size_t)s*1024+d]=f2bf(g*a[(size_t)s*1024+d]+(1.f-g)*b[(size_t)s*1024+d]);
}
extern "C" void kernel_launch(void* const* d_in, const int* in_sizes, int n_in, void* d_out, int out_size, void* d_ws, size_t ws_size, hipStream_t stream){
  const u16* x=(const u16*)d_in[0]; const u16* wqkv=(const u16*)d_in[1]; const u16* wo=(const u16*)d_in[2];
  const u16* w1=(const u16*)d_in[4]; const u16* w2=(const u16*)d_in[5]; const u16* gw=(const u16*)d_in[6];
  char* ws=(char*)d_ws;
  u16* qkv=(u16*)ws; u16* o=(u16*)(ws+(16<<20)); u16* h=(u16*)(ws+(32<<20)); float* a=(float*)(ws+(64<<20)); float* b=(float*)(ws+(80<<20));
  gemm_naive<0><<<dim3(3072/16,128),256,0,stream>>>(x,wqkv,qkv,nullptr,2048,3072,1024);
  gemm_naive<1><<<dim3(3584/16,128),256,0,stream>>>(x,w1,h,nullptr,2048,3584,1024);
  attn_naive<<<2048*32/256,256,0,stream>>>(qkv,o);
  gemm_naive<2><<<dim3(1024/16,128),256,0,stream>>>(o,wo,nullptr,a,2048,1024,1024);
  gemm_naive<2><<<dim3(1024/16,128),256,0,stream>>>(h,w2,nullptr,b,2048,1024,3584);
  final_mix<<<2048,256,0,stream>>>(x,gw,a,b,(u16*)d_out);
}
```

```cpp
#include <hip/hip_runtime.h>
#include <hip/hip_bf16.h>

typedef unsigned short u16;
constexpr size_t WS_A2 = 0;
constexpr size_t WS_G = 20u << 20;
constexpr size_t WS_PART = 21u << 20;

__global__ __launch_bounds__(256, 1) void gated_mix_kernel(const u16* __restrict__ x, const u16* __restrict__ wqkv, const u16* __restrict__ w1, const u16* __restrict__ gw,
                                                            char* ws, const u16* __restrict__ wo, const u16* __restrict__ w2,
                                                            const u16* __restrict__ wo_, const u16* __restrict__ w2_, char* ws_, u16* __restrict__ out) {
  __shared__ __attribute__((aligned(16))) char smem[155648];
  const unsigned lds = (unsigned)(size_t)smem, b = blockIdx.x;
  unsigned tid = threadIdx.x;
  const auto karg = __builtin_amdgcn_kernarg_segment_ptr();
  asm volatile(
  "s_getpc_b64 s[64:65]\n"
  "K0_PC:\n"
  "s_load_dwordx8 s[84:91], s[36:37], 0x0\n"
  "s_lshr_b32 s66, s38, 3\n"
  "v_and_b32 v1, 15, v0\n"
  "v_lshl_or_b32 v1, s66, 4, v1\n"
  "v_lshlrev_b32 v1, 7, v1\n"
  "v_min_u32 v1, K2_CODE_END-K0_PC-4, v1\n"
  "global_load_dword v27, v1, s[64:65]\n"
  "s_load_dwordx4 s[92:95], s[36:37], 0x20\n"
  "s_load_dwordx2 s[96:97], s[36:37], 0x30\n"
  "s_bitcmp1_b32 s38, 5\n"
  "s_cbranch_scc0 K1_TYPEA_%=\n"
  "v_readfirstlane_b32 s59, v0\n"
  "v_and_b32 v28, 63, v0\n"
  "v_lshrrev_b32 v29, 3, v28\n"
  "v_lshrrev_b32 v30, 4, v28\n"
  "s_lshr_b32 s59, s59, 6\n"
  "s_and_b32 s98, s38, 4\n"
  "s_lshr_b32 s69, s38, 6\n"
  "s_add_u32 s98, s98, s69\n"
  "s_and_b32 s99, s38, 3\n"
  "s_lshl_b32 s99, s99, 2\n"
  "s_bfe_u32 s69, s38, 0x20003\n"
  "s_add_u32 s99, s99, s69\n"
  "v_bfe_u32 v31, v28, 1, 2\n"
  "v_and_b32 v25, 1, v30\n"
  "v_xor_b32 v31, v31, v25\n"
  "v_lshlrev_b32 v31, 5, v31\n"
  "v_and_b32 v24, 1, v28\n"
  "v_lshl_or_b32 v31, v24, 4, v31\n"
  "v_mul_u32_u24 v21, 7168, v29\n"
  "v_add_u32 v23, v21, v31\n"
  "v_xor_b32 v22, 64, v31\n"
  "v_add_u32 v22, v21, v22\n"
  "v_add_u32 v9, 4096, v23\n"
  "v_add_u32 v10, 60416, v22\n"
  "v_add_u32 v11, 4224, v23\n"
  "v_add_u32 v12, 60544, v22\n"
  "v_add_u32 v13, 4352, v23\n"
  "v_add_u32 v14, 60672, v22\n"
  "s_mul_i32 s83, s99, 448\n"
  "s_and_b32 s69, s99, 1\n"
  "s_lshl_b32 s71, s69, 6\n"
  "s_add_u32 s83, s83, s71\n"
  "s_cmp_eq_u32 s69, 0\n"
  "s_cselect_b32 s71, 448, 0\n"
  "s_add_u32 s71, s71, 4032\n"
  "v_lshrrev_b32 v21, 2, v28\n"
  "v_mul_u32_u24 v21, 7168, v21\n"
  "v_and_b32 v20, 3, v28\n"
  "v_lshrrev_b32 v19, 5, v28\n"
  "v_xor_b32 v20, v20, v19\n"
  "v_lshlrev_b32 v20, 4, v20\n"
  "v_add3_u32 v8, v21, v20, s71\n"
  "s_mul_i32 s82, s59, 114688\n"
  "s_add_u32 s83, s83, s82\n"
  "s_sub_u32 s46, 7344128, s83\n"
  "s_mov_b32 s47, 0x20000\n"
  "s_lshl_b32 s69, s59, 11\n"
  "s_add_u32 s51, s39, 65536\n"
  "s_add_u32 s51, s51, s69\n"
  "s_add_u32 s52, s51, 86016\n"
  "s_add_u32 s51, s51, 28672\n"
  "s_and_b32 s48, s98, 4\n"
  "s_lshl_b32 s48, s48, 8\n"
  "s_mul_i32 s49, s48, 3584\n"
  "s_lshl_b32 s69, s59, 10\n"
  "s_sub_u32 s57, 24576, s69\n"
  "v_and_b32 v31, 7, v28\n"
  "v_xor_b32 v31, v31, v30\n"
  "v_lshlrev_b32 v31, 4, v31\n"
  "v_lshl_or_b32 v23, v29, 11, v31\n"
  "v_xor_b32 v22, 64, v23\n"
  "v_add_u32 v0, 4096, v23\n"
  "v_add_u32 v1, 19456, v22\n"
  "v_add_u32 v2, 34816, v23\n"
  "v_add_u32 v3, 50176, v22\n"
  "v_add_u32 v4, 69632, v23\n"
  "v_add_u32 v5, 84992, v22\n"
  "v_add_u32 v6, 100352, v23\n"
  "v_add_u32 v7, 115712, v22\n"
  "s_lshl_b32 s82, s98, 19\n"
  "s_lshl_b32 s69, s59, 17\n"
  "s_add_u32 s82, s82, s69\n"
  "s_mov_b32 s42, 135168\n"
  "s_mov_b32 s55, 135168\n"
  "s_mov_b32 s43, 0x20000\n"
  "s_lshl_b32 s69, s59, 13\n"
  "s_add_u32 s50, s39, s69\n"
  "s_sub_u32 s83, s83, 4096\n"
  "s_subb_u32 s71, 0, 0\n"
  "s_waitcnt lgkmcnt(0)\n"
  "s_add_u32 s44, s88, s83\n"
  "s_addc_u32 s45, s89, s71\n"
  "s_and_b32 s45, s45, 0xffff\n"
  "s_add_u32 m0, s51, 0\n"
  "s_nop 0\n"
  "buffer_load_dwordx4 v9, s[44:47], s49 offen offset:0 lds\n"
  "buffer_load_dwordx4 v10, s[44:47], s49 offen offset:1024 lds\n"
  "s_add_u32 m0, s51, 8192\n"
  "s_nop 0\n"
  "buffer_load_dwordx4 v11, s[44:47], s49 offen offset:0 lds\n"
  "buffer_load_dwordx4 v12, s[44:47], s49 offen offset:1024 lds\n"
  "s_add_u32 m0, s51, 16384\n"
  "s_nop 0\n"
  "buffer_load_dwordx4 v13, s[44:47], s49 offen offset:0 lds\n"
  "buffer_load_dwordx4 v14, s[44:47], s49 offen offset:1024 lds\n"
  "s_add_u32 m0, s51, s57\n"
  "s_nop 0\n"
  "buffer_load_dwordx4 v8, s[44:47], s49 offen lds\n"
  "s_add_u32 s49, s49, 458752\n"
  "s_cmp_eq_u32 s49, 7340032\n"
  "s_cselect_b32 s49, 0, s49\n"
  "s_add_u32 s51, s51, 28672\n"
  "s_cmp_ge_u32 s51, s52\n"
  "s_cselect_b32 s26, 86016, 0\n"
  "s_sub_u32 s51, s51, s26\n"
  "s_sub_u32 s83, s82, 4096\n"
  "s_subb_u32 s71, 0, 0\n"
  "s_add_u32 s40, s84, s83\n"
  "s_addc_u32 s41, s85, s71\n"
  "s_and_b32 s41, s41, 0xffff\n"
  "s_add_u32 m0, s50, 0\n"
  "s_nop 0\n"
  "buffer_load_dwordx4 v0, s[40:43], s48 offen offset:0 lds\n"
  "buffer_load_dwordx4 v1, s[40:43], s48 offen offset:1024 lds\n"
  "buffer_load_dwordx4 v2, s[40:43], s48 offen offset:2048 lds\n"
  "buffer_load_dwordx4 v3, s[40:43], s48 offen offset:3072 lds\n"
  "s_add_u32 m0, s50, 4096\n"
  "s_nop 0\n"
  "buffer_load_dwordx4 v4, s[40:43], s48 offen offset:0 lds\n"
  "buffer_load_dwordx4 v5, s[40:43], s48 offen offset:1024 lds\n"
  "buffer_load_dwordx4 v6, s[40:43], s48 offen offset:2048 lds\n"
  "buffer_load_dwordx4 v7, s[40:43], s48 offen offset:3072 lds\n"
  "s_add_u32 s48, s48, 128\n"
  "s_and_b32 s48, s48, 2047\n"
  "v_and_b32 v31, 15, v28\n"
  "v_bfe_u32 v24, v28, 1, 3\n"
  "v_xor_b32 v24, v24, v30\n"
  "v_lshlrev_b32 v24, 4, v24\n"
  "v_lshl_or_b32 v24, v31, 7, v24\n"
  "s_lshr_b32 s69, s59, 1\n"
  "s_lshl_b32 s69, s69, 14\n"
  "s_add_u32 s69, s69, s39\n"
  "v_add_u32 v16, s69, v24\n"
  "v_xor_b32 v17, 64, v16\n"
  "v_bfe_u32 v24, v28, 2, 2\n"
  "v_lshl_add_u32 v24, v30, 3, v24\n"
  "v_and_b32 v23, 3, v28\n"
  "v_lshlrev_b32 v23, 3, v23\n"
  "v_bfe_u32 v22, v28, 3, 2\n"
  "v_lshl_or_b32 v22, v22, 5, v23\n"
  "v_lshl_or_b32 v22, v24, 7, v22\n"
  "s_and_b32 s69, s59, 1\n"
  "s_add_u32 s71, s39, 94208\n"
  "s_lshl_b32 s82, s69, 14\n"
  "s_add_u32 s82, s82, s71\n"
  "v_add_u32 v18, s82, v22\n"
  "v_xor_b32 v19, 32, v18\n"
  "v_xor_b32 v20, 64, v18\n"
  "v_xor_b32 v21, 96, v18\n"
  "v_and_b32 v31, 1, v30\n"
  "v_xor_b32 v31, s69, v31\n"
  "v_and_b32 v23, 1, v28\n"
  "v_lshl_or_b32 v31, v23, 1, v31\n"
  "v_lshlrev_b32 v31, 4, v31\n"
  "v_bfe_u32 v23, v28, 1, 1\n"
  "v_lshl_or_b32 v31, v23, 3, v31\n"
  "v_lshl_or_b32 v31, v24, 6, v31\n"
  "s_add_u32 s82, s71, 24576\n"
  "v_add_u32 v24, s82, v31\n"
  "s_lshl_b32 s82, s69, 6\n"
  "v_xor_b32 v22, s82, v22\n"
  "s_add_u32 s82, s71, 8192\n"
  "v_add_u32 v22, s82, v22\n"
  "v_xor_b32 v23, 32, v22\n"
  "s_mov_b32 s70, 4294909952\n"
  "s_mov_b32 s56, 1\n"
  "s_waitcnt vmcnt(8)\n"
  "s_barrier\n"
  "ds_read_b64_tr_b16 a[0:1], v18 offset:0\n"
  "ds_read_b64_tr_b16 a[2:3], v18 offset:512\n"
  "ds_read_b64_tr_b16 a[8:9], v19 offset:0\n"
  "ds_read_b64_tr_b16 a[10:11], v19 offset:512\n"
  "ds_read_b64_tr_b16 a[16:17], v20 offset:0\n"
  "ds_read_b64_tr_b16 a[18:19], v20 offset:512\n"
  "ds_read_b64_tr_b16 a[24:25], v21 offset:0\n"
  "ds_read_b64_tr_b16 a[26:27], v21 offset:512\n"
  "ds_read_b64_tr_b16 a[32:33], v22 offset:0\n"
  "ds_read_b64_tr_b16 a[34:35], v22 offset:512\n"
  "ds_read_b64_tr_b16 a[40:41], v23 offset:0\n"
  "ds_read_b64_tr_b16 a[42:43], v23 offset:512\n"
  "ds_read_b64_tr_b16 a[48:49], v24 offset:0\n"
  "ds_read_b64_tr_b16 a[50:51], v24 offset:256\n"
  "ds_read_b64_tr_b16 a[4:5], v18 offset:4096\n"
  "ds_read_b64_tr_b16 a[6:7], v18 offset:4608\n"
  "ds_read_b64_tr_b16 a[12:13], v19 offset:4096\n"
  "ds_read_b64_tr_b16 a[14:15], v19 offset:4608\n"
  "ds_read_b64_tr_b16 a[20:21], v20 offset:4096\n"
  "ds_read_b64_tr_b16 a[22:23], v20 offset:4608\n"
  "ds_read_b64_tr_b16 a[28:29], v21 offset:4096\n"
  "ds_read_b64_tr_b16 a[30:31], v21 offset:4608\n"
  "ds_read_b64_tr_b16 a[36:37], v22 offset:4096\n"
  "ds_read_b64_tr_b16 a[38:39], v22 offset:4608\n"
  "ds_read_b64_tr_b16 a[44:45], v23 offset:4096\n"
  "ds_read_b64_tr_b16 a[46:47], v23 offset:4608\n"
  "ds_read_b64_tr_b16 a[52:53], v24 offset:2048\n"
  "ds_read_b64_tr_b16 a[54:55], v24 offset:2304\n"
  "s_add_u32 m0, s51, 0\n"
  "s_nop 0\n"
  "buffer_load_dwordx4 v9, s[44:47], s49 offen offset:0 lds\n"
  "buffer_load_dwordx4 v10, s[44:47], s49 offen offset:1024 lds\n"
  "s_add_u32 m0, s51, 8192\n"
  "s_nop 0\n"
  "buffer_load_dwordx4 v11, s[44:47], s49 offen offset:0 lds\n"
  "buffer_load_dwordx4 v12, s[44:47], s49 offen offset:1024 lds\n"
  "s_add_u32 m0, s51, 16384\n"
  "s_nop 0\n"
  "buffer_load_dwordx4 v13, s[44:47], s49 offen offset:0 lds\n"
  "buffer_load_dwordx4 v14, s[44:47], s49 offen offset:1024 lds\n"
  "s_add_u32 m0, s51, s57\n"
  "s_nop 0\n"
  "buffer_load_dwordx4 v8, s[44:47], s49 offen lds\n"
  "s_add_u32 s49, s49, 458752\n"
  "s_cmp_eq_u32 s49, 7340032\n"
  "s_cselect_b32 s49, 0, s49\n"
  "s_add_u32 s51, s51, 28672\n"
  "s_cmp_ge_u32 s51, s52\n"
  "s_cselect_b32 s26, 86016, 0\n"
  "s_sub_u32 s51, s51, s26\n"
  "s_cmp_eq_u32 s56, 0\n"
  "s_cselect_b32 s53, s70, 28672\n"
  "s_cmp_eq_u32 s56, 0\n"
  "s_cselect_b32 s56, 3, s56\n"
  "s_sub_u32 s56, s56, 1\n"
  "s_waitcnt vmcnt(7) lgkmcnt(0)\n"
  "s_barrier\n"
  "ds_read_b128 a[168:171], v16 offset:0\n"
  "ds_read_b128 a[172:175], v17 offset:0\n"
  "ds_read_b128 a[176:179], v16 offset:2048\n"
  "ds_read_b128 a[180:183], v17 offset:2048\n"
  "v_add_u32 v18, s53, v18\n"
  "v_add_u32 v19, s53, v19\n"
  "v_add_u32 v20, s53, v20\n"
  "v_add_u32 v21, s53, v21\n"
  "v_add_u32 v22, s53, v22\n"
  "v_add_u32 v23, s53, v23\n"
  "v_add_u32 v24, s53, v24\n"
  "s_waitcnt lgkmcnt(2)\n"
  "v_mfma_f32_16x16x32_bf16 v[32:35], a[0:3], a[168:171], 0\n"
  "ds_read_b128 a[184:187], v16 offset:4096\n"
  "v_mfma_f32_16x16x32_bf16 v[36:39], a[8:11], a[168:171], 0\n"
  "ds_read_b128 a[188:191], v17 offset:4096\n"
  "s_add_u32 m0, s50, 32768\n"
  "s_lshl_b32 s69, s38, 2\n"
  "s_add_u32 s69, s69, s59\n"
  "s_lshl_b32 s71, s69, 12\n"
  "v_mfma_f32_16x16x32_bf16 v[40:43], a[16:19], a[168:171], 0\n"
  "buffer_load_dwordx4 v0, s[40:43], s48 offen offset:0 lds\n"
  "s_add_u32 s72, s84, s71\n"
  "s_addc_u32 s73, s85, 0\n"
  "s_and_b32 s73, s73, 0xffff\n"
  "v_mfma_f32_16x16x32_bf16 v[44:47], a[24:27], a[168:171], 0\n"
  "buffer_load_dwordx4 v1, s[40:43], s48 offen offset:1024 lds\n"
  "s_mov_b32 s74, 4096\n"
  "s_mov_b32 s75, 0x20000\n"
  "v_lshlrev_b32 v26, 6, v28\n"
  "v_mfma_f32_16x16x32_bf16 v[48:51], a[32:35], a[168:171], 0\n"
  "s_lshl_b32 s69, s98, 4\n"
  "s_add_u32 s69, s69, s99\n"
  "v_mfma_f32_16x16x32_bf16 v[52:55], a[40:43], a[168:171], 0\n"
  "buffer_load_dwordx4 v2, s[40:43], s48 offen offset:2048 lds\n"
  "s_lshl_b32 s69, s69, 2\n"
  "s_add_u32 s69, s69, s59\n"
  "s_lshl_b32 s71, s69, 13\n"
  "v_mfma_f32_16x16x32_bf16 v[56:59], a[48:51], a[168:171], 0\n"
  "s_add_u32 s72, s84, s71\n"
  "s_addc_u32 s73, s85, 0\n"
  "s_and_b32 s73, s73, 0xffff\n"
  "v_mfma_f32_16x16x32_bf16 v[32:35], a[4:7], a[172:175], v[32:35]\n"
  "buffer_load_dwordx4 v3, s[40:43], s48 offen offset:3072 lds\n"
  "s_mov_b32 s74, 8192\n"
  "s_mov_b32 s76, s90\n"
  "s_and_b32 s77, s91, 0xffff\n"
  "v_mfma_f32_16x16x32_bf16 v[36:39], a[12:15], a[172:175], v[36:39]\n"
  "s_add_u32 m0, s50, 36864\n"
  "s_mov_b32 s78, 2048\n"
  "s_mov_b32 s79, 0x20000\n"
  "v_mfma_f32_16x16x32_bf16 v[40:43], a[20:23], a[172:175], v[40:43]\n"
  "buffer_load_dwordx4 v4, s[40:43], s48 offen offset:0 lds\n"
  "s_lshl_b32 s69, s69, 4\n"
  "s_add_u32 s69, s69, 20971520\n"
  "s_add_u32 s80, s92, s69\n"
  "s_addc_u32 s81, s93, 0\n"
  "v_mfma_f32_16x16x32_bf16 v[44:47], a[28:31], a[172:175], v[44:47]\n"
  "s_mov_b32 s17, 4096\n"
  "s_lshr_b32 s69, s59, 1\n"
  "v_mfma_f32_16x16x32_bf16 v[48:51], a[36:39], a[172:175], v[48:51]\n"
  "buffer_load_dwordx4 v5, s[40:43], s48 offen offset:1024 lds\n"
  "s_lshl_b32 s71, s98, 1\n"
  "s_add_u32 s69, s69, s71\n"
  "s_mul_i32 s69, s69, 1179648\n"
  "v_mfma_f32_16x16x32_bf16 v[52:55], a[44:47], a[172:175], v[52:55]\n"
  "s_add_u32 s28, s92, s69\n"
  "s_addc_u32 s29, s93, 0\n"
  "s_and_b32 s29, s29, 0xffff\n"
  "v_mfma_f32_16x16x32_bf16 v[56:59], a[52:55], a[172:175], v[56:59]\n"
  "buffer_load_dwordx4 v6, s[40:43], s48 offen offset:2048 lds\n"
  "s_mov_b32 s30, 1179648\n"
  "s_mov_b32 s31, 0x20000\n"
  "s_mov_b32 s69, s99\n"
  "s_waitcnt lgkmcnt(2)\n"
  "v_mfma_f32_16x16x32_bf16 v[60:63], a[0:3], a[176:179], 0\n"
  "ds_read_b128 a[192:195], v16 offset:6144\n"
  "s_and_b32 s71, s59, 1\n"
  "s_mul_i32 s69, s69, 7\n"
  "v_mfma_f32_16x16x32_bf16 v[64:67], a[8:11], a[176:179], 0\n"
  "ds_read_b128 a[196:199], v17 offset:6144\n"
  "s_add_u32 s69, s69, 32\n"
  "s_and_b32 s82, s99, 1\n"
  "s_cmp_eq_u32 s82, 0\n"
  "s_cselect_b32 s27, 6, 0\n"
  "v_mfma_f32_16x16x32_bf16 v[68:71], a[16:19], a[176:179], 0\n"
  "buffer_load_dwordx4 v7, s[40:43], s48 offen offset:3072 lds\n"
  "s_add_u32 s27, s27, s69\n"
  "s_add_u32 s69, s69, s82\n"
  "v_mfma_f32_16x16x32_bf16 v[72:75], a[24:27], a[176:179], 0\n"
  "s_add_u32 s48, s48, 128\n"
  "s_lshl_b32 s82, s71, 2\n"
  "s_add_u32 s33, s69, s82\n"
  "s_add_u32 s34, s33, 1\n"
  "v_mfma_f32_16x16x32_bf16 v[76:79], a[32:35], a[176:179], 0\n"
  "s_and_b32 s48, s48, 2047\n"
  "s_add_u32 m0, s51, 0\n"
  "s_add_u32 s35, s69, s71\n"
  "s_add_u32 s35, s35, 2\n"
  "v_mfma_f32_16x16x32_bf16 v[80:83], a[40:43], a[176:179], 0\n"
  "buffer_load_dwordx4 v9, s[44:47], s49 offen offset:0 lds\n"
  "s_lshr_b32 s83, s33, 1\n"
  "s_lshl_b32 s83, s83, 12\n"
  "s_and_b32 s33, s33, 1\n"
  "v_mfma_f32_16x16x32_bf16 v[84:87], a[48:51], a[176:179], 0\n"
  "s_lshl_b32 s33, s33, 10\n"
  "s_or_b32 s33, s33, s83\n"
  "v_mfma_f32_16x16x32_bf16 v[60:63], a[4:7], a[180:183], v[60:63]\n"
  "buffer_load_dwordx4 v10, s[44:47], s49 offen offset:1024 lds\n"
  "s_lshr_b32 s83, s34, 1\n"
  "s_lshl_b32 s83, s83, 12\n"
  "s_and_b32 s34, s34, 1\n"
  "v_mfma_f32_16x16x32_bf16 v[64:67], a[12:15], a[180:183], v[64:67]\n"
  "s_add_u32 m0, s51, 8192\n"
  "s_lshl_b32 s34, s34, 10\n"
  "s_or_b32 s34, s34, s83\n"
  "v_mfma_f32_16x16x32_bf16 v[68:71], a[20:23], a[180:183], v[68:71]\n"
  "buffer_load_dwordx4 v11, s[44:47], s49 offen offset:0 lds\n"
  "s_lshr_b32 s83, s35, 1\n"
  "s_lshl_b32 s83, s83, 12\n"
  "s_and_b32 s35, s35, 1\n"
  "v_mfma_f32_16x16x32_bf16 v[72:75], a[28:31], a[180:183], v[72:75]\n"
  "s_lshl_b32 s35, s35, 10\n"
  "s_or_b32 s35, s35, s83\n"
  "v_mfma_f32_16x16x32_bf16 v[76:79], a[36:39], a[180:183], v[76:79]\n"
  "buffer_load_dwordx4 v12, s[44:47], s49 offen offset:1024 lds\n"
  "s_lshr_b32 s83, s27, 1\n"
  "s_lshl_b32 s83, s83, 12\n"
  "s_and_b32 s27, s27, 1\n"
  "v_mfma_f32_16x16x32_bf16 v[80:83], a[44:47], a[180:183], v[80:83]\n"
  "s_add_u32 m0, s51, 16384\n"
  "s_lshl_b32 s27, s27, 10\n"
  "s_or_b32 s27, s27, s83\n"
  "v_mfma_f32_16x16x32_bf16 v[84:87], a[52:55], a[180:183], v[84:87]\n"
  "buffer_load_dwordx4 v13, s[44:47], s49 offen offset:0 lds\n"
  "s_lshl_b32 s69, s98, 8\n"
  "s_lshr_b32 s71, s99, 2\n"
  "s_lshl_b32 s71, s71, 6\n"
  "s_waitcnt lgkmcnt(2)\n"
  "v_mfma_f32_16x16x32_bf16 v[88:91], a[0:3], a[184:187], 0\n"
  "ds_read_b128 a[168:171], v16 offset:8192\n"
  "s_add_u32 s69, s69, s71\n"
  "s_and_b32 s71, s99, 3\n"
  "v_mfma_f32_16x16x32_bf16 v[92:95], a[8:11], a[184:187], 0\n"
  "ds_read_b128 a[172:175], v17 offset:8192\n"
  "s_lshl_b32 s71, s71, 3\n"
  "s_add_u32 s69, s69, s71\n"
  "s_add_u32 s69, s69, 21303296\n"
  "v_mfma_f32_16x16x32_bf16 v[96:99], a[16:19], a[184:187], 0\n"
  "buffer_load_dwordx4 v14, s[44:47], s49 offen offset:1024 lds\n"
  "s_add_u32 s12, s92, s69\n"
  "s_addc_u32 s13, s93, 0\n"
  "s_add_u32 s14, s92, 21561344\n"
  "s_addc_u32 s15, s93, 0\n"
  "v_mfma_f32_16x16x32_bf16 v[100:103], a[24:27], a[184:187], 0\n"
  "s_add_u32 m0, s51, s57\n"
  "v_mov_b32 v25, 0\n"
  "s_lshl_b32 s69, s38, 2\n"
  "s_add_u32 s69, s69, s59\n"
  "v_mfma_f32_16x16x32_bf16 v[104:107], a[32:35], a[184:187], 0\n"
  "buffer_load_dwordx4 v8, s[44:47], s49 offen lds\n"
  "s_lshl_b32 s69, s69, 8\n"
  "s_add_u32 s69, s69, 21037056\n"
  "v_mfma_f32_16x16x32_bf16 v[108:111], a[40:43], a[184:187], 0\n"
  "s_add_u32 s49, s49, 458752\n"
  "s_cmp_eq_u32 s49, 7340032\n"
  "s_cselect_b32 s49, 0, s49\n"
  "s_add_u32 s86, s92, s69\n"
  "s_addc_u32 s87, s93, 0\n"
  "v_and_b32 v30, 0xc0, v26\n"
  "s_lshl_b32 s69, s38, 15\n"
  "v_mfma_f32_16x16x32_bf16 v[112:115], a[48:51], a[184:187], 0\n"
  "s_add_u32 s51, s51, 28672\n"
  "s_sub_u32 s71, s38, 64\n"
  "s_mul_i32 s71, s71, 38272\n"
  "v_mfma_f32_16x16x32_bf16 v[88:91], a[4:7], a[188:191], v[88:91]\n"
  "s_cmp_ge_u32 s51, s52\n"
  "s_cselect_b32 s26, 86016, 0\n"
  "s_sub_u32 s82, 7340032, s71\n"
  "s_min_u32 s82, s82, 38272\n"
  "s_cmp_lt_u32 s38, 64\n"
  "s_cselect_b32 s69, s69, s71\n"
  "s_cselect_b32 s66, 32768, s82\n"
  "s_cselect_b64 s[64:65], s[94:95], s[96:97]\n"
  "v_mfma_f32_16x16x32_bf16 v[92:95], a[12:15], a[188:191], v[92:95]\n"
  "s_sub_u32 s51, s51, s26\n"
  "s_add_u32 s64, s64, s69\n"
  "s_addc_u32 s65, s65, 0\n"
  "s_and_b32 s65, s65, 0xffff\n"
  "v_mfma_f32_16x16x32_bf16 v[96:99], a[20:23], a[188:191], v[96:99]\n"
  "s_mov_b32 s67, 0x20000\n"
  "s_mul_i32 s68, s59, 1536\n"
  "s_add_u32 s68, s68, 67584\n"
  "v_mfma_f32_16x16x32_bf16 v[100:103], a[28:31], a[188:191], v[100:103]\n"
  "v_lshlrev_b32 v15, 7, v28\n"
  "v_mov_b32 v31, 0x40000000\n"
  "v_mfma_f32_16x16x32_bf16 v[104:107], a[36:39], a[188:191], v[104:107]\n"
  "v_cmp_gt_u32 vcc, 12, v28\n"
  "s_nop 1\n"
  "v_cndmask_b32 v15, v31, v15, vcc\n"
  "v_mfma_f32_16x16x32_bf16 v[108:111], a[44:47], a[188:191], v[108:111]\n"
  "global_load_dwordx2 a[200:201], v25, s[14:15] sc1\n"
  "v_mfma_f32_16x16x32_bf16 v[112:115], a[52:55], a[188:191], v[112:115]\n"
  "global_load_dword v27, v30, s[86:87]\n"
  "s_waitcnt lgkmcnt(2)\n"
  "v_mfma_f32_16x16x32_bf16 v[116:119], a[0:3], a[192:195], 0\n"
  "ds_read_b128 a[176:179], v16 offset:10240\n"
  "v_mfma_f32_16x16x32_bf16 v[120:123], a[8:11], a[192:195], 0\n"
  "ds_read_b128 a[180:183], v17 offset:10240\n"
  "v_mfma_f32_16x16x32_bf16 v[124:127], a[16:19], a[192:195], 0\n"
  "global_load_dword v27, v25, s[12:13]\n"
  "global_load_dword v27, v25, s[80:81]\n"
  "v_mfma_f32_16x16x32_bf16 v[128:131], a[24:27], a[192:195], 0\n"
  "v_mfma_f32_16x16x32_bf16 v[132:135], a[32:35], a[192:195], 0\n"
  "s_add_u32 m0, s51, 0\n"
  "v_mfma_f32_16x16x32_bf16 v[136:139], a[40:43], a[192:195], 0\n"
  "buffer_load_dwordx4 v9, s[44:47], s49 offen offset:0 lds\n"
  "v_mfma_f32_16x16x32_bf16 v[140:143], a[48:51], a[192:195], 0\n"
  "v_mfma_f32_16x16x32_bf16 v[116:119], a[4:7], a[196:199], v[116:119]\n"
  "v_mfma_f32_16x16x32_bf16 v[120:123], a[12:15], a[196:199], v[120:123]\n"
  "v_mfma_f32_16x16x32_bf16 v[124:127], a[20:23], a[196:199], v[124:127]\n"
  "buffer_load_dwordx4 v10, s[44:47], s49 offen offset:1024 lds\n"
  "v_mfma_f32_16x16x32_bf16 v[128:131], a[28:31], a[196:199], v[128:131]\n"
  "v_mfma_f32_16x16x32_bf16 v[132:135], a[36:39], a[196:199], v[132:135]\n"
  "v_mfma_f32_16x16x32_bf16 v[136:139], a[44:47], a[196:199], v[136:139]\n"
  "v_mfma_f32_16x16x32_bf16 v[140:143], a[52:55], a[196:199], v[140:143]\n"
  "s_waitcnt lgkmcnt(2)\n"
  "v_mfma_f32_16x16x32_bf16 v[144:147], a[0:3], a[168:171], 0\n"
  "ds_read_b128 a[184:187], v16 offset:12288\n"
  "v_mfma_f32_16x16x32_bf16 v[148:151], a[8:11], a[168:171], 0\n"
  "ds_read_b128 a[188:191], v17 offset:12288\n"
  "s_add_u32 m0, s51, 8192\n"
  "s_waitcnt vmcnt(21)\n"
  "s_barrier\n"
  "v_mfma_f32_16x16x32_bf16 v[152:155], a[16:19], a[168:171], 0\n"
  "buffer_load_dwordx4 v11, s[44:47], s49 offen offset:0 lds\n"
  "v_mfma_f32_16x16x32_bf16 v[156:159], a[24:27], a[168:171], 0\n"
  "ds_read_b64_tr_b16 a[56:57], v18 offset:0\n"
  "v_mfma_f32_16x16x32_bf16 v[160:163], a[32:35], a[168:171], 0\n"
  "buffer_load_dwordx4 v12, s[44:47], s49 offen offset:1024 lds\n"
  "v_mfma_f32_16x16x32_bf16 v[164:167], a[40:43], a[168:171], 0\n"
  "ds_read_b64_tr_b16 a[58:59], v18 offset:512\n"
  "v_mfma_f32_16x16x32_bf16 v[168:171], a[48:51], a[168:171], 0\n"
  "ds_read_b64_tr_b16 a[64:65], v19 offset:0\n"
  "v_mfma_f32_16x16x32_bf16 v[144:147], a[4:7], a[172:175], v[144:147]\n"
  "ds_read_b64_tr_b16 a[66:67], v19 offset:512\n"
  "v_mfma_f32_16x16x32_bf16 v[148:151], a[12:15], a[172:175], v[148:151]\n"
  "s_add_u32 m0, s51, 16384\n"
  "ds_read_b64_tr_b16 a[72:73], v20 offset:0\n"
  "v_mfma_f32_16x16x32_bf16 v[152:155], a[20:23], a[172:175], v[152:155]\n"
  "buffer_load_dwordx4 v13, s[44:47], s49 offen offset:0 lds\n"
  "v_mfma_f32_16x16x32_bf16 v[156:159], a[28:31], a[172:175], v[156:159]\n"
  "ds_read_b64_tr_b16 a[74:75], v20 offset:512\n"
  "v_mfma_f32_16x16x32_bf16 v[160:163], a[36:39], a[172:175], v[160:163]\n"
  "ds_read_b64_tr_b16 a[80:81], v21 offset:0\n"
  "v_mfma_f32_16x16x32_bf16 v[164:167], a[44:47], a[172:175], v[164:167]\n"
  "ds_read_b64_tr_b16 a[82:83], v21 offset:512\n"
  "v_mfma_f32_16x16x32_bf16 v[168:171], a[52:55], a[172:175], v[168:171]\n"
  "buffer_load_dwordx4 v14, s[44:47], s49 offen offset:1024 lds\n"
  "s_waitcnt lgkmcnt(10)\n"
  "v_mfma_f32_16x16x32_bf16 v[172:175], a[0:3], a[176:179], 0\n"
  "ds_read_b128 a[192:195], v16 offset:14336\n"
  "v_mfma_f32_16x16x32_bf16 v[176:179], a[8:11], a[176:179], 0\n"
  "ds_read_b128 a[196:199], v17 offset:14336\n"
  "v_mfma_f32_16x16x32_bf16 v[180:183], a[16:19], a[176:179], 0\n"
  "ds_read_b64_tr_b16 a[88:89], v22 offset:0\n"
  "v_mfma_f32_16x16x32_bf16 v[184:187], a[24:27], a[176:179], 0\n"
  "s_add_u32 m0, s51, s57\n"
  "ds_read_b64_tr_b16 a[90:91], v22 offset:512\n"
  "v_mfma_f32_16x16x32_bf16 v[188:191], a[32:35], a[176:179], 0\n"
  "buffer_load_dwordx4 v8, s[44:47], s49 offen lds\n"
  "v_mfma_f32_16x16x32_bf16 v[192:195], a[40:43], a[176:179], 0\n"
  "s_add_u32 s49, s49, 458752\n"
  "ds_read_b64_tr_b16 a[96:97], v23 offset:0\n"
  "v_mfma_f32_16x16x32_bf16 v[196:199], a[48:51], a[176:179], 0\n"
  "s_cmp_eq_u32 s49, 7340032\n"
  "s_cselect_b32 s49, 0, s49\n"
  "ds_read_b64_tr_b16 a[98:99], v23 offset:512\n"
  "v_mfma_f32_16x16x32_bf16 v[172:175], a[4:7], a[180:183], v[172:175]\n"
  "ds_read_b64_tr_b16 a[104:105], v24 offset:0\n"
  "v_mfma_f32_16x16x32_bf16 v[176:179], a[12:15], a[180:183], v[176:179]\n"
  "s_add_u32 s51, s51, 28672\n"
  "ds_read_b64_tr_b16 a[106:107], v24 offset:256\n"
  "v_mfma_f32_16x16x32_bf16 v[180:183], a[20:23], a[180:183], v[180:183]\n"
  "ds_read_b64_tr_b16 a[60:61], v18 offset:4096\n"
  "v_mfma_f32_16x16x32_bf16 v[184:187], a[28:31], a[180:183], v[184:187]\n"
  "s_cmp_ge_u32 s51, s52\n"
  "s_cselect_b32 s26, 86016, 0\n"
  "ds_read_b64_tr_b16 a[62:63], v18 offset:4608\n"
  "v_mfma_f32_16x16x32_bf16 v[188:191], a[36:39], a[180:183], v[188:191]\n"
  "ds_read_b64_tr_b16 a[68:69], v19 offset:4096\n"
  "ds_read_b64_tr_b16 a[70:71], v19 offset:4608\n"
  "ds_read_b64_tr_b16 a[76:77], v20 offset:4096\n"
  "ds_read_b64_tr_b16 a[78:79], v20 offset:4608\n"
  "ds_read_b64_tr_b16 a[84:85], v21 offset:4096\n"
  "ds_read_b64_tr_b16 a[86:87], v21 offset:4608\n"
  "ds_read_b64_tr_b16 a[92:93], v22 offset:4096\n"
  "ds_read_b64_tr_b16 a[94:95], v22 offset:4608\n"
  "ds_read_b64_tr_b16 a[100:101], v23 offset:4096\n"
  "ds_read_b64_tr_b16 a[102:103], v23 offset:4608\n"
  "ds_read_b64_tr_b16 a[108:109], v24 offset:2048\n"
  "ds_read_b64_tr_b16 a[110:111], v24 offset:2304\n"
  "v_mfma_f32_16x16x32_bf16 v[192:195], a[44:47], a[180:183], v[192:195]\n"
  "s_sub_u32 s51, s51, s26\n"
  "v_mfma_f32_16x16x32_bf16 v[196:199], a[52:55], a[180:183], v[196:199]\n"
  "s_waitcnt vmcnt(11) lgkmcnt(0)\n"
  "s_barrier\n"
  "v_mfma_f32_16x16x32_bf16 v[200:203], a[0:3], a[184:187], 0\n"
  "ds_read_b128 a[168:171], v16 offset:32768\n"
  "v_mfma_f32_16x16x32_bf16 v[204:207], a[8:11], a[184:187], 0\n"
  "ds_read_b128 a[172:175], v17 offset:32768\n"
  "v_mfma_f32_16x16x32_bf16 v[208:211], a[16:19], a[184:187], 0\n"
  "s_add_u32 m0, s50, 0\n"
  "v_mfma_f32_16x16x32_bf16 v[212:215], a[24:27], a[184:187], 0\n"
  "buffer_load_dwordx4 v0, s[40:43], s48 offen offset:0 lds\n"
  "v_mfma_f32_16x16x32_bf16 v[216:219], a[32:35], a[184:187], 0\n"
  "s_cmp_eq_u32 s56, 0\n"
  "s_cselect_b32 s53, s70, 28672\n"
  "v_mfma_f32_16x16x32_bf16 v[220:223], a[40:43], a[184:187], 0\n"
  "v_mfma_f32_16x16x32_bf16 v[224:227], a[48:51], a[184:187], 0\n"
  "s_cmp_eq_u32 s56, 0\n"
  "s_cselect_b32 s56, 3, s56\n"
  "v_mfma_f32_16x16x32_bf16 v[200:203], a[4:7], a[188:191], v[200:203]\n"
  "buffer_load_dwordx4 v1, s[40:43], s48 offen offset:1024 lds\n"
  "v_mfma_f32_16x16x32_bf16 v[204:207], a[12:15], a[188:191], v[204:207]\n"
  "s_sub_u32 s56, s56, 1\n"
  "v_mfma_f32_16x16x32_bf16 v[208:211], a[20:23], a[188:191], v[208:211]\n"
  "v_add_u32 v18, s53, v18\n"
  "v_mfma_f32_16x16x32_bf16 v[212:215], a[28:31], a[188:191], v[212:215]\n"
  "v_mfma_f32_16x16x32_bf16 v[216:219], a[36:39], a[188:191], v[216:219]\n"
  "v_add_u32 v19, s53, v19\n"
  "v_mfma_f32_16x16x32_bf16 v[220:223], a[44:47], a[188:191], v[220:223]\n"
  "buffer_load_dwordx4 v2, s[40:43], s48 offen offset:2048 lds\n"
  "v_mfma_f32_16x16x32_bf16 v[224:227], a[52:55], a[188:191], v[224:227]\n"
  "v_mfma_f32_16x16x32_bf16 v[228:231], a[0:3], a[192:195], 0\n"
  "ds_read_b128 a[176:179], v16 offset:34816\n"
  "v_mfma_f32_16x16x32_bf16 v[232:235], a[8:11], a[192:195], 0\n"
  "ds_read_b128 a[180:183], v17 offset:34816\n"
  "v_mfma_f32_16x16x32_bf16 v[236:239], a[16:19], a[192:195], 0\n"
  "v_add_u32 v20, s53, v20\n"
  "v_add_u32 v21, s53, v21\n"
  "v_mfma_f32_16x16x32_bf16 v[240:243], a[24:27], a[192:195], 0\n"
  "buffer_load_dwordx4 v3, s[40:43], s48 offen offset:3072 lds\n"
  "v_mfma_f32_16x16x32_bf16 v[244:247], a[32:35], a[192:195], 0\n"
  "v_mfma_f32_16x16x32_bf16 v[248:251], a[40:43], a[192:195], 0\n"
  "v_add_u32 v22, s53, v22\n"
  "v_mfma_f32_16x16x32_bf16 v[252:255], a[48:51], a[192:195], 0\n"
  "v_mfma_f32_16x16x32_bf16 v[228:231], a[4:7], a[196:199], v[228:231]\n"
  "s_add_u32 m0, s50, 4096\n"
  "v_add_u32 v23, s53, v23\n"
  "v_mfma_f32_16x16x32_bf16 v[232:235], a[12:15], a[196:199], v[232:235]\n"
  "buffer_load_dwordx4 v4, s[40:43], s48 offen offset:0 lds\n"
  "v_mfma_f32_16x16x32_bf16 v[236:239], a[20:23], a[196:199], v[236:239]\n"
  "v_mfma_f32_16x16x32_bf16 v[240:243], a[28:31], a[196:199], v[240:243]\n"
  "v_add_u32 v24, s53, v24\n"
  "v_mfma_f32_16x16x32_bf16 v[244:247], a[36:39], a[196:199], v[244:247]\n"
  "v_mfma_f32_16x16x32_bf16 v[248:251], a[44:47], a[196:199], v[248:251]\n"
  "v_mfma_f32_16x16x32_bf16 v[252:255], a[52:55], a[196:199], v[252:255]\n"
  "s_getpc_b64 s[82:83]\n"
  "K1B_PC2:\n"
  "s_sub_u32 s82, s82, K1B_PC2-K0_PC\n"
  "s_subb_u32 s83, s83, 0\n"
  "s_lshr_b32 s69, s38, 3\n"
  "v_and_b32 v31, 15, v28\n"
  "v_lshl_or_b32 v31, s69, 4, v31\n"
  "v_lshlrev_b32 v31, 7, v31\n"
  "v_min_u32 v31, K2_CODE_END-K0_PC-4, v31\n"
  "s_mov_b32 s54, 6\n"
  ".p2align 6\n"
  "s_nop 0\n"
  "K1B_LOOP_%=:\n"
  "s_waitcnt lgkmcnt(2)\n"
  "v_mfma_f32_16x16x32_bf16 v[32:35], a[56:59], a[168:171], v[32:35]\n"
  "ds_read_b128 a[184:187], v16 offset:36864\n"
  "v_mfma_f32_16x16x32_bf16 v[36:39], a[64:67], a[168:171], v[36:39]\n"
  "ds_read_b128 a[188:191], v17 offset:36864\n"
  "v_mfma_f32_16x16x32_bf16 v[40:43], a[72:75], a[168:171], v[40:43]\n"
  "buffer_load_dwordx4 v5, s[40:43], s48 offen offset:1024 lds\n"
  "v_mfma_f32_16x16x32_bf16 v[44:47], a[80:83], a[168:171], v[44:47]\n"
  "ds_read_b64_tr_b16 a[0:1], v18 offset:0\n"
  "v_mfma_f32_16x16x32_bf16 v[48:51], a[88:91], a[168:171], v[48:51]\n"
  "buffer_load_dwordx4 v6, s[40:43], s48 offen offset:2048 lds\n"
  "v_mfma_f32_16x16x32_bf16 v[52:55], a[96:99], a[168:171], v[52:55]\n"
  "ds_read_b64_tr_b16 a[2:3], v18 offset:512\n"
  "v_mfma_f32_16x16x32_bf16 v[56:59], a[104:107], a[168:171], v[56:59]\n"
  "v_mfma_f32_16x16x32_bf16 v[32:35], a[60:63], a[172:175], v[32:35]\n"
  "ds_read_b64_tr_b16 a[8:9], v19 offset:0\n"
  "v_mfma_f32_16x16x32_bf16 v[36:39], a[68:71], a[172:175], v[36:39]\n"
  "v_mfma_f32_16x16x32_bf16 v[40:43], a[76:79], a[172:175], v[40:43]\n"
  "buffer_load_dwordx4 v7, s[40:43], s48 offen offset:3072 lds\n"
  "v_mfma_f32_16x16x32_bf16 v[44:47], a[84:87], a[172:175], v[44:47]\n"
  "s_add_u32 s48, s48, 128\n"
  "ds_read_b64_tr_b16 a[10:11], v19 offset:512\n"
  "v_mfma_f32_16x16x32_bf16 v[48:51], a[92:95], a[172:175], v[48:51]\n"
  "s_and_b32 s48, s48, 2047\n"
  "v_mfma_f32_16x16x32_bf16 v[52:55], a[100:103], a[172:175], v[52:55]\n"
  "ds_read_b64_tr_b16 a[16:17], v20 offset:0\n"
  "v_mfma_f32_16x16x32_bf16 v[56:59], a[108:111], a[172:175], v[56:59]\n"
  "s_waitcnt lgkmcnt(7)\n"
  "v_mfma_f32_16x16x32_bf16 v[60:63], a[56:59], a[176:179], v[60:63]\n"
  "ds_read_b128 a[192:195], v16 offset:38912\n"
  "v_mfma_f32_16x16x32_bf16 v[64:67], a[64:67], a[176:179], v[64:67]\n"
  "ds_read_b128 a[196:199], v17 offset:38912\n"
  "v_mfma_f32_16x16x32_bf16 v[68:71], a[72:75], a[176:179], v[68:71]\n"
  "s_add_u32 m0, s51, 0\n"
  "ds_read_b64_tr_b16 a[18:19], v20 offset:512\n"
  "v_mfma_f32_16x16x32_bf16 v[72:75], a[80:83], a[176:179], v[72:75]\n"
  "buffer_load_dwordx4 v9, s[44:47], s49 offen offset:0 lds\n"
  "v_mfma_f32_16x16x32_bf16 v[76:79], a[88:91], a[176:179], v[76:79]\n"
  "ds_read_b64_tr_b16 a[24:25], v21 offset:0\n"
  "v_mfma_f32_16x16x32_bf16 v[80:83], a[96:99], a[176:179], v[80:83]\n"
  "buffer_load_dword v27, v15, s[64:67], s68 offen\n"
  "v_mfma_f32_16x16x32_bf16 v[84:87], a[104:107], a[176:179], v[84:87]\n"
  "s_sub_u32 s68, s68, 6144\n"
  "ds_read_b64_tr_b16 a[26:27], v21 offset:512\n"
  "v_mfma_f32_16x16x32_bf16 v[60:63], a[60:63], a[180:183], v[60:63]\n"
  "v_mfma_f32_16x16x32_bf16 v[64:67], a[68:71], a[180:183], v[64:67]\n"
  "ds_read_b64_tr_b16 a[32:33], v22 offset:0\n"
  "v_mfma_f32_16x16x32_bf16 v[68:71], a[76:79], a[180:183], v[68:71]\n"
  "global_load_dword v27, v31, s[82:83]\n"
  "v_mfma_f32_16x16x32_bf16 v[72:75], a[84:87], a[180:183], v[72:75]\n"
  "v_mfma_f32_16x16x32_bf16 v[76:79], a[92:95], a[180:183], v[76:79]\n"
  "ds_read_b64_tr_b16 a[34:35], v22 offset:512\n"
  "v_mfma_f32_16x16x32_bf16 v[80:83], a[100:103], a[180:183], v[80:83]\n"
  "buffer_load_dwordx4 v10, s[44:47], s49 offen offset:1024 lds\n"
  "v_mfma_f32_16x16x32_bf16 v[84:87], a[108:111], a[180:183], v[84:87]\n"
  "s_waitcnt lgkmcnt(12)\n"
  "v_mfma_f32_16x16x32_bf16 v[88:91], a[56:59], a[184:187], v[88:91]\n"
  "ds_read_b128 a[168:171], v16 offset:40960\n"
  "v_mfma_f32_16x16x32_bf16 v[92:95], a[64:67], a[184:187], v[92:95]\n"
  "ds_read_b128 a[172:175], v17 offset:40960\n"
  "v_mfma_f32_16x16x32_bf16 v[96:99], a[72:75], a[184:187], v[96:99]\n"
  "ds_read_b64_tr_b16 a[40:41], v23 offset:0\n"
  "v_mfma_f32_16x16x32_bf16 v[100:103], a[80:83], a[184:187], v[100:103]\n"
  "ds_read_b64_tr_b16 a[42:43], v23 offset:512\n"
  "v_mfma_f32_16x16x32_bf16 v[104:107], a[88:91], a[184:187], v[104:107]\n"
  "v_mfma_f32_16x16x32_bf16 v[108:111], a[96:99], a[184:187], v[108:111]\n"
  "ds_read_b64_tr_b16 a[48:49], v24 offset:0\n"
  "v_mfma_f32_16x16x32_bf16 v[112:115], a[104:107], a[184:187], v[112:115]\n"
  "s_add_u32 m0, s51, 8192\n"
  "v_mfma_f32_16x16x32_bf16 v[88:91], a[60:63], a[188:191], v[88:91]\n"
  "buffer_load_dwordx4 v11, s[44:47], s49 offen offset:0 lds\n"
  "v_mfma_f32_16x16x32_bf16 v[92:95], a[68:71], a[188:191], v[92:95]\n"
  "ds_read_b64_tr_b16 a[50:51], v24 offset:256\n"
  "v_mfma_f32_16x16x32_bf16 v[96:99], a[76:79], a[188:191], v[96:99]\n"
  "v_mfma_f32_16x16x32_bf16 v[100:103], a[84:87], a[188:191], v[100:103]\n"
  "ds_read_b64_tr_b16 a[4:5], v18 offset:4096\n"
  "v_mfma_f32_16x16x32_bf16 v[104:107], a[92:95], a[188:191], v[104:107]\n"
  "v_mfma_f32_16x16x32_bf16 v[108:111], a[100:103], a[188:191], v[108:111]\n"
  "v_mfma_f32_16x16x32_bf16 v[112:115], a[108:111], a[188:191], v[112:115]\n"
  "ds_read_b64_tr_b16 a[6:7], v18 offset:4608\n"
  "s_waitcnt lgkmcnt(13)\n"
  "v_mfma_f32_16x16x32_bf16 v[116:119], a[56:59], a[192:195], v[116:119]\n"
  "ds_read_b128 a[176:179], v16 offset:43008\n"
  "v_mfma_f32_16x16x32_bf16 v[120:123], a[64:67], a[192:195], v[120:123]\n"
  "ds_read_b128 a[180:183], v17 offset:43008\n"
  "v_mfma_f32_16x16x32_bf16 v[124:127], a[72:75], a[192:195], v[124:127]\n"
  "ds_read_b64_tr_b16 a[12:13], v19 offset:4096\n"
  "v_mfma_f32_16x16x32_bf16 v[128:131], a[80:83], a[192:195], v[128:131]\n"
  "buffer_load_dwordx4 v12, s[44:47], s49 offen offset:1024 lds\n"
  "v_mfma_f32_16x16x32_bf16 v[132:135], a[88:91], a[192:195], v[132:135]\n"
  "ds_read_b64_tr_b16 a[14:15], v19 offset:4608\n"
  "v_mfma_f32_16x16x32_bf16 v[136:139], a[96:99], a[192:195], v[136:139]\n"
  "v_mfma_f32_16x16x32_bf16 v[140:143], a[104:107], a[192:195], v[140:143]\n"
  "ds_read_b64_tr_b16 a[20:21], v20 offset:4096\n"
  "v_mfma_f32_16x16x32_bf16 v[116:119], a[60:63], a[196:199], v[116:119]\n"
  "v_mfma_f32_16x16x32_bf16 v[120:123], a[68:71], a[196:199], v[120:123]\n"
  "v_mfma_f32_16x16x32_bf16 v[124:127], a[76:79], a[196:199], v[124:127]\n"
  "ds_read_b64_tr_b16 a[22:23], v20 offset:4608\n"
  "v_mfma_f32_16x16x32_bf16 v[128:131], a[84:87], a[196:199], v[128:131]\n"
  "v_mfma_f32_16x16x32_bf16 v[132:135], a[92:95], a[196:199], v[132:135]\n"
  "s_add_u32 m0, s51, 16384\n"
  "v_mfma_f32_16x16x32_bf16 v[136:139], a[100:103], a[196:199], v[136:139]\n"
  "buffer_load_dwordx4 v13, s[44:47], s49 offen offset:0 lds\n"
  "v_mfma_f32_16x16x32_bf16 v[140:143], a[108:111], a[196:199], v[140:143]\n"
  "ds_read_b64_tr_b16 a[28:29], v21 offset:4096\n"
  "s_waitcnt lgkmcnt(13)\n"
  "v_mfma_f32_16x16x32_bf16 v[144:147], a[56:59], a[168:171], v[144:147]\n"
  "ds_read_b128 a[184:187], v16 offset:45056\n"
  "v_mfma_f32_16x16x32_bf16 v[148:151], a[64:67], a[168:171], v[148:151]\n"
  "ds_read_b128 a[188:191], v17 offset:45056\n"
  "v_mfma_f32_16x16x32_bf16 v[152:155], a[72:75], a[168:171], v[152:155]\n"
  "ds_read_b64_tr_b16 a[30:31], v21 offset:4608\n"
  "v_mfma_f32_16x16x32_bf16 v[156:159], a[80:83], a[168:171], v[156:159]\n"
  "ds_read_b64_tr_b16 a[36:37], v22 offset:4096\n"
  "v_mfma_f32_16x16x32_bf16 v[160:163], a[88:91], a[168:171], v[160:163]\n"
  "v_mfma_f32_16x16x32_bf16 v[164:167], a[96:99], a[168:171], v[164:167]\n"
  "ds_read_b64_tr_b16 a[38:39], v22 offset:4608\n"
  "v_mfma_f32_16x16x32_bf16 v[168:171], a[104:107], a[168:171], v[168:171]\n"
  "v_mfma_f32_16x16x32_bf16 v[144:147], a[60:63], a[172:175], v[144:147]\n"
  "buffer_load_dwordx4 v14, s[44:47], s49 offen offset:1024 lds\n"
  "v_mfma_f32_16x16x32_bf16 v[148:151], a[68:71], a[172:175], v[148:151]\n"
  "ds_read_b64_tr_b16 a[44:45], v23 offset:4096\n"
  "v_mfma_f32_16x16x32_bf16 v[152:155], a[76:79], a[172:175], v[152:155]\n"
  "v_mfma_f32_16x16x32_bf16 v[156:159], a[84:87], a[172:175], v[156:159]\n"
  "v_mfma_f32_16x16x32_bf16 v[160:163], a[92:95], a[172:175], v[160:163]\n"
  "ds_read_b64_tr_b16 a[46:47], v23 offset:4608\n"
  "v_mfma_f32_16x16x32_bf16 v[164:167], a[100:103], a[172:175], v[164:167]\n"
  "v_mfma_f32_16x16x32_bf16 v[168:171], a[108:111], a[172:175], v[168:171]\n"
  "ds_read_b64_tr_b16 a[52:53], v24 offset:2048\n"
  "s_waitcnt lgkmcnt(13)\n"
  "v_mfma_f32_16x16x32_bf16 v[172:175], a[56:59], a[176:179], v[172:175]\n"
  "ds_read_b128 a[192:195], v16 offset:47104\n"
  "v_mfma_f32_16x16x32_bf16 v[176:179], a[64:67], a[176:179], v[176:179]\n"
  "ds_read_b128 a[196:199], v17 offset:47104\n"
  "v_mfma_f32_16x16x32_bf16 v[180:183], a[72:75], a[176:179], v[180:183]\n"
  "s_add_u32 m0, s51, s57\n"
  "ds_read_b64_tr_b16 a[54:55], v24 offset:2304\n"
  "v_mfma_f32_16x16x32_bf16 v[184:187], a[80:83], a[176:179], v[184:187]\n"
  "buffer_load_dwordx4 v8, s[44:47], s49 offen lds\n"
  "v_mfma_f32_16x16x32_bf16 v[188:191], a[88:91], a[176:179], v[188:191]\n"
  "s_add_u32 s49, s49, 458752\n"
  "v_mfma_f32_16x16x32_bf16 v[192:195], a[96:99], a[176:179], v[192:195]\n"
  "v_mfma_f32_16x16x32_bf16 v[196:199], a[104:107], a[176:179], v[196:199]\n"
  "s_cmp_eq_u32 s49, 7340032\n"
  "s_cselect_b32 s49, 0, s49\n"
  "v_mfma_f32_16x16x32_bf16 v[172:175], a[60:63], a[180:183], v[172:175]\n"
  "v_mfma_f32_16x16x32_bf16 v[176:179], a[68:71], a[180:183], v[176:179]\n"
  "s_add_u32 s51, s51, 28672\n"
  "v_mfma_f32_16x16x32_bf16 v[180:183], a[76:79], a[180:183], v[180:183]\n"
  "v_mfma_f32_16x16x32_bf16 v[184:187], a[84:87], a[180:183], v[184:187]\n"
  "s_cmp_ge_u32 s51, s52\n"
  "s_cselect_b32 s26, 86016, 0\n"
  "v_mfma_f32_16x16x32_bf16 v[188:191], a[92:95], a[180:183], v[188:191]\n"
  "v_mfma_f32_16x16x32_bf16 v[192:195], a[100:103], a[180:183], v[192:195]\n"
  "s_sub_u32 s51, s51, s26\n"
  "v_mfma_f32_16x16x32_bf16 v[196:199], a[108:111], a[180:183], v[196:199]\n"
  "s_waitcnt vmcnt(9) lgkmcnt(0)\n"
  "s_barrier\n"
  "v_mfma_f32_16x16x32_bf16 v[200:203], a[56:59], a[184:187], v[200:203]\n"
  "ds_read_b128 a[168:171], v16 offset:0\n"
  "v_mfma_f32_16x16x32_bf16 v[204:207], a[64:67], a[184:187], v[204:207]\n"
  "ds_read_b128 a[172:175], v17 offset:0\n"
  "v_mfma_f32_16x16x32_bf16 v[208:211], a[72:75], a[184:187], v[208:211]\n"
  "s_add_u32 m0, s50, 32768\n"
  "v_mfma_f32_16x16x32_bf16 v[212:215], a[80:83], a[184:187], v[212:215]\n"
  "buffer_load_dwordx4 v0, s[40:43], s48 offen offset:0 lds\n"
  "v_mfma_f32_16x16x32_bf16 v[216:219], a[88:91], a[184:187], v[216:219]\n"
  "s_cmp_eq_u32 s56, 0\n"
  "s_cselect_b32 s53, s70, 28672\n"
  "v_mfma_f32_16x16x32_bf16 v[220:223], a[96:99], a[184:187], v[220:223]\n"
  "v_mfma_f32_16x16x32_bf16 v[224:227], a[104:107], a[184:187], v[224:227]\n"
  "s_cmp_eq_u32 s56, 0\n"
  "s_cselect_b32 s56, 3, s56\n"
  "v_mfma_f32_16x16x32_bf16 v[200:203], a[60:63], a[188:191], v[200:203]\n"
  "buffer_load_dwordx4 v1, s[40:43], s48 offen offset:1024 lds\n"
  "v_mfma_f32_16x16x32_bf16 v[204:207], a[68:71], a[188:191], v[204:207]\n"
  "s_sub_u32 s56, s56, 1\n"
  "v_mfma_f32_16x16x32_bf16 v[208:211], a[76:79], a[188:191], v[208:211]\n"
  "v_add_u32 v18, s53, v18\n"
  "v_mfma_f32_16x16x32_bf16 v[212:215], a[84:87], a[188:191], v[212:215]\n"
  "v_mfma_f32_16x16x32_bf16 v[216:219], a[92:95], a[188:191], v[216:219]\n"
  "v_add_u32 v19, s53, v19\n"
  "v_mfma_f32_16x16x32_bf16 v[220:223], a[100:103], a[188:191], v[220:223]\n"
  "buffer_load_dwordx4 v2, s[40:43], s48 offen offset:2048 lds\n"
  "v_mfma_f32_16x16x32_bf16 v[224:227], a[108:111], a[188:191], v[224:227]\n"
  "v_mfma_f32_16x16x32_bf16 v[228:231], a[56:59], a[192:195], v[228:231]\n"
  "ds_read_b128 a[176:179], v16 offset:2048\n"
  "v_mfma_f32_16x16x32_bf16 v[232:235], a[64:67], a[192:195], v[232:235]\n"
  "ds_read_b128 a[180:183], v17 offset:2048\n"
  "v_mfma_f32_16x16x32_bf16 v[236:239], a[72:75], a[192:195], v[236:239]\n"
  "v_add_u32 v20, s53, v20\n"
  "v_add_u32 v21, s53, v21\n"
  "v_mfma_f32_16x16x32_bf16 v[240:243], a[80:83], a[192:195], v[240:243]\n"
  "buffer_load_dwordx4 v3, s[40:43], s48 offen offset:3072 lds\n"
  "v_mfma_f32_16x16x32_bf16 v[244:247], a[88:91], a[192:195], v[244:247]\n"
  "v_mfma_f32_16x16x32_bf16 v[248:251], a[96:99], a[192:195], v[248:251]\n"
  "v_add_u32 v22, s53, v22\n"
  "v_mfma_f32_16x16x32_bf16 v[252:255], a[104:107], a[192:195], v[252:255]\n"
  "v_mfma_f32_16x16x32_bf16 v[228:231], a[60:63], a[196:199], v[228:231]\n"
  "s_add_u32 m0, s50, 36864\n"
  "v_add_u32 v23, s53, v23\n"
  "v_mfma_f32_16x16x32_bf16 v[232:235], a[68:71], a[196:199], v[232:235]\n"
  "buffer_load_dwordx4 v4, s[40:43], s48 offen offset:0 lds\n"
  "v_mfma_f32_16x16x32_bf16 v[236:239], a[76:79], a[196:199], v[236:239]\n"
  "v_mfma_f32_16x16x32_bf16 v[240:243], a[84:87], a[196:199], v[240:243]\n"
  "v_add_u32 v24, s53, v24\n"
  "v_mfma_f32_16x16x32_bf16 v[244:247], a[92:95], a[196:199], v[244:247]\n"
  "v_mfma_f32_16x16x32_bf16 v[248:251], a[100:103], a[196:199], v[248:251]\n"
  "v_mfma_f32_16x16x32_bf16 v[252:255], a[108:111], a[196:199], v[252:255]\n"
  "s_waitcnt lgkmcnt(2)\n"
  "v_mfma_f32_16x16x32_bf16 v[32:35], a[0:3], a[168:171], v[32:35]\n"
  "ds_read_b128 a[184:187], v16 offset:4096\n"
  "v_mfma_f32_16x16x32_bf16 v[36:39], a[8:11], a[168:171], v[36:39]\n"
  "ds_read_b128 a[188:191], v17 offset:4096\n"
  "v_mfma_f32_16x16x32_bf16 v[40:43], a[16:19], a[168:171], v[40:43]\n"
  "buffer_load_dwordx4 v5, s[40:43], s48 offen offset:1024 lds\n"
  "v_mfma_f32_16x16x32_bf16 v[44:47], a[24:27], a[168:171], v[44:47]\n"
  "ds_read_b64_tr_b16 a[56:57], v18 offset:0\n"
  "v_mfma_f32_16x16x32_bf16 v[48:51], a[32:35], a[168:171], v[48:51]\n"
  "buffer_load_dwordx4 v6, s[40:43], s48 offen offset:2048 lds\n"
  "v_mfma_f32_16x16x32_bf16 v[52:55], a[40:43], a[168:171], v[52:55]\n"
  "ds_read_b64_tr_b16 a[58:59], v18 offset:512\n"
  "v_mfma_f32_16x16x32_bf16 v[56:59], a[48:51], a[168:171], v[56:59]\n"
  "v_mfma_f32_16x16x32_bf16 v[32:35], a[4:7], a[172:175], v[32:35]\n"
  "ds_read_b64_tr_b16 a[64:65], v19 offset:0\n"
  "v_mfma_f32_16x16x32_bf16 v[36:39], a[12:15], a[172:175], v[36:39]\n"
  "v_mfma_f32_16x16x32_bf16 v[40:43], a[20:23], a[172:175], v[40:43]\n"
  "buffer_load_dwordx4 v7, s[40:43], s48 offen offset:3072 lds\n"
  "v_mfma_f32_16x16x32_bf16 v[44:47], a[28:31], a[172:175], v[44:47]\n"
  "s_add_u32 s48, s48, 128\n"
  "ds_read_b64_tr_b16 a[66:67], v19 offset:512\n"
  "v_mfma_f32_16x16x32_bf16 v[48:51], a[36:39], a[172:175], v[48:51]\n"
  "s_and_b32 s48, s48, 2047\n"
  "v_mfma_f32_16x16x32_bf16 v[52:55], a[44:47], a[172:175], v[52:55]\n"
  "ds_read_b64_tr_b16 a[72:73], v20 offset:0\n"
  "v_mfma_f32_16x16x32_bf16 v[56:59], a[52:55], a[172:175], v[56:59]\n"
  "s_waitcnt lgkmcnt(7)\n"
  "v_mfma_f32_16x16x32_bf16 v[60:63], a[0:3], a[176:179], v[60:63]\n"
  "ds_read_b128 a[192:195], v16 offset:6144\n"
  "v_mfma_f32_16x16x32_bf16 v[64:67], a[8:11], a[176:179], v[64:67]\n"
  "ds_read_b128 a[196:199], v17 offset:6144\n"
  "v_mfma_f32_16x16x32_bf16 v[68:71], a[16:19], a[176:179], v[68:71]\n"
  "s_add_u32 m0, s51, 0\n"
  "ds_read_b64_tr_b16 a[74:75], v20 offset:512\n"
  "v_mfma_f32_16x16x32_bf16 v[72:75], a[24:27], a[176:179], v[72:75]\n"
  "buffer_load_dwordx4 v9, s[44:47], s49 offen offset:0 lds\n"
  "v_mfma_f32_16x16x32_bf16 v[76:79], a[32:35], a[176:179], v[76:79]\n"
  "ds_read_b64_tr_b16 a[80:81], v21 offset:0\n"
  "v_mfma_f32_16x16x32_bf16 v[80:83], a[40:43], a[176:179], v[80:83]\n"
  "buffer_load_dword v27, v15, s[64:67], s68 offen\n"
  "v_mfma_f32_16x16x32_bf16 v[84:87], a[48:51], a[176:179], v[84:87]\n"
  "s_sub_u32 s68, s68, 6144\n"
  "ds_read_b64_tr_b16 a[82:83], v21 offset:512\n"
  "v_mfma_f32_16x16x32_bf16 v[60:63], a[4:7], a[180:183], v[60:63]\n"
  "v_mfma_f32_16x16x32_bf16 v[64:67], a[12:15], a[180:183], v[64:67]\n"
  "ds_read_b64_tr_b16 a[88:89], v22 offset:0\n"
  "v_mfma_f32_16x16x32_bf16 v[68:71], a[20:23], a[180:183], v[68:71]\n"
  "v_mfma_f32_16x16x32_bf16 v[72:75], a[28:31], a[180:183], v[72:75]\n"
  "v_mfma_f32_16x16x32_bf16 v[76:79], a[36:39], a[180:183], v[76:79]\n"
  "ds_read_b64_tr_b16 a[90:91], v22 offset:512\n"
  "v_mfma_f32_16x16x32_bf16 v[80:83], a[44:47], a[180:183], v[80:83]\n"
  "buffer_load_dwordx4 v10, s[44:47], s49 offen offset:1024 lds\n"
  "v_mfma_f32_16x16x32_bf16 v[84:87], a[52:55], a[180:183], v[84:87]\n"
  "s_waitcnt lgkmcnt(12)\n"
  "v_mfma_f32_16x16x32_bf16 v[88:91], a[0:3], a[184:187], v[88:91]\n"
  "ds_read_b128 a[168:171], v16 offset:8192\n"
  "v_mfma_f32_16x16x32_bf16 v[92:95], a[8:11], a[184:187], v[92:95]\n"
  "ds_read_b128 a[172:175], v17 offset:8192\n"
  "v_mfma_f32_16x16x32_bf16 v[96:99], a[16:19], a[184:187], v[96:99]\n"
  "ds_read_b64_tr_b16 a[96:97], v23 offset:0\n"
  "v_mfma_f32_16x16x32_bf16 v[100:103], a[24:27], a[184:187], v[100:103]\n"
  "ds_read_b64_tr_b16 a[98:99], v23 offset:512\n"
  "v_mfma_f32_16x16x32_bf16 v[104:107], a[32:35], a[184:187], v[104:107]\n"
  "v_mfma_f32_16x16x32_bf16 v[108:111], a[40:43], a[184:187], v[108:111]\n"
  "ds_read_b64_tr_b16 a[104:105], v24 offset:0\n"
  "v_mfma_f32_16x16x32_bf16 v[112:115], a[48:51], a[184:187], v[112:115]\n"
  "s_add_u32 m0, s51, 8192\n"
  "v_mfma_f32_16x16x32_bf16 v[88:91], a[4:7], a[188:191], v[88:91]\n"
  "buffer_load_dwordx4 v11, s[44:47], s49 offen offset:0 lds\n"
  "v_mfma_f32_16x16x32_bf16 v[92:95], a[12:15], a[188:191], v[92:95]\n"
  "ds_read_b64_tr_b16 a[106:107], v24 offset:256\n"
  "v_mfma_f32_16x16x32_bf16 v[96:99], a[20:23], a[188:191], v[96:99]\n"
  "v_mfma_f32_16x16x32_bf16 v[100:103], a[28:31], a[188:191], v[100:103]\n"
  "ds_read_b64_tr_b16 a[60:61], v18 offset:4096\n"
  "v_mfma_f32_16x16x32_bf16 v[104:107], a[36:39], a[188:191], v[104:107]\n"
  "v_mfma_f32_16x16x32_bf16 v[108:111], a[44:47], a[188:191], v[108:111]\n"
  "v_mfma_f32_16x16x32_bf16 v[112:115], a[52:55], a[188:191], v[112:115]\n"
  "ds_read_b64_tr_b16 a[62:63], v18 offset:4608\n"
  "s_waitcnt lgkmcnt(13)\n"
  "v_mfma_f32_16x16x32_bf16 v[116:119], a[0:3], a[192:195], v[116:119]\n"
  "ds_read_b128 a[176:179], v16 offset:10240\n"
  "v_mfma_f32_16x16x32_bf16 v[120:123], a[8:11], a[192:195], v[120:123]\n"
  "ds_read_b128 a[180:183], v17 offset:10240\n"
  "v_mfma_f32_16x16x32_bf16 v[124:127], a[16:19], a[192:195], v[124:127]\n"
  "ds_read_b64_tr_b16 a[68:69], v19 offset:4096\n"
  "v_mfma_f32_16x16x32_bf16 v[128:131], a[24:27], a[192:195], v[128:131]\n"
  "buffer_load_dwordx4 v12, s[44:47], s49 offen offset:1024 lds\n"
  "v_mfma_f32_16x16x32_bf16 v[132:135], a[32:35], a[192:195], v[132:135]\n"
  "ds_read_b64_tr_b16 a[70:71], v19 offset:4608\n"
  "v_mfma_f32_16x16x32_bf16 v[136:139], a[40:43], a[192:195], v[136:139]\n"
  "v_mfma_f32_16x16x32_bf16 v[140:143], a[48:51], a[192:195], v[140:143]\n"
  "ds_read_b64_tr_b16 a[76:77], v20 offset:4096\n"
  "v_mfma_f32_16x16x32_bf16 v[116:119], a[4:7], a[196:199], v[116:119]\n"
  "v_mfma_f32_16x16x32_bf16 v[120:123], a[12:15], a[196:199], v[120:123]\n"
  "v_mfma_f32_16x16x32_bf16 v[124:127], a[20:23], a[196:199], v[124:127]\n"
  "ds_read_b64_tr_b16 a[78:79], v20 offset:4608\n"
  "v_mfma_f32_16x16x32_bf16 v[128:131], a[28:31], a[196:199], v[128:131]\n"
  "v_mfma_f32_16x16x32_bf16 v[132:135], a[36:39], a[196:199], v[132:135]\n"
  "s_add_u32 m0, s51, 16384\n"
  "v_mfma_f32_16x16x32_bf16 v[136:139], a[44:47], a[196:199], v[136:139]\n"
  "buffer_load_dwordx4 v13, s[44:47], s49 offen offset:0 lds\n"
  "v_mfma_f32_16x16x32_bf16 v[140:143], a[52:55], a[196:199], v[140:143]\n"
  "ds_read_b64_tr_b16 a[84:85], v21 offset:4096\n"
  "s_waitcnt lgkmcnt(13)\n"
  "v_mfma_f32_16x16x32_bf16 v[144:147], a[0:3], a[168:171], v[144:147]\n"
  "ds_read_b128 a[184:187], v16 offset:12288\n"
  "v_mfma_f32_16x16x32_bf16 v[148:151], a[8:11], a[168:171], v[148:151]\n"
  "ds_read_b128 a[188:191], v17 offset:12288\n"
  "v_mfma_f32_16x16x32_bf16 v[152:155], a[16:19], a[168:171], v[152:155]\n"
  "ds_read_b64_tr_b16 a[86:87], v21 offset:4608\n"
  "v_mfma_f32_16x16x32_bf16 v[156:159], a[24:27], a[168:171], v[156:159]\n"
  "ds_read_b64_tr_b16 a[92:93], v22 offset:4096\n"
  "v_mfma_f32_16x16x32_bf16 v[160:163], a[32:35], a[168:171], v[160:163]\n"
  "v_mfma_f32_16x16x32_bf16 v[164:167], a[40:43], a[168:171], v[164:167]\n"
  "ds_read_b64_tr_b16 a[94:95], v22 offset:4608\n"
  "v_mfma_f32_16x16x32_bf16 v[168:171], a[48:51], a[168:171], v[168:171]\n"
  "v_mfma_f32_16x16x32_bf16 v[144:147], a[4:7], a[172:175], v[144:147]\n"
  "buffer_load_dwordx4 v14, s[44:47], s49 offen offset:1024 lds\n"
  "v_mfma_f32_16x16x32_bf16 v[148:151], a[12:15], a[172:175], v[148:151]\n"
  "ds_read_b64_tr_b16 a[100:101], v23 offset:4096\n"
  "v_mfma_f32_16x16x32_bf16 v[152:155], a[20:23], a[172:175], v[152:155]\n"
  "v_mfma_f32_16x16x32_bf16 v[156:159], a[28:31], a[172:175], v[156:159]\n"
  "v_mfma_f32_16x16x32_bf16 v[160:163], a[36:39], a[172:175], v[160:163]\n"
  "ds_read_b64_tr_b16 a[102:103], v23 offset:4608\n"
  "v_mfma_f32_16x16x32_bf16 v[164:167], a[44:47], a[172:175], v[164:167]\n"
  "v_mfma_f32_16x16x32_bf16 v[168:171], a[52:55], a[172:175], v[168:171]\n"
  "ds_read_b64_tr_b16 a[108:109], v24 offset:2048\n"
  "s_waitcnt lgkmcnt(13)\n"
  "v_mfma_f32_16x16x32_bf16 v[172:175], a[0:3], a[176:179], v[172:175]\n"
  "ds_read_b128 a[192:195], v16 offset:14336\n"
  "v_mfma_f32_16x16x32_bf16 v[176:179], a[8:11], a[176:179], v[176:179]\n"
  "ds_read_b128 a[196:199], v17 offset:14336\n"
  "v_mfma_f32_16x16x32_bf16 v[180:183], a[16:19], a[176:179], v[180:183]\n"
  "s_add_u32 m0, s51, s57\n"
  "ds_read_b64_tr_b16 a[110:111], v24 offset:2304\n"
  "v_mfma_f32_16x16x32_bf16 v[184:187], a[24:27], a[176:179], v[184:187]\n"
  "buffer_load_dwordx4 v8, s[44:47], s49 offen lds\n"
  "v_mfma_f32_16x16x32_bf16 v[188:191], a[32:35], a[176:179], v[188:191]\n"
  "s_add_u32 s49, s49, 458752\n"
  "v_mfma_f32_16x16x32_bf16 v[192:195], a[40:43], a[176:179], v[192:195]\n"
  "v_mfma_f32_16x16x32_bf16 v[196:199], a[48:51], a[176:179], v[196:199]\n"
  "s_cmp_eq_u32 s49, 7340032\n"
  "s_cselect_b32 s49, 0, s49\n"
  "v_mfma_f32_16x16x32_bf16 v[172:175], a[4:7], a[180:183], v[172:175]\n"
  "v_mfma_f32_16x16x32_bf16 v[176:179], a[12:15], a[180:183], v[176:179]\n"
  "s_add_u32 s51, s51, 28672\n"
  "v_mfma_f32_16x16x32_bf16 v[180:183], a[20:23], a[180:183], v[180:183]\n"
  "v_mfma_f32_16x16x32_bf16 v[184:187], a[28:31], a[180:183], v[184:187]\n"
  "s_cmp_ge_u32 s51, s52\n"
  "s_cselect_b32 s26, 86016, 0\n"
  "v_mfma_f32_16x16x32_bf16 v[188:191], a[36:39], a[180:183], v[188:191]\n"
  "v_mfma_f32_16x16x32_bf16 v[192:195], a[44:47], a[180:183], v[192:195]\n"
  "s_sub_u32 s51, s51, s26\n"
  "v_mfma_f32_16x16x32_bf16 v[196:199], a[52:55], a[180:183], v[196:199]\n"
  "s_waitcnt vmcnt(8) lgkmcnt(0)\n"
  "s_barrier\n"
  "v_mfma_f32_16x16x32_bf16 v[200:203], a[0:3], a[184:187], v[200:203]\n"
  "ds_read_b128 a[168:171], v16 offset:32768\n"
  "v_mfma_f32_16x16x32_bf16 v[204:207], a[8:11], a[184:187], v[204:207]\n"
  "ds_read_b128 a[172:175], v17 offset:32768\n"
  "v_mfma_f32_16x16x32_bf16 v[208:211], a[16:19], a[184:187], v[208:211]\n"
  "s_add_u32 m0, s50, 0\n"
  "v_mfma_f32_16x16x32_bf16 v[212:215], a[24:27], a[184:187], v[212:215]\n"
  "buffer_load_dwordx4 v0, s[40:43], s48 offen offset:0 lds\n"
  "v_mfma_f32_16x16x32_bf16 v[216:219], a[32:35], a[184:187], v[216:219]\n"
  "s_cmp_eq_u32 s56, 0\n"
  "s_cselect_b32 s53, s70, 28672\n"
  "v_mfma_f32_16x16x32_bf16 v[220:223], a[40:43], a[184:187], v[220:223]\n"
  "v_mfma_f32_16x16x32_bf16 v[224:227], a[48:51], a[184:187], v[224:227]\n"
  "s_cmp_eq_u32 s56, 0\n"
  "s_cselect_b32 s56, 3, s56\n"
  "v_mfma_f32_16x16x32_bf16 v[200:203], a[4:7], a[188:191], v[200:203]\n"
  "buffer_load_dwordx4 v1, s[40:43], s48 offen offset:1024 lds\n"
  "v_mfma_f32_16x16x32_bf16 v[204:207], a[12:15], a[188:191], v[204:207]\n"
  "s_sub_u32 s56, s56, 1\n"
  "v_mfma_f32_16x16x32_bf16 v[208:211], a[20:23], a[188:191], v[208:211]\n"
  "v_add_u32 v18, s53, v18\n"
  "v_mfma_f32_16x16x32_bf16 v[212:215], a[28:31], a[188:191], v[212:215]\n"
  "v_mfma_f32_16x16x32_bf16 v[216:219], a[36:39], a[188:191], v[216:219]\n"
  "v_add_u32 v19, s53, v19\n"
  "v_mfma_f32_16x16x32_bf16 v[220:223], a[44:47], a[188:191], v[220:223]\n"
  "buffer_load_dwordx4 v2, s[40:43], s48 offen offset:2048 lds\n"
  "v_mfma_f32_16x16x32_bf16 v[224:227], a[52:55], a[188:191], v[224:227]\n"
  "v_mfma_f32_16x16x32_bf16 v[228:231], a[0:3], a[192:195], v[228:231]\n"
  "ds_read_b128 a[176:179], v16 offset:34816\n"
  "v_mfma_f32_16x16x32_bf16 v[232:235], a[8:11], a[192:195], v[232:235]\n"
  "ds_read_b128 a[180:183], v17 offset:34816\n"
  "v_mfma_f32_16x16x32_bf16 v[236:239], a[16:19], a[192:195], v[236:239]\n"
  "v_add_u32 v20, s53, v20\n"
  "v_add_u32 v21, s53, v21\n"
  "v_mfma_f32_16x16x32_bf16 v[240:243], a[24:27], a[192:195], v[240:243]\n"
  "buffer_load_dwordx4 v3, s[40:43], s48 offen offset:3072 lds\n"
  "v_mfma_f32_16x16x32_bf16 v[244:247], a[32:35], a[192:195], v[244:247]\n"
  "v_mfma_f32_16x16x32_bf16 v[248:251], a[40:43], a[192:195], v[248:251]\n"
  "v_add_u32 v22, s53, v22\n"
  "v_mfma_f32_16x16x32_bf16 v[252:255], a[48:51], a[192:195], v[252:255]\n"
  "v_mfma_f32_16x16x32_bf16 v[228:231], a[4:7], a[196:199], v[228:231]\n"
  "s_add_u32 m0, s50, 4096\n"
  "v_add_u32 v23, s53, v23\n"
  "v_mfma_f32_16x16x32_bf16 v[232:235], a[12:15], a[196:199], v[232:235]\n"
  "buffer_load_dwordx4 v4, s[40:43], s48 offen offset:0 lds\n"
  "v_mfma_f32_16x16x32_bf16 v[236:239], a[20:23], a[196:199], v[236:239]\n"
  "v_mfma_f32_16x16x32_bf16 v[240:243], a[28:31], a[196:199], v[240:243]\n"
  "v_add_u32 v24, s53, v24\n"
  "v_mfma_f32_16x16x32_bf16 v[244:247], a[36:39], a[196:199], v[244:247]\n"
  "v_mfma_f32_16x16x32_bf16 v[248:251], a[44:47], a[196:199], v[248:251]\n"
  "v_mfma_f32_16x16x32_bf16 v[252:255], a[52:55], a[196:199], v[252:255]\n"
  "s_sub_u32 s54, s54, 1\n"
  "s_cmp_lg_u32 s54, 0\n"
  "s_cbranch_scc1 K1B_LOOP_%=\n"
  "s_waitcnt lgkmcnt(2)\n"
  "v_mfma_f32_16x16x32_bf16 v[32:35], a[56:59], a[168:171], v[32:35]\n"
  "ds_read_b128 a[184:187], v16 offset:36864\n"
  "v_mfma_f32_16x16x32_bf16 v[36:39], a[64:67], a[168:171], v[36:39]\n"
  "ds_read_b128 a[188:191], v17 offset:36864\n"
  "v_mfma_f32_16x16x32_bf16 v[40:43], a[72:75], a[168:171], v[40:43]\n"
  "buffer_load_dwordx4 v5, s[40:43], s48 offen offset:1024 lds\n"
  "v_mfma_f32_16x16x32_bf16 v[44:47], a[80:83], a[168:171], v[44:47]\n"
  "ds_read_b64_tr_b16 a[0:1], v18 offset:0\n"
  "v_mfma_f32_16x16x32_bf16 v[48:51], a[88:91], a[168:171], v[48:51]\n"
  "buffer_load_dwordx4 v6, s[40:43], s48 offen offset:2048 lds\n"
  "v_mfma_f32_16x16x32_bf16 v[52:55], a[96:99], a[168:171], v[52:55]\n"
  "ds_read_b64_tr_b16 a[2:3], v18 offset:512\n"
  "v_mfma_f32_16x16x32_bf16 v[56:59], a[104:107], a[168:171], v[56:59]\n"
  "buffer_load_dwordx4 v7, s[40:43], s48 offen offset:3072 lds\n"
  "v_mfma_f32_16x16x32_bf16 v[32:35], a[60:63], a[172:175], v[32:35]\n"
  "s_add_u32 s48, s48, 128\n"
  "ds_read_b64_tr_b16 a[8:9], v19 offset:0\n"
  "v_mfma_f32_16x16x32_bf16 v[36:39], a[68:71], a[172:175], v[36:39]\n"
  "s_and_b32 s48, s48, 2047\n"
  "s_add_u32 m0, s50, 122880\n"
  "ds_read_b64_tr_b16 a[10:11], v19 offset:512\n"
  "v_mfma_f32_16x16x32_bf16 v[40:43], a[76:79], a[172:175], v[40:43]\n"
  "buffer_load_dwordx4 v0, s[40:43], s48 offen offset:0 lds\n"
  "v_mfma_f32_16x16x32_bf16 v[44:47], a[84:87], a[172:175], v[44:47]\n"
  "ds_read_b64_tr_b16 a[16:17], v20 offset:0\n"
  "v_mfma_f32_16x16x32_bf16 v[48:51], a[92:95], a[172:175], v[48:51]\n"
  "ds_read_b64_tr_b16 a[18:19], v20 offset:512\n"
  "v_add_u32 v9, 122880, v16\n"
  "v_add_u32 v10, 122880, v17\n"
  "v_mfma_f32_16x16x32_bf16 v[52:55], a[100:103], a[172:175], v[52:55]\n"
  "buffer_load_dwordx4 v1, s[40:43], s48 offen offset:1024 lds\n"
  "v_mfma_f32_16x16x32_bf16 v[56:59], a[108:111], a[172:175], v[56:59]\n"
  "ds_read_b64_tr_b16 a[24:25], v21 offset:0\n"
  "s_waitcnt lgkmcnt(9)\n"
  "v_mfma_f32_16x16x32_bf16 v[60:63], a[56:59], a[176:179], v[60:63]\n"
  "ds_read_b128 a[192:195], v16 offset:38912\n"
  "v_mfma_f32_16x16x32_bf16 v[64:67], a[64:67], a[176:179], v[64:67]\n"
  "ds_read_b128 a[196:199], v17 offset:38912\n"
  "v_mfma_f32_16x16x32_bf16 v[68:71], a[72:75], a[176:179], v[68:71]\n"
  "buffer_load_dwordx4 v2, s[40:43], s48 offen offset:2048 lds\n"
  "v_mfma_f32_16x16x32_bf16 v[72:75], a[80:83], a[176:179], v[72:75]\n"
  "ds_read_b64_tr_b16 a[26:27], v21 offset:512\n"
  "v_mfma_f32_16x16x32_bf16 v[76:79], a[88:91], a[176:179], v[76:79]\n"
  "buffer_load_dwordx4 v3, s[40:43], s48 offen offset:3072 lds\n"
  "v_mfma_f32_16x16x32_bf16 v[80:83], a[96:99], a[176:179], v[80:83]\n"
  "ds_read_b64_tr_b16 a[32:33], v22 offset:0\n"
  "v_mfma_f32_16x16x32_bf16 v[84:87], a[104:107], a[176:179], v[84:87]\n"
  "s_add_u32 m0, s50, 126976\n"
  "ds_read_b64_tr_b16 a[34:35], v22 offset:512\n"
  "v_mfma_f32_16x16x32_bf16 v[60:63], a[60:63], a[180:183], v[60:63]\n"
  "buffer_load_dwordx4 v4, s[40:43], s48 offen offset:0 lds\n"
  "v_mfma_f32_16x16x32_bf16 v[64:67], a[68:71], a[180:183], v[64:67]\n"
  "ds_read_b64_tr_b16 a[40:41], v23 offset:0\n"
  "v_mfma_f32_16x16x32_bf16 v[68:71], a[76:79], a[180:183], v[68:71]\n"
  "ds_read_b64_tr_b16 a[42:43], v23 offset:512\n"
  "v_mfma_f32_16x16x32_bf16 v[72:75], a[84:87], a[180:183], v[72:75]\n"
  "buffer_load_dwordx4 v5, s[40:43], s48 offen offset:1024 lds\n"
  "v_mfma_f32_16x16x32_bf16 v[76:79], a[92:95], a[180:183], v[76:79]\n"
  "ds_read_b64_tr_b16 a[48:49], v24 offset:0\n"
  "v_mfma_f32_16x16x32_bf16 v[80:83], a[100:103], a[180:183], v[80:83]\n"
  "ds_read_b64_tr_b16 a[50:51], v24 offset:256\n"
  "v_mfma_f32_16x16x32_bf16 v[84:87], a[108:111], a[180:183], v[84:87]\n"
  "buffer_load_dwordx4 v6, s[40:43], s48 offen offset:2048 lds\n"
  "s_waitcnt lgkmcnt(15)\n"
  "v_mfma_f32_16x16x32_bf16 v[88:91], a[56:59], a[184:187], v[88:91]\n"
  "ds_read_b128 a[168:171], v16 offset:40960\n"
  "v_mfma_f32_16x16x32_bf16 v[92:95], a[64:67], a[184:187], v[92:95]\n"
  "ds_read_b128 a[172:175], v17 offset:40960\n"
  "v_mfma_f32_16x16x32_bf16 v[96:99], a[72:75], a[184:187], v[96:99]\n"
  "buffer_load_dwordx4 v7, s[40:43], s48 offen offset:3072 lds\n"
  "v_mfma_f32_16x16x32_bf16 v[100:103], a[80:83], a[184:187], v[100:103]\n"
  "ds_read_b64_tr_b16 a[4:5], v18 offset:4096\n"
  "v_mfma_f32_16x16x32_bf16 v[104:107], a[88:91], a[184:187], v[104:107]\n"
  "ds_read_b64_tr_b16 a[6:7], v18 offset:4608\n"
  "v_mfma_f32_16x16x32_bf16 v[108:111], a[96:99], a[184:187], v[108:111]\n"
  "ds_read_b64_tr_b16 a[12:13], v19 offset:4096\n"
  "v_mfma_f32_16x16x32_bf16 v[112:115], a[104:107], a[184:187], v[112:115]\n"
  "ds_read_b64_tr_b16 a[14:15], v19 offset:4608\n"
  "v_mfma_f32_16x16x32_bf16 v[88:91], a[60:63], a[188:191], v[88:91]\n"
  "ds_read_b64_tr_b16 a[20:21], v20 offset:4096\n"
  "v_mfma_f32_16x16x32_bf16 v[92:95], a[68:71], a[188:191], v[92:95]\n"
  "v_lshlrev_b32 v0, 4, v28\n"
  "v_add_u32 v1, 294912, v0\n"
  "v_mfma_f32_16x16x32_bf16 v[96:99], a[76:79], a[188:191], v[96:99]\n"
  "ds_read_b64_tr_b16 a[22:23], v20 offset:4608\n"
  "v_mfma_f32_16x16x32_bf16 v[100:103], a[84:87], a[188:191], v[100:103]\n"
  "v_add_u32 v2, 589824, v0\n"
  "v_mfma_f32_16x16x32_bf16 v[104:107], a[92:95], a[188:191], v[104:107]\n"
  "ds_read_b64_tr_b16 a[28:29], v21 offset:4096\n"
  "v_mfma_f32_16x16x32_bf16 v[108:111], a[100:103], a[188:191], v[108:111]\n"
  "v_add_u32 v3, 884736, v0\n"
  "v_mfma_f32_16x16x32_bf16 v[112:115], a[108:111], a[188:191], v[112:115]\n"
  "ds_read_b64_tr_b16 a[30:31], v21 offset:4608\n"
  "s_waitcnt lgkmcnt(15)\n"
  "v_mfma_f32_16x16x32_bf16 v[116:119], a[56:59], a[192:195], v[116:119]\n"
  "ds_read_b128 a[176:179], v16 offset:43008\n"
  "v_mfma_f32_16x16x32_bf16 v[120:123], a[64:67], a[192:195], v[120:123]\n"
  "ds_read_b128 a[180:183], v17 offset:43008\n"
  "v_mfma_f32_16x16x32_bf16 v[124:127], a[72:75], a[192:195], v[124:127]\n"
  "ds_read_b64_tr_b16 a[36:37], v22 offset:4096\n"
  "v_mfma_f32_16x16x32_bf16 v[128:131], a[80:83], a[192:195], v[128:131]\n"
  "ds_read_b64_tr_b16 a[38:39], v22 offset:4608\n"
  "v_mfma_f32_16x16x32_bf16 v[132:135], a[88:91], a[192:195], v[132:135]\n"
  "ds_read_b64_tr_b16 a[44:45], v23 offset:4096\n"
  "v_mfma_f32_16x16x32_bf16 v[136:139], a[96:99], a[192:195], v[136:139]\n"
  "v_and_b32 v4, 15, v28\n"
  "v_lshlrev_b32 v4, 4, v4\n"
  "v_mfma_f32_16x16x32_bf16 v[140:143], a[104:107], a[192:195], v[140:143]\n"
  "ds_read_b64_tr_b16 a[46:47], v23 offset:4608\n"
  "v_mfma_f32_16x16x32_bf16 v[116:119], a[60:63], a[196:199], v[116:119]\n"
  "v_lshrrev_b32 v5, 5, v28\n"
  "v_lshl_or_b32 v4, v5, 8, v4\n"
  "v_mfma_f32_16x16x32_bf16 v[120:123], a[68:71], a[196:199], v[120:123]\n"
  "ds_read_b64_tr_b16 a[52:53], v24 offset:2048\n"
  "v_mfma_f32_16x16x32_bf16 v[124:127], a[76:79], a[196:199], v[124:127]\n"
  "v_bfe_u32 v5, v28, 4, 1\n"
  "v_mfma_f32_16x16x32_bf16 v[128:131], a[84:87], a[196:199], v[128:131]\n"
  "ds_read_b64_tr_b16 a[54:55], v24 offset:2304\n"
  "v_mfma_f32_16x16x32_bf16 v[132:135], a[92:95], a[196:199], v[132:135]\n"
  "s_cmp_eq_u32 s56, 0\n"
  "s_cselect_b32 s53, s70, 28672\n"
  "v_lshl_or_b32 v4, v5, 11, v4\n"
  "v_mfma_f32_16x16x32_bf16 v[136:139], a[100:103], a[196:199], v[136:139]\n"
  "s_cmp_eq_u32 s56, 0\n"
  "s_cselect_b32 s56, 3, s56\n"
  "v_mfma_f32_16x16x32_bf16 v[140:143], a[108:111], a[196:199], v[140:143]\n"
  "s_and_b32 s69, s59, 1\n"
  "s_waitcnt lgkmcnt(15)\n"
  "v_mfma_f32_16x16x32_bf16 v[144:147], a[56:59], a[168:171], v[144:147]\n"
  "s_sub_u32 s56, s56, 1\n"
  "v_mfma_f32_16x16x32_bf16 v[148:151], a[64:67], a[168:171], v[148:151]\n"
  "v_mfma_f32_16x16x32_bf16 v[152:155], a[72:75], a[168:171], v[152:155]\n"
  "v_add_u32 v18, s53, v18\n"
  "s_lshl_b32 s69, s69, 9\n"
  "v_mfma_f32_16x16x32_bf16 v[156:159], a[80:83], a[168:171], v[156:159]\n"
  "v_add_u32 v19, s53, v19\n"
  "v_mfma_f32_16x16x32_bf16 v[160:163], a[88:91], a[168:171], v[160:163]\n"
  "v_add_u32 v4, s69, v4\n"
  "v_mfma_f32_16x16x32_bf16 v[164:167], a[96:99], a[168:171], v[164:167]\n"
  "v_add_u32 v20, s53, v20\n"
  "v_mfma_f32_16x16x32_bf16 v[168:171], a[104:107], a[168:171], v[168:171]\n"
  "v_add_u32 v5, 294912, v4\n"
  "v_mfma_f32_16x16x32_bf16 v[144:147], a[60:63], a[172:175], v[144:147]\n"
  "v_add_u32 v21, s53, v21\n"
  "v_mfma_f32_16x16x32_bf16 v[148:151], a[68:71], a[172:175], v[148:151]\n"
  "v_add_u32 v22, s53, v22\n"
  "v_mfma_f32_16x16x32_bf16 v[152:155], a[76:79], a[172:175], v[152:155]\n"
  "v_add_u32 v6, 589824, v4\n"
  "v_mfma_f32_16x16x32_bf16 v[156:159], a[84:87], a[172:175], v[156:159]\n"
  "v_add_u32 v23, s53, v23\n"
  "v_mfma_f32_16x16x32_bf16 v[160:163], a[92:95], a[172:175], v[160:163]\n"
  "v_add_u32 v7, 884736, v4\n"
  "v_mfma_f32_16x16x32_bf16 v[164:167], a[100:103], a[172:175], v[164:167]\n"
  "v_add_u32 v24, s53, v24\n"
  "v_mfma_f32_16x16x32_bf16 v[168:171], a[108:111], a[172:175], v[168:171]\n"
  "s_waitcnt vmcnt(0)\n"
  "s_barrier\n"
  "s_waitcnt lgkmcnt(6)\n"
  "v_mfma_f32_16x16x32_bf16 v[172:175], a[56:59], a[176:179], v[172:175]\n"
  "ds_read_b128 a[184:187], v16 offset:0\n"
  "ds_read_b128 a[192:195], v9 offset:0\n"
  "v_mfma_f32_16x16x32_bf16 v[176:179], a[64:67], a[176:179], v[176:179]\n"
  "ds_read_b128 a[188:191], v17 offset:0\n"
  "ds_read_b128 a[196:199], v10 offset:0\n"
  "v_mfma_f32_16x16x32_bf16 v[180:183], a[72:75], a[176:179], v[180:183]\n"
  "ds_read_b64_tr_b16 a[112:113], v18 offset:0\n"
  "buffer_load_dwordx4 a[236:239], v0, s[76:79], 0 offen offset:0\n"
  "v_mfma_f32_16x16x32_bf16 v[184:187], a[80:83], a[176:179], v[184:187]\n"
  "ds_read_b64_tr_b16 a[114:115], v18 offset:512\n"
  "v_mfma_f32_16x16x32_bf16 v[188:191], a[88:91], a[176:179], v[188:191]\n"
  "ds_read_b64_tr_b16 a[120:121], v19 offset:0\n"
  "v_mfma_f32_16x16x32_bf16 v[192:195], a[96:99], a[176:179], v[192:195]\n"
  "ds_read_b64_tr_b16 a[122:123], v19 offset:512\n"
  "buffer_load_dwordx4 a[240:243], v0, s[76:79], 0 offen offset:1024\n"
  "v_mfma_f32_16x16x32_bf16 v[196:199], a[104:107], a[176:179], v[196:199]\n"
  "ds_read_b64_tr_b16 a[128:129], v20 offset:0\n"
  "v_mfma_f32_16x16x32_bf16 v[172:175], a[60:63], a[180:183], v[172:175]\n"
  "ds_read_b64_tr_b16 a[130:131], v20 offset:512\n"
  "v_mfma_f32_16x16x32_bf16 v[176:179], a[68:71], a[180:183], v[176:179]\n"
  "ds_read_b64_tr_b16 a[136:137], v21 offset:0\n"
  "buffer_load_dwordx4 a[204:207], v0, s[72:75], 0 offen offset:0\n"
  "v_mfma_f32_16x16x32_bf16 v[180:183], a[76:79], a[180:183], v[180:183]\n"
  "ds_read_b64_tr_b16 a[138:139], v21 offset:512\n"
  "v_mfma_f32_16x16x32_bf16 v[184:187], a[84:87], a[180:183], v[184:187]\n"
  "ds_read_b64_tr_b16 a[144:145], v22 offset:0\n"
  "v_mfma_f32_16x16x32_bf16 v[188:191], a[92:95], a[180:183], v[188:191]\n"
  "ds_read_b64_tr_b16 a[146:147], v22 offset:512\n"
  "buffer_load_dwordx4 a[208:211], v0, s[72:75], 0 offen offset:1024\n"
  "v_mfma_f32_16x16x32_bf16 v[192:195], a[100:103], a[180:183], v[192:195]\n"
  "ds_read_b64_tr_b16 a[152:153], v23 offset:0\n"
  "v_mfma_f32_16x16x32_bf16 v[196:199], a[108:111], a[180:183], v[196:199]\n"
  "ds_read_b64_tr_b16 a[154:155], v23 offset:512\n"
  "s_waitcnt lgkmcnt(13)\n"
  "v_mfma_f32_16x16x32_bf16 v[32:35], a[0:3], a[184:187], v[32:35]\n"
  "ds_read_b128 a[168:171], v16 offset:45056\n"
  "buffer_load_dwordx4 a[212:215], v0, s[72:75], 0 offen offset:2048\n"
  "v_mfma_f32_16x16x32_bf16 v[36:39], a[8:11], a[184:187], v[36:39]\n"
  "ds_read_b128 a[172:175], v17 offset:45056\n"
  "v_mfma_f32_16x16x32_bf16 v[40:43], a[16:19], a[184:187], v[40:43]\n"
  "ds_read_b64_tr_b16 a[160:161], v24 offset:0\n"
  "v_mfma_f32_16x16x32_bf16 v[44:47], a[24:27], a[184:187], v[44:47]\n"
  "ds_read_b64_tr_b16 a[162:163], v24 offset:256\n"
  "buffer_load_dwordx4 a[216:219], v0, s[72:75], 0 offen offset:3072\n"
  "v_mfma_f32_16x16x32_bf16 v[48:51], a[32:35], a[184:187], v[48:51]\n"
  "ds_read_b64_tr_b16 a[116:117], v18 offset:4096\n"
  "v_mfma_f32_16x16x32_bf16 v[52:55], a[40:43], a[184:187], v[52:55]\n"
  "ds_read_b64_tr_b16 a[118:119], v18 offset:4608\n"
  "v_mfma_f32_16x16x32_bf16 v[56:59], a[48:51], a[184:187], v[56:59]\n"
  "ds_read_b64_tr_b16 a[124:125], v19 offset:4096\n"
  "buffer_load_dwordx4 a[220:223], v0, s[72:75], s17 offen offset:0\n"
  "v_mfma_f32_16x16x32_bf16 v[32:35], a[4:7], a[188:191], v[32:35]\n"
  "ds_read_b64_tr_b16 a[126:127], v19 offset:4608\n"
  "v_mfma_f32_16x16x32_bf16 v[36:39], a[12:15], a[188:191], v[36:39]\n"
  "ds_read_b64_tr_b16 a[132:133], v20 offset:4096\n"
  "v_mfma_f32_16x16x32_bf16 v[40:43], a[20:23], a[188:191], v[40:43]\n"
  "ds_read_b64_tr_b16 a[134:135], v20 offset:4608\n"
  "buffer_load_dwordx4 a[224:227], v0, s[72:75], s17 offen offset:1024\n"
  "v_mfma_f32_16x16x32_bf16 v[44:47], a[28:31], a[188:191], v[44:47]\n"
  "ds_read_b64_tr_b16 a[140:141], v21 offset:4096\n"
  "v_mfma_f32_16x16x32_bf16 v[48:51], a[36:39], a[188:191], v[48:51]\n"
  "ds_read_b64_tr_b16 a[142:143], v21 offset:4608\n"
  "v_mfma_f32_16x16x32_bf16 v[52:55], a[44:47], a[188:191], v[52:55]\n"
  "ds_read_b64_tr_b16 a[148:149], v22 offset:4096\n"
  "buffer_load_dwordx4 a[228:231], v0, s[72:75], s17 offen offset:2048\n"
  "v_mfma_f32_16x16x32_bf16 v[56:59], a[52:55], a[188:191], v[56:59]\n"
  "ds_read_b64_tr_b16 a[150:151], v22 offset:4608\n"
  "ds_read_b64_tr_b16 a[156:157], v23 offset:4096\n"
  "ds_read_b64_tr_b16 a[158:159], v23 offset:4608\n"
  "ds_read_b64_tr_b16 a[164:165], v24 offset:2048\n"
  "ds_read_b64_tr_b16 a[166:167], v24 offset:2304\n"
  "s_waitcnt lgkmcnt(15)\n"
  "v_mfma_f32_16x16x32_bf16 v[32:35], a[112:115], a[192:195], v[32:35]\n"
  "ds_read_b128 a[176:179], v16 offset:2048\n"
  "s_waitcnt lgkmcnt(13)\n"
  "v_mfma_f32_16x16x32_bf16 v[32:35], a[116:119], a[196:199], v[32:35]\n"
  "ds_read_b128 a[180:183], v17 offset:2048\n"
  "buffer_load_dwordx4 a[232:235], v0, s[72:75], s17 offen offset:3072\n"
  "v_mfma_f32_16x16x32_bf16 v[36:39], a[120:123], a[192:195], v[36:39]\n"
  "s_waitcnt lgkmcnt(12)\n"
  "v_mfma_f32_16x16x32_bf16 v[36:39], a[124:127], a[196:199], v[36:39]\n"
  "v_mfma_f32_16x16x32_bf16 v[40:43], a[128:131], a[192:195], v[40:43]\n"
  "s_waitcnt lgkmcnt(10)\n"
  "v_mfma_f32_16x16x32_bf16 v[40:43], a[132:135], a[196:199], v[40:43]\n"
  "v_cvt_pk_bf16_f32 v32, v32, v33\n"
  "v_cvt_pk_bf16_f32 v33, v34, v35\n"
  "v_mfma_f32_16x16x32_bf16 v[44:47], a[136:139], a[192:195], v[44:47]\n"
  "s_waitcnt lgkmcnt(8)\n"
  "v_mfma_f32_16x16x32_bf16 v[44:47], a[140:143], a[196:199], v[44:47]\n"
  "v_cvt_pk_bf16_f32 v34, v36, v37\n"
  "v_cvt_pk_bf16_f32 v35, v38, v39\n"
  "v_mfma_f32_16x16x32_bf16 v[48:51], a[144:147], a[192:195], v[48:51]\n"
  "v_pk_max_i16 v32, v32, 0\n"
  "v_pk_max_i16 v33, v33, 0\n"
  "s_waitcnt lgkmcnt(6)\n"
  "v_mfma_f32_16x16x32_bf16 v[48:51], a[148:151], a[196:199], v[48:51]\n"
  "v_pk_max_i16 v34, v34, 0\n"
  "v_pk_max_i16 v35, v35, 0\n"
  "v_mfma_f32_16x16x32_bf16 v[52:55], a[152:155], a[192:195], v[52:55]\n"
  "buffer_store_dwordx4 v[32:35], v0, s[28:31], s33 offen offset:0 sc1\n"
  "s_waitcnt lgkmcnt(4)\n"
  "v_mfma_f32_16x16x32_bf16 v[52:55], a[156:159], a[196:199], v[52:55]\n"
  "v_cvt_pk_bf16_f32 v40, v40, v41\n"
  "v_cvt_pk_bf16_f32 v41, v42, v43\n"
  "v_mfma_f32_16x16x32_bf16 v[56:59], a[160:163], a[192:195], v[56:59]\n"
  "v_cvt_pk_bf16_f32 v42, v44, v45\n"
  "v_cvt_pk_bf16_f32 v43, v46, v47\n"
  "s_waitcnt lgkmcnt(2)\n"
  "v_mfma_f32_16x16x32_bf16 v[56:59], a[164:167], a[196:199], v[56:59]\n"
  "v_pk_max_i16 v40, v40, 0\n"
  "v_pk_max_i16 v41, v41, 0\n"
  "v_mfma_f32_16x16x32_bf16 v[200:203], a[56:59], a[168:171], v[200:203]\n"
  "ds_read_b128 a[184:187], v9 offset:2048\n"
  "v_pk_max_i16 v42, v42, 0\n"
  "v_pk_max_i16 v43, v43, 0\n"
  "v_mfma_f32_16x16x32_bf16 v[204:207], a[64:67], a[168:171], v[204:207]\n"
  "ds_read_b128 a[188:191], v10 offset:2048\n"
  "buffer_store_dwordx4 v[40:43], v0, s[28:31], s34 offen offset:0 sc1\n"
  "v_mfma_f32_16x16x32_bf16 v[208:211], a[72:75], a[168:171], v[208:211]\n"
  "v_cvt_pk_bf16_f32 v48, v48, v49\n"
  "v_cvt_pk_bf16_f32 v49, v50, v51\n"
  "v_mfma_f32_16x16x32_bf16 v[212:215], a[80:83], a[168:171], v[212:215]\n"
  "v_cvt_pk_bf16_f32 v50, v52, v53\n"
  "v_cvt_pk_bf16_f32 v51, v54, v55\n"
  "v_mfma_f32_16x16x32_bf16 v[216:219], a[88:91], a[168:171], v[216:219]\n"
  "v_pk_max_i16 v48, v48, 0\n"
  "v_pk_max_i16 v49, v49, 0\n"
  "v_mfma_f32_16x16x32_bf16 v[220:223], a[96:99], a[168:171], v[220:223]\n"
  "v_pk_max_i16 v50, v50, 0\n"
  "v_pk_max_i16 v51, v51, 0\n"
  "v_mfma_f32_16x16x32_bf16 v[224:227], a[104:107], a[168:171], v[224:227]\n"
  "buffer_store_dwordx4 v[48:51], v0, s[28:31], s35 offen offset:0 sc1\n"
  "v_mfma_f32_16x16x32_bf16 v[200:203], a[60:63], a[172:175], v[200:203]\n"
  "v_mfma_f32_16x16x32_bf16 v[204:207], a[68:71], a[172:175], v[204:207]\n"
  "v_mfma_f32_16x16x32_bf16 v[208:211], a[76:79], a[172:175], v[208:211]\n"
  "v_mfma_f32_16x16x32_bf16 v[212:215], a[84:87], a[172:175], v[212:215]\n"
  "v_mfma_f32_16x16x32_bf16 v[216:219], a[92:95], a[172:175], v[216:219]\n"
  "v_mfma_f32_16x16x32_bf16 v[220:223], a[100:103], a[172:175], v[220:223]\n"
  "v_mfma_f32_16x16x32_bf16 v[224:227], a[108:111], a[172:175], v[224:227]\n"
  "s_waitcnt lgkmcnt(2)\n"
  "v_mfma_f32_16x16x32_bf16 v[60:63], a[0:3], a[176:179], v[60:63]\n"
  "ds_read_b128 a[192:195], v16 offset:47104\n"
  "v_mfma_f32_16x16x32_bf16 v[64:67], a[8:11], a[176:179], v[64:67]\n"
  "ds_read_b128 a[196:199], v17 offset:47104\n"
  "v_mfma_f32_16x16x32_bf16 v[68:71], a[16:19], a[176:179], v[68:71]\n"
  "v_mfma_f32_16x16x32_bf16 v[72:75], a[24:27], a[176:179], v[72:75]\n"
  "v_mfma_f32_16x16x32_bf16 v[76:79], a[32:35], a[176:179], v[76:79]\n"
  "v_mfma_f32_16x16x32_bf16 v[80:83], a[40:43], a[176:179], v[80:83]\n"
  "v_mfma_f32_16x16x32_bf16 v[84:87], a[48:51], a[176:179], v[84:87]\n"
  "v_mfma_f32_16x16x32_bf16 v[60:63], a[4:7], a[180:183], v[60:63]\n"
  "v_mfma_f32_16x16x32_bf16 v[64:67], a[12:15], a[180:183], v[64:67]\n"
  "v_mfma_f32_16x16x32_bf16 v[68:71], a[20:23], a[180:183], v[68:71]\n"
  "v_mfma_f32_16x16x32_bf16 v[72:75], a[28:31], a[180:183], v[72:75]\n"
  "v_mfma_f32_16x16x32_bf16 v[76:79], a[36:39], a[180:183], v[76:79]\n"
  "v_mfma_f32_16x16x32_bf16 v[80:83], a[44:47], a[180:183], v[80:83]\n"
  "v_mfma_f32_16x16x32_bf16 v[84:87], a[52:55], a[180:183], v[84:87]\n"
  "s_waitcnt lgkmcnt(2)\n"
  "v_mfma_f32_16x16x32_bf16 v[60:63], a[112:115], a[184:187], v[60:63]\n"
  "ds_read_b128 a[168:171], v16 offset:4096\n"
  "v_mfma_f32_16x16x32_bf16 v[60:63], a[116:119], a[188:191], v[60:63]\n"
  "ds_read_b128 a[172:175], v17 offset:4096\n"
  "v_mfma_f32_16x16x32_bf16 v[64:67], a[120:123], a[184:187], v[64:67]\n"
  "v_mfma_f32_16x16x32_bf16 v[64:67], a[124:127], a[188:191], v[64:67]\n"
  "v_mfma_f32_16x16x32_bf16 v[68:71], a[128:131], a[184:187], v[68:71]\n"
  "v_mfma_f32_16x16x32_bf16 v[68:71], a[132:135], a[188:191], v[68:71]\n"
  "s_nop 2\n"
  "v_cvt_pk_bf16_f32 v60, v60, v61\n"
  "v_cvt_pk_bf16_f32 v61, v62, v63\n"
  "v_mfma_f32_16x16x32_bf16 v[72:75], a[136:139], a[184:187], v[72:75]\n"
  "v_mfma_f32_16x16x32_bf16 v[72:75], a[140:143], a[188:191], v[72:75]\n"
  "v_cvt_pk_bf16_f32 v62, v64, v65\n"
  "v_cvt_pk_bf16_f32 v63, v66, v67\n"
  "v_mfma_f32_16x16x32_bf16 v[76:79], a[144:147], a[184:187], v[76:79]\n"
  "v_pk_max_i16 v60, v60, 0\n"
  "v_pk_max_i16 v61, v61, 0\n"
  "v_mfma_f32_16x16x32_bf16 v[76:79], a[148:151], a[188:191], v[76:79]\n"
  "v_pk_max_i16 v62, v62, 0\n"
  "v_pk_max_i16 v63, v63, 0\n"
  "v_mfma_f32_16x16x32_bf16 v[80:83], a[152:155], a[184:187], v[80:83]\n"
  "buffer_store_dwordx4 v[60:63], v0, s[28:31], s33 offen offset:2048 sc1\n"
  "v_mfma_f32_16x16x32_bf16 v[80:83], a[156:159], a[188:191], v[80:83]\n"
  "v_cvt_pk_bf16_f32 v68, v68, v69\n"
  "v_cvt_pk_bf16_f32 v69, v70, v71\n"
  "v_mfma_f32_16x16x32_bf16 v[84:87], a[160:163], a[184:187], v[84:87]\n"
  "v_cvt_pk_bf16_f32 v70, v72, v73\n"
  "v_cvt_pk_bf16_f32 v71, v74, v75\n"
  "v_mfma_f32_16x16x32_bf16 v[84:87], a[164:167], a[188:191], v[84:87]\n"
  "v_pk_max_i16 v68, v68, 0\n"
  "v_pk_max_i16 v69, v69, 0\n"
  "s_waitcnt lgkmcnt(2)\n"
  "v_mfma_f32_16x16x32_bf16 v[228:231], a[56:59], a[192:195], v[228:231]\n"
  "ds_read_b128 a[176:179], v9 offset:4096\n"
  "v_pk_max_i16 v70, v70, 0\n"
  "v_pk_max_i16 v71, v71, 0\n"
  "v_mfma_f32_16x16x32_bf16 v[232:235], a[64:67], a[192:195], v[232:235]\n"
  "ds_read_b128 a[180:183], v10 offset:4096\n"
  "buffer_store_dwordx4 v[68:71], v0, s[28:31], s34 offen offset:2048 sc1\n"
  "v_mfma_f32_16x16x32_bf16 v[236:239], a[72:75], a[192:195], v[236:239]\n"
  "v_mfma_f32_16x16x32_bf16 v[240:243], a[80:83], a[192:195], v[240:243]\n"
  "v_cvt_pk_bf16_f32 v87, v86, v87\n"
  "v_cvt_pk_bf16_f32 v86, v84, v85\n"
  "v_mfma_f32_16x16x32_bf16 v[244:247], a[88:91], a[192:195], v[244:247]\n"
  "v_cvt_pk_bf16_f32 v84, v56, v57\n"
  "v_cvt_pk_bf16_f32 v85, v58, v59\n"
  "v_mfma_f32_16x16x32_bf16 v[248:251], a[96:99], a[192:195], v[248:251]\n"
  "v_pk_max_i16 v86, v86, 0\n"
  "v_pk_max_i16 v84, v84, 0\n"
  "v_mfma_f32_16x16x32_bf16 v[252:255], a[104:107], a[192:195], v[252:255]\n"
  "v_pk_max_i16 v87, v87, 0\n"
  "v_pk_max_i16 v85, v85, 0\n"
  "v_mfma_f32_16x16x32_bf16 v[228:231], a[60:63], a[196:199], v[228:231]\n"
  "v_cvt_pk_bf16_f32 v76, v76, v77\n"
  "v_cvt_pk_bf16_f32 v77, v78, v79\n"
  "v_mfma_f32_16x16x32_bf16 v[232:235], a[68:71], a[196:199], v[232:235]\n"
  "v_cvt_pk_bf16_f32 v78, v80, v81\n"
  "v_cvt_pk_bf16_f32 v79, v82, v83\n"
  "v_mfma_f32_16x16x32_bf16 v[236:239], a[76:79], a[196:199], v[236:239]\n"
  "v_pk_max_i16 v76, v76, 0\n"
  "v_pk_max_i16 v77, v77, 0\n"
  "v_mfma_f32_16x16x32_bf16 v[240:243], a[84:87], a[196:199], v[240:243]\n"
  "v_pk_max_i16 v78, v78, 0\n"
  "v_pk_max_i16 v79, v79, 0\n"
  "v_mfma_f32_16x16x32_bf16 v[244:247], a[92:95], a[196:199], v[244:247]\n"
  "buffer_store_dwordx4 v[76:79], v0, s[28:31], s35 offen offset:2048 sc1\n"
  "v_mfma_f32_16x16x32_bf16 v[248:251], a[100:103], a[196:199], v[248:251]\n"
  "v_permlane16_swap_b32 v84, v86\n"
  "v_mfma_f32_16x16x32_bf16 v[252:255], a[108:111], a[196:199], v[252:255]\n"
  "v_permlane16_swap_b32 v85, v87\n"
  "s_waitcnt lgkmcnt(2)\n"
  "v_mfma_f32_16x16x32_bf16 v[88:91], a[0:3], a[168:171], v[88:91]\n"
  "ds_read_b128 a[184:187], v16 offset:6144\n"
  "buffer_store_dwordx4 v[84:87], v4, s[28:31], s27 offen sc1\n"
  "v_mfma_f32_16x16x32_bf16 v[92:95], a[8:11], a[168:171], v[92:95]\n"
  "ds_read_b128 a[188:191], v17 offset:6144\n"
  "s_waitcnt vmcnt(7)\n"
  "v_accvgpr_read_b32 v32, a204\n"
  "v_mfma_f32_16x16x32_bf16 v[96:99], a[16:19], a[168:171], v[96:99]\n"
  "v_accvgpr_read_b32 v33, a205\n"
  "v_accvgpr_read_b32 v34, a206\n"
  "v_mfma_f32_16x16x32_bf16 v[100:103], a[24:27], a[168:171], v[100:103]\n"
  "v_accvgpr_read_b32 v35, a207\n"
  "v_accvgpr_read_b32 v36, a208\n"
  "v_mfma_f32_16x16x32_bf16 v[104:107], a[32:35], a[168:171], v[104:107]\n"
  "v_accvgpr_read_b32 v37, a209\n"
  "v_accvgpr_read_b32 v38, a210\n"
  "v_mfma_f32_16x16x32_bf16 v[108:111], a[40:43], a[168:171], v[108:111]\n"
  "v_accvgpr_read_b32 v39, a211\n"
  "v_accvgpr_read_b32 v40, a212\n"
  "v_mfma_f32_16x16x32_bf16 v[112:115], a[48:51], a[168:171], v[112:115]\n"
  "v_accvgpr_read_b32 v41, a213\n"
  "v_accvgpr_read_b32 v42, a214\n"
  "v_mfma_f32_16x16x32_bf16 v[88:91], a[4:7], a[172:175], v[88:91]\n"
  "v_accvgpr_read_b32 v43, a215\n"
  "v_accvgpr_read_b32 v44, a216\n"
  "v_mfma_f32_16x16x32_bf16 v[92:95], a[12:15], a[172:175], v[92:95]\n"
  "v_accvgpr_read_b32 v45, a217\n"
  "v_accvgpr_read_b32 v46, a218\n"
  "v_mfma_f32_16x16x32_bf16 v[96:99], a[20:23], a[172:175], v[96:99]\n"
  "v_accvgpr_read_b32 v47, a219\n"
  "v_accvgpr_read_b32 v48, a220\n"
  "v_mfma_f32_16x16x32_bf16 v[100:103], a[28:31], a[172:175], v[100:103]\n"
  "v_accvgpr_read_b32 v49, a221\n"
  "v_accvgpr_read_b32 v50, a222\n"
  "v_mfma_f32_16x16x32_bf16 v[104:107], a[36:39], a[172:175], v[104:107]\n"
  "v_accvgpr_read_b32 v51, a223\n"
  "v_accvgpr_read_b32 v52, a224\n"
  "v_mfma_f32_16x16x32_bf16 v[108:111], a[44:47], a[172:175], v[108:111]\n"
  "v_accvgpr_read_b32 v53, a225\n"
  "v_accvgpr_read_b32 v54, a226\n"
  "v_mfma_f32_16x16x32_bf16 v[112:115], a[52:55], a[172:175], v[112:115]\n"
  "v_accvgpr_read_b32 v55, a227\n"
  "v_accvgpr_read_b32 v56, a228\n"
  "s_waitcnt lgkmcnt(2)\n"
  "v_mfma_f32_16x16x32_bf16 v[88:91], a[112:115], a[176:179], v[88:91]\n"
  "ds_read_b128 a[192:195], v9 offset:6144\n"
  "v_accvgpr_read_b32 v57, a229\n"
  "v_accvgpr_read_b32 v58, a230\n"
  "v_mfma_f32_16x16x32_bf16 v[88:91], a[116:119], a[180:183], v[88:91]\n"
  "ds_read_b128 a[196:199], v10 offset:6144\n"
  "v_accvgpr_read_b32 v59, a231\n"
  "v_accvgpr_read_b32 v60, a232\n"
  "v_mfma_f32_16x16x32_bf16 v[92:95], a[120:123], a[176:179], v[92:95]\n"
  "v_accvgpr_read_b32 v61, a233\n"
  "v_accvgpr_read_b32 v62, a234\n"
  "v_mfma_f32_16x16x32_bf16 v[92:95], a[124:127], a[180:183], v[92:95]\n"
  "v_accvgpr_read_b32 v63, a235\n"
  "v_accvgpr_read_b32 v64, a236\n"
  "v_mfma_f32_16x16x32_bf16 v[96:99], a[128:131], a[176:179], v[96:99]\n"
  "v_accvgpr_read_b32 v65, a237\n"
  "v_accvgpr_read_b32 v66, a238\n"
  "v_mfma_f32_16x16x32_bf16 v[96:99], a[132:135], a[180:183], v[96:99]\n"
  "v_accvgpr_read_b32 v67, a239\n"
  "v_accvgpr_read_b32 v68, a240\n"
  "v_mfma_f32_16x16x32_bf16 v[100:103], a[136:139], a[176:179], v[100:103]\n"
  "v_accvgpr_read_b32 v69, a241\n"
  "v_accvgpr_read_b32 v70, a242\n"
  "v_mfma_f32_16x16x32_bf16 v[100:103], a[140:143], a[180:183], v[100:103]\n"
  "v_accvgpr_read_b32 v71, a243\n"
  "v_cvt_pk_bf16_f32 v88, v88, v89\n"
  "v_mfma_f32_16x16x32_bf16 v[104:107], a[144:147], a[176:179], v[104:107]\n"
  "v_cvt_pk_bf16_f32 v89, v90, v91\n"
  "v_cvt_pk_bf16_f32 v90, v92, v93\n"
  "v_mfma_f32_16x16x32_bf16 v[104:107], a[148:151], a[180:183], v[104:107]\n"
  "v_cvt_pk_bf16_f32 v91, v94, v95\n"
  "v_pk_max_i16 v88, v88, 0\n"
  "v_mfma_f32_16x16x32_bf16 v[108:111], a[152:155], a[176:179], v[108:111]\n"
  "v_pk_max_i16 v89, v89, 0\n"
  "v_pk_max_i16 v90, v90, 0\n"
  "v_mfma_f32_16x16x32_bf16 v[108:111], a[156:159], a[180:183], v[108:111]\n"
  "v_pk_max_i16 v91, v91, 0\n"
  "buffer_store_dwordx4 v[88:91], v1, s[28:31], s33 offen offset:0 sc1\n"
  "v_mfma_f32_16x16x32_bf16 v[112:115], a[160:163], a[176:179], v[112:115]\n"
  "v_cvt_pk_bf16_f32 v96, v96, v97\n"
  "v_cvt_pk_bf16_f32 v97, v98, v99\n"
  "v_mfma_f32_16x16x32_bf16 v[112:115], a[164:167], a[180:183], v[112:115]\n"
  "v_cvt_pk_bf16_f32 v98, v100, v101\n"
  "v_cvt_pk_bf16_f32 v99, v102, v103\n"
  "s_waitcnt lgkmcnt(2)\n"
  "v_mfma_f32_16x16x32_bf16 v[116:119], a[0:3], a[184:187], v[116:119]\n"
  "ds_read_b128 a[168:171], v16 offset:8192\n"
  "v_pk_max_i16 v96, v96, 0\n"
  "v_pk_max_i16 v97, v97, 0\n"
  "v_mfma_f32_16x16x32_bf16 v[120:123], a[8:11], a[184:187], v[120:123]\n"
  "ds_read_b128 a[172:175], v17 offset:8192\n"
  "v_pk_max_i16 v98, v98, 0\n"
  "v_pk_max_i16 v99, v99, 0\n"
  "v_mfma_f32_16x16x32_bf16 v[124:127], a[16:19], a[184:187], v[124:127]\n"
  "buffer_store_dwordx4 v[96:99], v1, s[28:31], s34 offen offset:0 sc1\n"
  "v_mfma_f32_16x16x32_bf16 v[128:131], a[24:27], a[184:187], v[128:131]\n"
  "v_cvt_pk_bf16_f32 v104, v104, v105\n"
  "v_cvt_pk_bf16_f32 v105, v106, v107\n"
  "v_mfma_f32_16x16x32_bf16 v[132:135], a[32:35], a[184:187], v[132:135]\n"
  "v_cvt_pk_bf16_f32 v106, v108, v109\n"
  "v_cvt_pk_bf16_f32 v107, v110, v111\n"
  "v_mfma_f32_16x16x32_bf16 v[136:139], a[40:43], a[184:187], v[136:139]\n"
  "v_pk_max_i16 v104, v104, 0\n"
  "v_pk_max_i16 v105, v105, 0\n"
  "v_mfma_f32_16x16x32_bf16 v[140:143], a[48:51], a[184:187], v[140:143]\n"
  "v_pk_max_i16 v106, v106, 0\n"
  "v_pk_max_i16 v107, v107, 0\n"
  "v_mfma_f32_16x16x32_bf16 v[116:119], a[4:7], a[188:191], v[116:119]\n"
  "buffer_store_dwordx4 v[104:107], v1, s[28:31], s35 offen offset:0 sc1\n"
  "v_mfma_f32_16x16x32_bf16 v[120:123], a[12:15], a[188:191], v[120:123]\n"
  "v_mfma_f32_16x16x32_bf16 v[124:127], a[20:23], a[188:191], v[124:127]\n"
  "v_mfma_f32_16x16x32_bf16 v[128:131], a[28:31], a[188:191], v[128:131]\n"
  "v_mfma_f32_16x16x32_bf16 v[132:135], a[36:39], a[188:191], v[132:135]\n"
  "v_mfma_f32_16x16x32_bf16 v[136:139], a[44:47], a[188:191], v[136:139]\n"
  "v_mfma_f32_16x16x32_bf16 v[140:143], a[52:55], a[188:191], v[140:143]\n"
  "s_waitcnt lgkmcnt(2)\n"
  "v_mfma_f32_16x16x32_bf16 v[116:119], a[112:115], a[192:195], v[116:119]\n"
  "ds_read_b128 a[176:179], v9 offset:8192\n"
  "v_mfma_f32_16x16x32_bf16 v[116:119], a[116:119], a[196:199], v[116:119]\n"
  "ds_read_b128 a[180:183], v10 offset:8192\n"
  "v_mfma_f32_16x16x32_bf16 v[120:123], a[120:123], a[192:195], v[120:123]\n"
  "v_mfma_f32_16x16x32_bf16 v[120:123], a[124:127], a[196:199], v[120:123]\n"
  "v_mfma_f32_16x16x32_bf16 v[124:127], a[128:131], a[192:195], v[124:127]\n"
  "v_mfma_f32_16x16x32_bf16 v[124:127], a[132:135], a[196:199], v[124:127]\n"
  "s_nop 2\n"
  "v_cvt_pk_bf16_f32 v116, v116, v117\n"
  "v_cvt_pk_bf16_f32 v117, v118, v119\n"
  "v_mfma_f32_16x16x32_bf16 v[128:131], a[136:139], a[192:195], v[128:131]\n"
  "v_mfma_f32_16x16x32_bf16 v[128:131], a[140:143], a[196:199], v[128:131]\n"
  "v_cvt_pk_bf16_f32 v118, v120, v121\n"
  "v_cvt_pk_bf16_f32 v119, v122, v123\n"
  "v_mfma_f32_16x16x32_bf16 v[132:135], a[144:147], a[192:195], v[132:135]\n"
  "v_pk_max_i16 v116, v116, 0\n"
  "v_pk_max_i16 v117, v117, 0\n"
  "v_mfma_f32_16x16x32_bf16 v[132:135], a[148:151], a[196:199], v[132:135]\n"
  "v_pk_max_i16 v118, v118, 0\n"
  "v_pk_max_i16 v119, v119, 0\n"
  "v_mfma_f32_16x16x32_bf16 v[136:139], a[152:155], a[192:195], v[136:139]\n"
  "buffer_store_dwordx4 v[116:119], v1, s[28:31], s33 offen offset:2048 sc1\n"
  "v_mfma_f32_16x16x32_bf16 v[136:139], a[156:159], a[196:199], v[136:139]\n"
  "v_cvt_pk_bf16_f32 v124, v124, v125\n"
  "v_cvt_pk_bf16_f32 v125, v126, v127\n"
  "v_mfma_f32_16x16x32_bf16 v[140:143], a[160:163], a[192:195], v[140:143]\n"
  "v_cvt_pk_bf16_f32 v126, v128, v129\n"
  "v_cvt_pk_bf16_f32 v127, v130, v131\n"
  "v_mfma_f32_16x16x32_bf16 v[140:143], a[164:167], a[196:199], v[140:143]\n"
  "v_pk_max_i16 v124, v124, 0\n"
  "v_pk_max_i16 v125, v125, 0\n"
  "s_waitcnt lgkmcnt(2)\n"
  "v_mfma_f32_16x16x32_bf16 v[144:147], a[0:3], a[168:171], v[144:147]\n"
  "ds_read_b128 a[184:187], v16 offset:10240\n"
  "v_pk_max_i16 v126, v126, 0\n"
  "v_pk_max_i16 v127, v127, 0\n"
  "v_mfma_f32_16x16x32_bf16 v[148:151], a[8:11], a[168:171], v[148:151]\n"
  "ds_read_b128 a[188:191], v17 offset:10240\n"
  "buffer_store_dwordx4 v[124:127], v1, s[28:31], s34 offen offset:2048 sc1\n"
  "v_mfma_f32_16x16x32_bf16 v[152:155], a[16:19], a[168:171], v[152:155]\n"
  "v_mfma_f32_16x16x32_bf16 v[156:159], a[24:27], a[168:171], v[156:159]\n"
  "v_cvt_pk_bf16_f32 v143, v142, v143\n"
  "v_cvt_pk_bf16_f32 v142, v140, v141\n"
  "v_mfma_f32_16x16x32_bf16 v[160:163], a[32:35], a[168:171], v[160:163]\n"
  "v_cvt_pk_bf16_f32 v140, v112, v113\n"
  "v_cvt_pk_bf16_f32 v141, v114, v115\n"
  "v_mfma_f32_16x16x32_bf16 v[164:167], a[40:43], a[168:171], v[164:167]\n"
  "v_pk_max_i16 v142, v142, 0\n"
  "v_pk_max_i16 v140, v140, 0\n"
  "v_mfma_f32_16x16x32_bf16 v[168:171], a[48:51], a[168:171], v[168:171]\n"
  "v_pk_max_i16 v143, v143, 0\n"
  "v_pk_max_i16 v141, v141, 0\n"
  "v_mfma_f32_16x16x32_bf16 v[144:147], a[4:7], a[172:175], v[144:147]\n"
  "v_cvt_pk_bf16_f32 v132, v132, v133\n"
  "v_cvt_pk_bf16_f32 v133, v134, v135\n"
  "v_mfma_f32_16x16x32_bf16 v[148:151], a[12:15], a[172:175], v[148:151]\n"
  "v_cvt_pk_bf16_f32 v134, v136, v137\n"
  "v_cvt_pk_bf16_f32 v135, v138, v139\n"
  "v_mfma_f32_16x16x32_bf16 v[152:155], a[20:23], a[172:175], v[152:155]\n"
  "v_pk_max_i16 v132, v132, 0\n"
  "v_pk_max_i16 v133, v133, 0\n"
  "v_mfma_f32_16x16x32_bf16 v[156:159], a[28:31], a[172:175], v[156:159]\n"
  "v_pk_max_i16 v134, v134, 0\n"
  "v_pk_max_i16 v135, v135, 0\n"
  "v_mfma_f32_16x16x32_bf16 v[160:163], a[36:39], a[172:175], v[160:163]\n"
  "buffer_store_dwordx4 v[132:135], v1, s[28:31], s35 offen offset:2048 sc1\n"
  "v_mfma_f32_16x16x32_bf16 v[164:167], a[44:47], a[172:175], v[164:167]\n"
  "v_permlane16_swap_b32 v140, v142\n"
  "v_mfma_f32_16x16x32_bf16 v[168:171], a[52:55], a[172:175], v[168:171]\n"
  "v_permlane16_swap_b32 v141, v143\n"
  "s_waitcnt lgkmcnt(2)\n"
  "v_mfma_f32_16x16x32_bf16 v[144:147], a[112:115], a[176:179], v[144:147]\n"
  "ds_read_b128 a[192:195], v9 offset:10240\n"
  "buffer_store_dwordx4 v[140:143], v5, s[28:31], s27 offen sc1\n"
  "v_mfma_f32_16x16x32_bf16 v[144:147], a[116:119], a[180:183], v[144:147]\n"
  "ds_read_b128 a[196:199], v10 offset:10240\n"
  "v_mfma_f32_16x16x32_bf16 v[148:151], a[120:123], a[176:179], v[148:151]\n"
  "v_mfma_f32_16x16x32_bf16 v[148:151], a[124:127], a[180:183], v[148:151]\n"
  "v_mfma_f32_16x16x32_bf16 v[152:155], a[128:131], a[176:179], v[152:155]\n"
  "v_mfma_f32_16x16x32_bf16 v[152:155], a[132:135], a[180:183], v[152:155]\n"
  "s_nop 2\n"
  "v_cvt_pk_bf16_f32 v144, v144, v145\n"
  "v_cvt_pk_bf16_f32 v145, v146, v147\n"
  "v_mfma_f32_16x16x32_bf16 v[156:159], a[136:139], a[176:179], v[156:159]\n"
  "v_mfma_f32_16x16x32_bf16 v[156:159], a[140:143], a[180:183], v[156:159]\n"
  "v_cvt_pk_bf16_f32 v146, v148, v149\n"
  "v_cvt_pk_bf16_f32 v147, v150, v151\n"
  "v_mfma_f32_16x16x32_bf16 v[160:163], a[144:147], a[176:179], v[160:163]\n"
  "v_pk_max_i16 v144, v144, 0\n"
  "v_pk_max_i16 v145, v145, 0\n"
  "v_mfma_f32_16x16x32_bf16 v[160:163], a[148:151], a[180:183], v[160:163]\n"
  "v_pk_max_i16 v146, v146, 0\n"
  "v_pk_max_i16 v147, v147, 0\n"
  "v_mfma_f32_16x16x32_bf16 v[164:167], a[152:155], a[176:179], v[164:167]\n"
  "buffer_store_dwordx4 v[144:147], v2, s[28:31], s33 offen offset:0 sc1\n"
  "v_mfma_f32_16x16x32_bf16 v[164:167], a[156:159], a[180:183], v[164:167]\n"
  "v_cvt_pk_bf16_f32 v152, v152, v153\n"
  "v_cvt_pk_bf16_f32 v153, v154, v155\n"
  "v_mfma_f32_16x16x32_bf16 v[168:171], a[160:163], a[176:179], v[168:171]\n"
  "v_cvt_pk_bf16_f32 v154, v156, v157\n"
  "v_cvt_pk_bf16_f32 v155, v158, v159\n"
  "v_mfma_f32_16x16x32_bf16 v[168:171], a[164:167], a[180:183], v[168:171]\n"
  "v_pk_max_i16 v152, v152, 0\n"
  "v_pk_max_i16 v153, v153, 0\n"
  "s_waitcnt lgkmcnt(2)\n"
  "v_mfma_f32_16x16x32_bf16 v[172:175], a[0:3], a[184:187], v[172:175]\n"
  "ds_read_b128 a[168:171], v16 offset:12288\n"
  "v_pk_max_i16 v154, v154, 0\n"
  "v_pk_max_i16 v155, v155, 0\n"
  "v_mfma_f32_16x16x32_bf16 v[176:179], a[8:11], a[184:187], v[176:179]\n"
  "ds_read_b128 a[172:175], v17 offset:12288\n"
  "buffer_store_dwordx4 v[152:155], v2, s[28:31], s34 offen offset:0 sc1\n"
  "v_mfma_f32_16x16x32_bf16 v[180:183], a[16:19], a[184:187], v[180:183]\n"
  "v_cvt_pk_bf16_f32 v160, v160, v161\n"
  "v_cvt_pk_bf16_f32 v161, v162, v163\n"
  "v_mfma_f32_16x16x32_bf16 v[184:187], a[24:27], a[184:187], v[184:187]\n"
  "v_cvt_pk_bf16_f32 v162, v164, v165\n"
  "v_cvt_pk_bf16_f32 v163, v166, v167\n"
  "v_mfma_f32_16x16x32_bf16 v[188:191], a[32:35], a[184:187], v[188:191]\n"
  "v_pk_max_i16 v160, v160, 0\n"
  "v_pk_max_i16 v161, v161, 0\n"
  "v_mfma_f32_16x16x32_bf16 v[192:195], a[40:43], a[184:187], v[192:195]\n"
  "v_pk_max_i16 v162, v162, 0\n"
  "v_pk_max_i16 v163, v163, 0\n"
  "v_mfma_f32_16x16x32_bf16 v[196:199], a[48:51], a[184:187], v[196:199]\n"
  "buffer_store_dwordx4 v[160:163], v2, s[28:31], s35 offen offset:0 sc1\n"
  "v_mfma_f32_16x16x32_bf16 v[172:175], a[4:7], a[188:191], v[172:175]\n"
  "v_mfma_f32_16x16x32_bf16 v[176:179], a[12:15], a[188:191], v[176:179]\n"
  "v_mfma_f32_16x16x32_bf16 v[180:183], a[20:23], a[188:191], v[180:183]\n"
  "v_mfma_f32_16x16x32_bf16 v[184:187], a[28:31], a[188:191], v[184:187]\n"
  "v_mfma_f32_16x16x32_bf16 v[188:191], a[36:39], a[188:191], v[188:191]\n"
  "v_mfma_f32_16x16x32_bf16 v[192:195], a[44:47], a[188:191], v[192:195]\n"
  "v_mfma_f32_16x16x32_bf16 v[196:199], a[52:55], a[188:191], v[196:199]\n"
  "s_waitcnt lgkmcnt(2)\n"
  "v_mfma_f32_16x16x32_bf16 v[172:175], a[112:115], a[192:195], v[172:175]\n"
  "ds_read_b128 a[176:179], v9 offset:12288\n"
  "v_mfma_f32_16x16x32_bf16 v[172:175], a[116:119], a[196:199], v[172:175]\n"
  "ds_read_b128 a[180:183], v10 offset:12288\n"
  "v_mfma_f32_16x16x32_bf16 v[176:179], a[120:123], a[192:195], v[176:179]\n"
  "v_mfma_f32_16x16x32_bf16 v[176:179], a[124:127], a[196:199], v[176:179]\n"
  "v_mfma_f32_16x16x32_bf16 v[180:183], a[128:131], a[192:195], v[180:183]\n"
  "v_mfma_f32_16x16x32_bf16 v[180:183], a[132:135], a[196:199], v[180:183]\n"
  "s_nop 2\n"
  "v_cvt_pk_bf16_f32 v172, v172, v173\n"
  "v_cvt_pk_bf16_f32 v173, v174, v175\n"
  "v_mfma_f32_16x16x32_bf16 v[184:187], a[136:139], a[192:195], v[184:187]\n"
  "v_mfma_f32_16x16x32_bf16 v[184:187], a[140:143], a[196:199], v[184:187]\n"
  "v_cvt_pk_bf16_f32 v174, v176, v177\n"
  "v_cvt_pk_bf16_f32 v175, v178, v179\n"
  "v_mfma_f32_16x16x32_bf16 v[188:191], a[144:147], a[192:195], v[188:191]\n"
  "v_pk_max_i16 v172, v172, 0\n"
  "v_pk_max_i16 v173, v173, 0\n"
  "v_mfma_f32_16x16x32_bf16 v[188:191], a[148:151], a[196:199], v[188:191]\n"
  "v_pk_max_i16 v174, v174, 0\n"
  "v_pk_max_i16 v175, v175, 0\n"
  "v_mfma_f32_16x16x32_bf16 v[192:195], a[152:155], a[192:195], v[192:195]\n"
  "buffer_store_dwordx4 v[172:175], v2, s[28:31], s33 offen offset:2048 sc1\n"
  "v_mfma_f32_16x16x32_bf16 v[192:195], a[156:159], a[196:199], v[192:195]\n"
  "v_cvt_pk_bf16_f32 v180, v180, v181\n"
  "v_cvt_pk_bf16_f32 v181, v182, v183\n"
  "v_mfma_f32_16x16x32_bf16 v[196:199], a[160:163], a[192:195], v[196:199]\n"
  "v_cvt_pk_bf16_f32 v182, v184, v185\n"
  "v_cvt_pk_bf16_f32 v183, v186, v187\n"
  "v_mfma_f32_16x16x32_bf16 v[196:199], a[164:167], a[196:199], v[196:199]\n"
  "v_pk_max_i16 v180, v180, 0\n"
  "v_pk_max_i16 v181, v181, 0\n"
  "s_waitcnt lgkmcnt(2)\n"
  "v_mfma_f32_16x16x32_bf16 v[200:203], a[0:3], a[168:171], v[200:203]\n"
  "ds_read_b128 a[184:187], v16 offset:14336\n"
  "v_pk_max_i16 v182, v182, 0\n"
  "v_pk_max_i16 v183, v183, 0\n"
  "v_mfma_f32_16x16x32_bf16 v[204:207], a[8:11], a[168:171], v[204:207]\n"
  "ds_read_b128 a[188:191], v17 offset:14336\n"
  "buffer_store_dwordx4 v[180:183], v2, s[28:31], s34 offen offset:2048 sc1\n"
  "v_mfma_f32_16x16x32_bf16 v[208:211], a[16:19], a[168:171], v[208:211]\n"
  "v_mfma_f32_16x16x32_bf16 v[212:215], a[24:27], a[168:171], v[212:215]\n"
  "v_cvt_pk_bf16_f32 v199, v198, v199\n"
  "v_cvt_pk_bf16_f32 v198, v196, v197\n"
  "v_mfma_f32_16x16x32_bf16 v[216:219], a[32:35], a[168:171], v[216:219]\n"
  "v_cvt_pk_bf16_f32 v196, v168, v169\n"
  "v_cvt_pk_bf16_f32 v197, v170, v171\n"
  "v_mfma_f32_16x16x32_bf16 v[220:223], a[40:43], a[168:171], v[220:223]\n"
  "v_pk_max_i16 v198, v198, 0\n"
  "v_pk_max_i16 v196, v196, 0\n"
  "v_mfma_f32_16x16x32_bf16 v[224:227], a[48:51], a[168:171], v[224:227]\n"
  "v_pk_max_i16 v199, v199, 0\n"
  "v_pk_max_i16 v197, v197, 0\n"
  "v_mfma_f32_16x16x32_bf16 v[200:203], a[4:7], a[172:175], v[200:203]\n"
  "v_cvt_pk_bf16_f32 v188, v188, v189\n"
  "v_cvt_pk_bf16_f32 v189, v190, v191\n"
  "v_mfma_f32_16x16x32_bf16 v[204:207], a[12:15], a[172:175], v[204:207]\n"
  "v_cvt_pk_bf16_f32 v190, v192, v193\n"
  "v_cvt_pk_bf16_f32 v191, v194, v195\n"
  "v_mfma_f32_16x16x32_bf16 v[208:211], a[20:23], a[172:175], v[208:211]\n"
  "v_pk_max_i16 v188, v188, 0\n"
  "v_pk_max_i16 v189, v189, 0\n"
  "v_mfma_f32_16x16x32_bf16 v[212:215], a[28:31], a[172:175], v[212:215]\n"
  "v_pk_max_i16 v190, v190, 0\n"
  "v_pk_max_i16 v191, v191, 0\n"
  "v_mfma_f32_16x16x32_bf16 v[216:219], a[36:39], a[172:175], v[216:219]\n"
  "buffer_store_dwordx4 v[188:191], v2, s[28:31], s35 offen offset:2048 sc1\n"
  "v_mfma_f32_16x16x32_bf16 v[220:223], a[44:47], a[172:175], v[220:223]\n"
  "v_permlane16_swap_b32 v196, v198\n"
  "v_mfma_f32_16x16x32_bf16 v[224:227], a[52:55], a[172:175], v[224:227]\n"
  "v_permlane16_swap_b32 v197, v199\n"
  "s_waitcnt lgkmcnt(2)\n"
  "v_mfma_f32_16x16x32_bf16 v[200:203], a[112:115], a[176:179], v[200:203]\n"
  "ds_read_b128 a[192:195], v9 offset:14336\n"
  "buffer_store_dwordx4 v[196:199], v6, s[28:31], s27 offen sc1\n"
  "v_mfma_f32_16x16x32_bf16 v[200:203], a[116:119], a[180:183], v[200:203]\n"
  "ds_read_b128 a[196:199], v10 offset:14336\n"
  "v_mfma_f32_16x16x32_bf16 v[204:207], a[120:123], a[176:179], v[204:207]\n"
  "v_mfma_f32_16x16x32_bf16 v[204:207], a[124:127], a[180:183], v[204:207]\n"
  "v_mfma_f32_16x16x32_bf16 v[208:211], a[128:131], a[176:179], v[208:211]\n"
  "v_mfma_f32_16x16x32_bf16 v[208:211], a[132:135], a[180:183], v[208:211]\n"
  "s_nop 2\n"
  "v_cvt_pk_bf16_f32 v200, v200, v201\n"
  "v_cvt_pk_bf16_f32 v201, v202, v203\n"
  "v_mfma_f32_16x16x32_bf16 v[212:215], a[136:139], a[176:179], v[212:215]\n"
  "v_mfma_f32_16x16x32_bf16 v[212:215], a[140:143], a[180:183], v[212:215]\n"
  "v_cvt_pk_bf16_f32 v202, v204, v205\n"
  "v_cvt_pk_bf16_f32 v203, v206, v207\n"
  "v_mfma_f32_16x16x32_bf16 v[216:219], a[144:147], a[176:179], v[216:219]\n"
  "v_pk_max_i16 v200, v200, 0\n"
  "v_pk_max_i16 v201, v201, 0\n"
  "v_mfma_f32_16x16x32_bf16 v[216:219], a[148:151], a[180:183], v[216:219]\n"
  "v_pk_max_i16 v202, v202, 0\n"
  "v_pk_max_i16 v203, v203, 0\n"
  "v_mfma_f32_16x16x32_bf16 v[220:223], a[152:155], a[176:179], v[220:223]\n"
  "buffer_store_dwordx4 v[200:203], v3, s[28:31], s33 offen offset:0 sc1\n"
  "v_mfma_f32_16x16x32_bf16 v[220:223], a[156:159], a[180:183], v[220:223]\n"
  "v_cvt_pk_bf16_f32 v208, v208, v209\n"
  "v_cvt_pk_bf16_f32 v209, v210, v211\n"
  "v_mfma_f32_16x16x32_bf16 v[224:227], a[160:163], a[176:179], v[224:227]\n"
  "v_cvt_pk_bf16_f32 v210, v212, v213\n"
  "v_cvt_pk_bf16_f32 v211, v214, v215\n"
  "v_mfma_f32_16x16x32_bf16 v[224:227], a[164:167], a[180:183], v[224:227]\n"
  "v_pk_max_i16 v208, v208, 0\n"
  "v_pk_max_i16 v209, v209, 0\n"
  "s_waitcnt lgkmcnt(2)\n"
  "v_mfma_f32_16x16x32_bf16 v[228:231], a[0:3], a[184:187], v[228:231]\n"
  "v_pk_max_i16 v210, v210, 0\n"
  "v_pk_max_i16 v211, v211, 0\n"
  "v_mfma_f32_16x16x32_bf16 v[232:235], a[8:11], a[184:187], v[232:235]\n"
  "buffer_store_dwordx4 v[208:211], v3, s[28:31], s34 offen offset:0 sc1\n"
  "v_mfma_f32_16x16x32_bf16 v[236:239], a[16:19], a[184:187], v[236:239]\n"
  "v_cvt_pk_bf16_f32 v216, v216, v217\n"
  "v_cvt_pk_bf16_f32 v217, v218, v219\n"
  "v_mfma_f32_16x16x32_bf16 v[240:243], a[24:27], a[184:187], v[240:243]\n"
  "v_cvt_pk_bf16_f32 v218, v220, v221\n"
  "v_cvt_pk_bf16_f32 v219, v222, v223\n"
  "v_mfma_f32_16x16x32_bf16 v[244:247], a[32:35], a[184:187], v[244:247]\n"
  "v_pk_max_i16 v216, v216, 0\n"
  "v_pk_max_i16 v217, v217, 0\n"
  "v_mfma_f32_16x16x32_bf16 v[248:251], a[40:43], a[184:187], v[248:251]\n"
  "v_pk_max_i16 v218, v218, 0\n"
  "v_pk_max_i16 v219, v219, 0\n"
  "v_mfma_f32_16x16x32_bf16 v[252:255], a[48:51], a[184:187], v[252:255]\n"
  "buffer_store_dwordx4 v[216:219], v3, s[28:31], s35 offen offset:0 sc1\n"
  "v_mfma_f32_16x16x32_bf16 v[228:231], a[4:7], a[188:191], v[228:231]\n"
  "v_mfma_f32_16x16x32_bf16 v[232:235], a[12:15], a[188:191], v[232:235]\n"
  "v_mfma_f32_16x16x32_bf16 v[236:239], a[20:23], a[188:191], v[236:239]\n"
  "v_mfma_f32_16x16x32_bf16 v[240:243], a[28:31], a[188:191], v[240:243]\n"
  "v_mfma_f32_16x16x32_bf16 v[244:247], a[36:39], a[188:191], v[244:247]\n"
  "v_mfma_f32_16x16x32_bf16 v[248:251], a[44:47], a[188:191], v[248:251]\n"
  "v_mfma_f32_16x16x32_bf16 v[252:255], a[52:55], a[188:191], v[252:255]\n"
  "s_waitcnt lgkmcnt(0)\n"
  "v_mfma_f32_16x16x32_bf16 v[228:231], a[112:115], a[192:195], v[228:231]\n"
  "v_mfma_f32_16x16x32_bf16 v[228:231], a[116:119], a[196:199], v[228:231]\n"
  "v_mfma_f32_16x16x32_bf16 v[232:235], a[120:123], a[192:195], v[232:235]\n"
  "v_mfma_f32_16x16x32_bf16 v[232:235], a[124:127], a[196:199], v[232:235]\n"
  "v_mfma_f32_16x16x32_bf16 v[236:239], a[128:131], a[192:195], v[236:239]\n"
  "v_mfma_f32_16x16x32_bf16 v[236:239], a[132:135], a[196:199], v[236:239]\n"
  "s_nop 3\n"
  "v_cvt_pk_bf16_f32 v228, v228, v229\n"
  "v_cvt_pk_bf16_f32 v229, v230, v231\n"
  "v_mfma_f32_16x16x32_bf16 v[240:243], a[136:139], a[192:195], v[240:243]\n"
  "v_mfma_f32_16x16x32_bf16 v[240:243], a[140:143], a[196:199], v[240:243]\n"
  "v_cvt_pk_bf16_f32 v230, v232, v233\n"
  "v_cvt_pk_bf16_f32 v231, v234, v235\n"
  "v_mfma_f32_16x16x32_bf16 v[244:247], a[144:147], a[192:195], v[244:247]\n"
  "v_pk_max_i16 v228, v228, 0\n"
  "v_pk_max_i16 v229, v229, 0\n"
  "v_mfma_f32_16x16x32_bf16 v[244:247], a[148:151], a[196:199], v[244:247]\n"
  "v_pk_max_i16 v230, v230, 0\n"
  "v_pk_max_i16 v231, v231, 0\n"
  "v_mfma_f32_16x16x32_bf16 v[248:251], a[152:155], a[192:195], v[248:251]\n"
  "buffer_store_dwordx4 v[228:231], v3, s[28:31], s33 offen offset:2048 sc1\n"
  "v_mfma_f32_16x16x32_bf16 v[248:251], a[156:159], a[196:199], v[248:251]\n"
  "v_cvt_pk_bf16_f32 v236, v236, v237\n"
  "v_cvt_pk_bf16_f32 v237, v238, v239\n"
  "v_mfma_f32_16x16x32_bf16 v[252:255], a[160:163], a[192:195], v[252:255]\n"
  "v_cvt_pk_bf16_f32 v238, v240, v241\n"
  "v_cvt_pk_bf16_f32 v239, v242, v243\n"
  "v_mfma_f32_16x16x32_bf16 v[252:255], a[164:167], a[196:199], v[252:255]\n"
  "v_pk_max_i16 v236, v236, 0\n"
  "v_pk_max_i16 v237, v237, 0\n"
  "v_pk_max_i16 v238, v238, 0\n"
  "v_pk_max_i16 v239, v239, 0\n"
  "buffer_store_dwordx4 v[236:239], v3, s[28:31], s34 offen offset:2048 sc1\n"
  "s_nop 2\n"
  "v_cvt_pk_bf16_f32 v255, v254, v255\n"
  "v_cvt_pk_bf16_f32 v254, v252, v253\n"
  "v_cvt_pk_bf16_f32 v252, v224, v225\n"
  "v_cvt_pk_bf16_f32 v253, v226, v227\n"
  "v_pk_max_i16 v254, v254, 0\n"
  "v_pk_max_i16 v252, v252, 0\n"
  "v_pk_max_i16 v255, v255, 0\n"
  "v_pk_max_i16 v253, v253, 0\n"
  "v_cvt_pk_bf16_f32 v244, v244, v245\n"
  "v_cvt_pk_bf16_f32 v245, v246, v247\n"
  "v_cvt_pk_bf16_f32 v246, v248, v249\n"
  "v_cvt_pk_bf16_f32 v247, v250, v251\n"
  "v_pk_max_i16 v244, v244, 0\n"
  "v_pk_max_i16 v245, v245, 0\n"
  "v_pk_max_i16 v246, v246, 0\n"
  "v_pk_max_i16 v247, v247, 0\n"
  "buffer_store_dwordx4 v[244:247], v3, s[28:31], s35 offen offset:2048 sc1\n"
  "v_permlane16_swap_b32 v252, v254\n"
  "v_permlane16_swap_b32 v253, v255\n"
  "buffer_store_dwordx4 v[252:255], v7, s[28:31], s27 offen sc1\n"
  "v_dot2_f32_bf16 v72, v32, v64, 0\n"
  "v_dot2_f32_bf16 v73, v40, v64, 0\n"
  "v_dot2_f32_bf16 v74, v48, v64, 0\n"
  "v_dot2_f32_bf16 v75, v56, v64, 0\n"
  "v_dot2_f32_bf16 v72, v33, v65, v72\n"
  "v_dot2_f32_bf16 v73, v41, v65, v73\n"
  "v_dot2_f32_bf16 v74, v49, v65, v74\n"
  "v_dot2_f32_bf16 v75, v57, v65, v75\n"
  "v_dot2_f32_bf16 v72, v34, v66, v72\n"
  "v_dot2_f32_bf16 v73, v42, v66, v73\n"
  "v_dot2_f32_bf16 v74, v50, v66, v74\n"
  "v_dot2_f32_bf16 v75, v58, v66, v75\n"
  "v_dot2_f32_bf16 v72, v35, v67, v72\n"
  "v_dot2_f32_bf16 v73, v43, v67, v73\n"
  "v_dot2_f32_bf16 v74, v51, v67, v74\n"
  "v_dot2_f32_bf16 v75, v59, v67, v75\n"
  "v_dot2_f32_bf16 v72, v36, v68, v72\n"
  "v_dot2_f32_bf16 v73, v44, v68, v73\n"
  "v_dot2_f32_bf16 v74, v52, v68, v74\n"
  "v_dot2_f32_bf16 v75, v60, v68, v75\n"
  "v_dot2_f32_bf16 v72, v37, v69, v72\n"
  "v_dot2_f32_bf16 v73, v45, v69, v73\n"
  "v_dot2_f32_bf16 v74, v53, v69, v74\n"
  "v_dot2_f32_bf16 v75, v61, v69, v75\n"
  "v_dot2_f32_bf16 v72, v38, v70, v72\n"
  "v_dot2_f32_bf16 v73, v46, v70, v73\n"
  "v_dot2_f32_bf16 v74, v54, v70, v74\n"
  "v_dot2_f32_bf16 v75, v62, v70, v75\n"
  "v_dot2_f32_bf16 v72, v39, v71, v72\n"
  "v_dot2_f32_bf16 v73, v47, v71, v73\n"
  "v_dot2_f32_bf16 v74, v55, v71, v74\n"
  "v_dot2_f32_bf16 v75, v63, v71, v75\n"
  "v_lshrrev_b32 v76, 4, v28\n"
  "v_lshlrev_b32 v76, 2, v76\n"
  "v_permlane32_swap_b32 v72, v74\n"
  "v_permlane32_swap_b32 v73, v75\n"
  "v_add_f32 v72, v72, v74\n"
  "v_add_f32 v73, v73, v75\n"
  "s_nop 1\n"
  "v_permlane16_swap_b32 v72, v73\n"
  "v_add_f32 v72, v72, v73\n"
  "s_nop 1\n"
  "v_add_f32_dpp v72, v72, v72 row_shr:1 row_mask:0xf bank_mask:0xf bound_ctrl:0\n"
  "s_nop 1\n"
  "v_add_f32_dpp v72, v72, v72 row_shr:2 row_mask:0xf bank_mask:0xf bound_ctrl:0\n"
  "s_nop 1\n"
  "v_add_f32_dpp v72, v72, v72 row_shr:4 row_mask:0xf bank_mask:0xf bound_ctrl:0\n"
  "s_nop 1\n"
  "v_add_f32_dpp v72, v72, v72 row_shr:8 row_mask:0xf bank_mask:0xf bound_ctrl:0\n"
  "v_mul_f32 v72, 0xbfb8aa3b, v72\n"
  "v_exp_f32 v72, v72\n"
  "s_nop 0\n"
  "v_add_f32 v72, 1.0, v72\n"
  "v_rcp_f32 v72, v72\n"
  "s_mov_b32 exec_lo, 0x80008000\n"
  "s_mov_b32 exec_hi, 0x80008000\n"
  "global_store_dword v76, v72, s[80:81] sc1\n"
  "s_mov_b64 exec, -1\n"
  "v_accvgpr_read_b32 v16, a200\n"
  "v_accvgpr_read_b32 v17, a201\n"
  "v_mov_b32 v13, 0\n"
  "v_readfirstlane_b32 s10, v16\n"
  "v_readfirstlane_b32 s11, v17\n"
  "s_add_u32 s10, s10, 1\n"
  "s_addc_u32 s11, s11, 0\n"
  "v_mov_b32 v10, s10\n"
  "v_mov_b32 v11, s11\n"
  "s_barrier\n"
  "K1B_CODE_END:\n"
  "s_branch K2_CODE_START\n"
  "K1_TYPEA_%=:\n"
  "v_readfirstlane_b32 s59, v0\n"
  "v_and_b32 v28, 63, v0\n"
  "v_lshrrev_b32 v29, 3, v28\n"
  "v_lshrrev_b32 v30, 4, v28\n"
  "s_lshr_b32 s59, s59, 6\n"
  "s_and_b32 s98, s38, 4\n"
  "s_lshr_b32 s69, s38, 6\n"
  "s_add_u32 s98, s98, s69\n"
  "s_and_b32 s99, s38, 3\n"
  "s_lshl_b32 s99, s99, 2\n"
  "s_bfe_u32 s69, s38, 0x20003\n"
  "s_add_u32 s99, s99, s69\n"
  "v_bfe_u32 v31, v28, 1, 2\n"
  "v_and_b32 v25, 1, v30\n"
  "v_xor_b32 v31, v31, v25\n"
  "v_lshlrev_b32 v31, 5, v31\n"
  "v_and_b32 v24, 1, v28\n"
  "v_lshl_or_b32 v31, v24, 4, v31\n"
  "v_mul_u32_u24 v21, 6144, v29\n"
  "v_add_u32 v23, v21, v31\n"
  "v_xor_b32 v22, 64, v31\n"
  "v_add_u32 v22, v21, v22\n"
  "v_add_u32 v9, 4096, v23\n"
  "v_add_u32 v10, 52224, v22\n"
  "v_add_u32 v11, 6144, v23\n"
  "v_add_u32 v12, 54272, v22\n"
  "v_add_u32 v13, 8192, v23\n"
  "v_add_u32 v14, 56320, v22\n"
  "s_lshl_b32 s83, s99, 7\n"
  "s_mul_i32 s82, s59, 98304\n"
  "s_add_u32 s83, s83, s82\n"
  "s_sub_u32 s46, 6295552, s83\n"
  "s_mov_b32 s47, 0x20000\n"
  "s_lshl_b32 s69, s59, 11\n"
  "s_add_u32 s51, s39, 69632\n"
  "s_add_u32 s51, s51, s69\n"
  "s_add_u32 s52, s51, 73728\n"
  "s_add_u32 s51, s51, 24576\n"
  "s_and_b32 s48, s98, 4\n"
  "s_lshl_b32 s48, s48, 8\n"
  "s_mul_i32 s49, s48, 3072\n"
  "v_and_b32 v31, 7, v28\n"
  "v_xor_b32 v31, v31, v30\n"
  "v_lshlrev_b32 v31, 4, v31\n"
  "v_lshl_or_b32 v23, v29, 11, v31\n"
  "v_xor_b32 v22, 64, v23\n"
  "v_add_u32 v0, 4096, v23\n"
  "v_add_u32 v1, 19456, v22\n"
  "v_add_u32 v2, 34816, v23\n"
  "v_add_u32 v3, 50176, v22\n"
  "v_add_u32 v4, 69632, v23\n"
  "v_add_u32 v5, 84992, v22\n"
  "v_add_u32 v6, 100352, v23\n"
  "v_add_u32 v7, 115712, v22\n"
  "s_lshl_b32 s82, s98, 19\n"
  "s_lshl_b32 s69, s59, 17\n"
  "s_add_u32 s82, s82, s69\n"
  "s_mov_b32 s42, 135168\n"
  "s_mov_b32 s55, 135168\n"
  "s_mov_b32 s43, 0x20000\n"
  "s_lshl_b32 s69, s59, 13\n"
  "s_add_u32 s50, s39, s69\n"
  "s_add_u32 s50, s50, 2048\n"
  "s_sub_u32 s83, s83, 4096\n"
  "s_subb_u32 s71, 0, 0\n"
  "s_waitcnt lgkmcnt(0)\n"
  "s_add_u32 s44, s86, s83\n"
  "s_addc_u32 s45, s87, s71\n"
  "s_and_b32 s45, s45, 0xffff\n"
  "s_add_u32 m0, s51, 0\n"
  "s_nop 0\n"
  "buffer_load_dwordx4 v9, s[44:47], s49 offen offset:0 lds\n"
  "buffer_load_dwordx4 v10, s[44:47], s49 offen offset:1024 lds\n"
  "s_add_u32 m0, s51, 8192\n"
  "s_nop 0\n"
  "buffer_load_dwordx4 v11, s[44:47], s49 offen offset:0 lds\n"
  "buffer_load_dwordx4 v12, s[44:47], s49 offen offset:1024 lds\n"
  "s_add_u32 m0, s51, 16384\n"
  "s_nop 0\n"
  "buffer_load_dwordx4 v13, s[44:47], s49 offen offset:0 lds\n"
  "buffer_load_dwordx4 v14, s[44:47], s49 offen offset:1024 lds\n"
  "s_add_u32 s49, s49, 393216\n"
  "s_cmp_eq_u32 s49, 6291456\n"
  "s_cselect_b32 s49, 0, s49\n"
  "s_add_u32 s51, s51, 24576\n"
  "s_cmp_ge_u32 s51, s52\n"
  "s_cselect_b32 s26, 73728, 0\n"
  "s_sub_u32 s51, s51, s26\n"
  "s_sub_u32 s83, s82, 4096\n"
  "s_subb_u32 s71, 0, 0\n"
  "s_add_u32 s40, s84, s83\n"
  "s_addc_u32 s41, s85, s71\n"
  "s_and_b32 s41, s41, 0xffff\n"
  "s_and_b32 s69, s59, 1\n"
  "s_lshl_b32 s71, s69, 6\n"
  "v_xor_b32 v8, s71, v23\n"
  "s_lshl_b32 s71, s69, 10\n"
  "s_add_u32 s57, s39, s71\n"
  "s_lshl_b32 s83, s98, 19\n"
  "s_lshl_b32 s69, s69, 14\n"
  "s_add_u32 s83, s83, s69\n"
  "s_cmp_eq_u32 s98, 0\n"
  "s_cselect_b32 s58, 0, 16384\n"
  "s_cselect_b32 s69, 0, 32768\n"
  "s_sub_u32 s83, s83, s69\n"
  "s_add_u32 s60, s84, s83\n"
  "s_addc_u32 s61, s85, 0\n"
  "s_and_b32 s61, s61, 0xffff\n"
  "s_mov_b32 s62, s58\n"
  "s_mov_b32 s63, 0x20000\n"
  "s_add_u32 m0, s50, 0\n"
  "s_nop 0\n"
  "buffer_load_dwordx4 v0, s[40:43], s48 offen offset:0 lds\n"
  "buffer_load_dwordx4 v1, s[40:43], s48 offen offset:1024 lds\n"
  "buffer_load_dwordx4 v2, s[40:43], s48 offen offset:2048 lds\n"
  "buffer_load_dwordx4 v3, s[40:43], s48 offen offset:3072 lds\n"
  "s_add_u32 m0, s50, 4096\n"
  "s_nop 0\n"
  "buffer_load_dwordx4 v4, s[40:43], s48 offen offset:0 lds\n"
  "buffer_load_dwordx4 v5, s[40:43], s48 offen offset:1024 lds\n"
  "buffer_load_dwordx4 v6, s[40:43], s48 offen offset:2048 lds\n"
  "buffer_load_dwordx4 v7, s[40:43], s48 offen offset:3072 lds\n"
  "s_add_u32 m0, s57, 0\n"
  "s_nop 0\n"
  "buffer_load_dwordx4 v8, s[60:63], s48 offen lds\n"
  "s_add_u32 s48, s48, 128\n"
  "s_and_b32 s48, s48, 2047\n"
  "v_and_b32 v31, 15, v28\n"
  "v_bfe_u32 v24, v28, 1, 3\n"
  "v_xor_b32 v24, v24, v30\n"
  "v_lshlrev_b32 v24, 4, v24\n"
  "v_lshl_or_b32 v24, v31, 7, v24\n"
  "s_lshr_b32 s69, s59, 1\n"
  "s_lshl_b32 s69, s69, 14\n"
  "s_add_u32 s69, s69, s39\n"
  "v_add_u32 v16, s69, v24\n"
  "v_xor_b32 v17, 64, v16\n"
  "v_bfe_u32 v24, v28, 2, 2\n"
  "v_lshl_add_u32 v24, v30, 3, v24\n"
  "v_and_b32 v23, 3, v28\n"
  "v_lshlrev_b32 v23, 3, v23\n"
  "v_bfe_u32 v22, v28, 3, 2\n"
  "v_lshl_or_b32 v22, v22, 5, v23\n"
  "v_lshl_or_b32 v22, v24, 7, v22\n"
  "s_and_b32 s69, s59, 1\n"
  "s_add_u32 s71, s39, 94208\n"
  "s_lshl_b32 s82, s69, 6\n"
  "v_xor_b32 v22, s82, v22\n"
  "v_add_u32 v18, s71, v22\n"
  "v_xor_b32 v19, 32, v18\n"
  "s_mov_b32 s70, 4294918144\n"
  "s_mov_b32 s56, 1\n"
  "s_waitcnt vmcnt(9)\n"
  "s_barrier\n"
  "ds_read_b64_tr_b16 a[0:1], v18 offset:0\n"
  "ds_read_b64_tr_b16 a[2:3], v18 offset:512\n"
  "ds_read_b64_tr_b16 a[8:9], v19 offset:0\n"
  "ds_read_b64_tr_b16 a[10:11], v19 offset:512\n"
  "ds_read_b64_tr_b16 a[16:17], v18 offset:8192\n"
  "ds_read_b64_tr_b16 a[18:19], v18 offset:8704\n"
  "ds_read_b64_tr_b16 a[24:25], v19 offset:8192\n"
  "ds_read_b64_tr_b16 a[26:27], v19 offset:8704\n"
  "ds_read_b64_tr_b16 a[32:33], v18 offset:16384\n"
  "ds_read_b64_tr_b16 a[34:35], v18 offset:16896\n"
  "ds_read_b64_tr_b16 a[40:41], v19 offset:16384\n"
  "ds_read_b64_tr_b16 a[42:43], v19 offset:16896\n"
  "ds_read_b64_tr_b16 a[4:5], v18 offset:4096\n"
  "ds_read_b64_tr_b16 a[6:7], v18 offset:4608\n"
  "ds_read_b64_tr_b16 a[12:13], v19 offset:4096\n"
  "ds_read_b64_tr_b16 a[14:15], v19 offset:4608\n"
  "ds_read_b64_tr_b16 a[20:21], v18 offset:12288\n"
  "ds_read_b64_tr_b16 a[22:23], v18 offset:12800\n"
  "ds_read_b64_tr_b16 a[28:29], v19 offset:12288\n"
  "ds_read_b64_tr_b16 a[30:31], v19 offset:12800\n"
  "ds_read_b64_tr_b16 a[36:37], v18 offset:20480\n"
  "ds_read_b64_tr_b16 a[38:39], v18 offset:20992\n"
  "ds_read_b64_tr_b16 a[44:45], v19 offset:20480\n"
  "ds_read_b64_tr_b16 a[46:47], v19 offset:20992\n"
  "s_add_u32 m0, s51, 0\n"
  "s_nop 0\n"
  "buffer_load_dwordx4 v9, s[44:47], s49 offen offset:0 lds\n"
  "buffer_load_dwordx4 v10, s[44:47], s49 offen offset:1024 lds\n"
  "s_add_u32 m0, s51, 8192\n"
  "s_nop 0\n"
  "buffer_load_dwordx4 v11, s[44:47], s49 offen offset:0 lds\n"
  "buffer_load_dwordx4 v12, s[44:47], s49 offen offset:1024 lds\n"
  "s_add_u32 m0, s51, 16384\n"
  "s_nop 0\n"
  "buffer_load_dwordx4 v13, s[44:47], s49 offen offset:0 lds\n"
  "buffer_load_dwordx4 v14, s[44:47], s49 offen offset:1024 lds\n"
  "s_add_u32 s49, s49, 393216\n"
  "s_cmp_eq_u32 s49, 6291456\n"
  "s_cselect_b32 s49, 0, s49\n"
  "s_add_u32 s51, s51, 24576\n"
  "s_cmp_ge_u32 s51, s52\n"
  "s_cselect_b32 s26, 73728, 0\n"
  "s_sub_u32 s51, s51, s26\n"
  "s_cmp_eq_u32 s56, 0\n"
  "s_cselect_b32 s53, s70, 24576\n"
  "s_cmp_eq_u32 s56, 0\n"
  "s_cselect_b32 s56, 3, s56\n"
  "s_sub_u32 s56, s56, 1\n"
  "s_waitcnt vmcnt(6) lgkmcnt(0)\n"
  "s_barrier\n"
  "ds_read_b128 a[144:147], v16 offset:0\n"
  "ds_read_b128 a[148:151], v17 offset:0\n"
  "ds_read_b128 a[152:155], v16 offset:2048\n"
  "ds_read_b128 a[156:159], v17 offset:2048\n"
  "v_add_u32 v18, s53, v18\n"
  "v_add_u32 v19, s53, v19\n"
  "s_waitcnt lgkmcnt(2)\n"
  "v_mfma_f32_16x16x32_bf16 v[32:35], a[16:19], a[144:147], 0\n"
  "ds_read_b128 a[160:163], v16 offset:4096\n"
  "v_mfma_f32_16x16x32_bf16 v[36:39], a[24:27], a[144:147], 0\n"
  "ds_read_b128 a[164:167], v17 offset:4096\n"
  "s_add_u32 m0, s50, 34816\n"
  "s_lshl_b32 s69, s38, 2\n"
  "s_add_u32 s69, s69, s59\n"
  "s_lshl_b32 s71, s69, 12\n"
  "v_mfma_f32_16x16x32_bf16 v[40:43], a[144:147], a[32:35], 0\n"
  "buffer_load_dwordx4 v0, s[40:43], s48 offen offset:0 lds\n"
  "s_add_u32 s72, s84, s71\n"
  "s_addc_u32 s73, s85, 0\n"
  "s_and_b32 s73, s73, 0xffff\n"
  "v_mfma_f32_16x16x32_bf16 v[44:47], a[144:147], a[40:43], 0\n"
  "buffer_load_dwordx4 v1, s[40:43], s48 offen offset:1024 lds\n"
  "s_mov_b32 s74, 4096\n"
  "s_mov_b32 s75, 0x20000\n"
  "v_lshlrev_b32 v26, 6, v28\n"
  "v_mfma_f32_16x16x32_bf16 v[32:35], a[20:23], a[148:151], v[32:35]\n"
  "s_lshr_b32 s69, s59, 1\n"
  "s_lshl_b32 s71, s98, 1\n"
  "v_mfma_f32_16x16x32_bf16 v[36:39], a[28:31], a[148:151], v[36:39]\n"
  "buffer_load_dwordx4 v2, s[40:43], s48 offen offset:2048 lds\n"
  "s_add_u32 s69, s69, s71\n"
  "s_mul_i32 s69, s69, 1179648\n"
  "s_add_u32 s28, s92, s69\n"
  "s_addc_u32 s29, s93, 0\n"
  "v_mfma_f32_16x16x32_bf16 v[40:43], a[148:151], a[36:39], v[40:43]\n"
  "s_and_b32 s29, s29, 0xffff\n"
  "s_mov_b32 s30, 1179648\n"
  "v_mfma_f32_16x16x32_bf16 v[44:47], a[148:151], a[44:47], v[44:47]\n"
  "buffer_load_dwordx4 v3, s[40:43], s48 offen offset:3072 lds\n"
  "s_mov_b32 s31, 0x20000\n"
  "s_mov_b32 s69, s99\n"
  "s_and_b32 s71, s59, 1\n"
  "s_waitcnt lgkmcnt(2)\n"
  "v_mfma_f32_16x16x32_bf16 v[48:51], a[0:3], a[152:155], 0\n"
  "ds_read_b128 a[144:147], v16 offset:6144\n"
  "s_lshl_b32 s33, s69, 1\n"
  "s_add_u32 s33, s33, s71\n"
  "v_mfma_f32_16x16x32_bf16 v[52:55], a[8:11], a[152:155], 0\n"
  "ds_read_b128 a[148:151], v17 offset:6144\n"
  "s_add_u32 m0, s50, 38912\n"
  "s_lshr_b32 s83, s33, 1\n"
  "s_lshl_b32 s83, s83, 12\n"
  "s_and_b32 s33, s33, 1\n"
  "v_mfma_f32_16x16x32_bf16 v[56:59], a[16:19], a[152:155], 0\n"
  "buffer_load_dwordx4 v4, s[40:43], s48 offen offset:0 lds\n"
  "s_lshl_b32 s33, s33, 10\n"
  "s_or_b32 s33, s33, s83\n"
  "v_mfma_f32_16x16x32_bf16 v[60:63], a[24:27], a[152:155], 0\n"
  "buffer_load_dwordx4 v5, s[40:43], s48 offen offset:1024 lds\n"
  "s_add_u32 s34, s33, 294912\n"
  "s_add_u32 s35, s33, 589824\n"
  "s_add_u32 s27, s33, 884736\n"
  "v_mfma_f32_16x16x32_bf16 v[64:67], a[152:155], a[32:35], 0\n"
  "s_lshl_b32 s69, s98, 8\n"
  "s_lshr_b32 s71, s99, 2\n"
  "v_mfma_f32_16x16x32_bf16 v[68:71], a[152:155], a[40:43], 0\n"
  "buffer_load_dwordx4 v6, s[40:43], s48 offen offset:2048 lds\n"
  "s_lshl_b32 s71, s71, 6\n"
  "s_add_u32 s69, s69, s71\n"
  "s_and_b32 s71, s99, 3\n"
  "v_mfma_f32_16x16x32_bf16 v[48:51], a[4:7], a[156:159], v[48:51]\n"
  "s_lshl_b32 s71, s71, 3\n"
  "s_add_u32 s69, s69, s71\n"
  "v_mfma_f32_16x16x32_bf16 v[52:55], a[12:15], a[156:159], v[52:55]\n"
  "buffer_load_dwordx4 v7, s[40:43], s48 offen offset:3072 lds\n"
  "s_add_u32 s69, s69, 21299200\n"
  "s_add_u32 s12, s92, s69\n"
  "s_addc_u32 s13, s93, 0\n"
  "s_add_u32 s14, s92, 21561344\n"
  "s_addc_u32 s15, s93, 0\n"
  "v_mfma_f32_16x16x32_bf16 v[56:59], a[20:23], a[156:159], v[56:59]\n"
  "s_add_u32 m0, s57, 34816\n"
  "v_mov_b32 v25, 0\n"
  "s_lshl_b32 s69, s38, 2\n"
  "v_mfma_f32_16x16x32_bf16 v[60:63], a[28:31], a[156:159], v[60:63]\n"
  "buffer_load_dwordx4 v8, s[60:63], s48 offen lds\n"
  "s_add_u32 s69, s69, s59\n"
  "s_lshl_b32 s69, s69, 8\n"
  "s_add_u32 s69, s69, 21037056\n"
  "v_mfma_f32_16x16x32_bf16 v[64:67], a[156:159], a[36:39], v[64:67]\n"
  "s_add_u32 s48, s48, 128\n"
  "s_add_u32 s86, s92, s69\n"
  "s_addc_u32 s87, s93, 0\n"
  "v_and_b32 v30, 0xc0, v26\n"
  "v_mfma_f32_16x16x32_bf16 v[68:71], a[156:159], a[44:47], v[68:71]\n"
  "s_and_b32 s48, s48, 2047\n"
  "s_lshl_b32 s69, s38, 15\n"
  "s_sub_u32 s71, s38, 64\n"
  "s_mul_i32 s71, s71, 38272\n"
  "s_waitcnt lgkmcnt(2)\n"
  "v_mfma_f32_16x16x32_bf16 v[72:75], a[0:3], a[160:163], 0\n"
  "ds_read_b128 a[152:155], v16 offset:8192\n"
  "s_sub_u32 s82, 7340032, s71\n"
  "s_min_u32 s82, s82, 38272\n"
  "v_mfma_f32_16x16x32_bf16 v[76:79], a[8:11], a[160:163], 0\n"
  "ds_read_b128 a[156:159], v17 offset:8192\n"
  "s_add_u32 m0, s51, 0\n"
  "s_cmp_lt_u32 s38, 64\n"
  "s_cselect_b32 s69, s69, s71\n"
  "s_cselect_b32 s66, 32768, s82\n"
  "s_cselect_b64 s[64:65], s[94:95], s[96:97]\n"
  "s_add_u32 s64, s64, s69\n"
  "s_addc_u32 s65, s65, 0\n"
  "s_and_b32 s65, s65, 0xffff\n"
  "v_mfma_f32_16x16x32_bf16 v[80:83], a[16:19], a[160:163], 0\n"
  "buffer_load_dwordx4 v9, s[44:47], s49 offen offset:0 lds\n"
  "s_mov_b32 s67, 0x20000\n"
  "s_mul_i32 s68, s59, 1536\n"
  "v_mfma_f32_16x16x32_bf16 v[84:87], a[24:27], a[160:163], 0\n"
  "buffer_load_dwordx4 v10, s[44:47], s49 offen offset:1024 lds\n"
  "s_add_u32 s68, s68, 67584\n"
  "v_lshlrev_b32 v15, 7, v28\n"
  "v_mov_b32 v31, 0x40000000\n"
  "v_mfma_f32_16x16x32_bf16 v[88:91], a[160:163], a[32:35], 0\n"
  "s_add_u32 m0, s51, 8192\n"
  "v_cmp_gt_u32 vcc, 12, v28\n"
  "s_nop 1\n"
  "v_cndmask_b32 v15, v31, v15, vcc\n"
  "v_mfma_f32_16x16x32_bf16 v[92:95], a[160:163], a[40:43], 0\n"
  "buffer_load_dwordx4 v11, s[44:47], s49 offen offset:0 lds\n"
  "v_mfma_f32_16x16x32_bf16 v[72:75], a[4:7], a[164:167], v[72:75]\n"
  "buffer_load_dwordx4 v12, s[44:47], s49 offen offset:1024 lds\n"
  "v_mfma_f32_16x16x32_bf16 v[76:79], a[12:15], a[164:167], v[76:79]\n"
  "s_add_u32 m0, s51, 16384\n"
  "v_mfma_f32_16x16x32_bf16 v[80:83], a[20:23], a[164:167], v[80:83]\n"
  "buffer_load_dwordx4 v13, s[44:47], s49 offen offset:0 lds\n"
  "v_mfma_f32_16x16x32_bf16 v[84:87], a[28:31], a[164:167], v[84:87]\n"
  "v_mfma_f32_16x16x32_bf16 v[88:91], a[164:167], a[36:39], v[88:91]\n"
  "buffer_load_dwordx4 v14, s[44:47], s49 offen offset:1024 lds\n"
  "v_mfma_f32_16x16x32_bf16 v[92:95], a[164:167], a[44:47], v[92:95]\n"
  "s_add_u32 s49, s49, 393216\n"
  "s_cmp_eq_u32 s49, 6291456\n"
  "s_cselect_b32 s49, 0, s49\n"
  "s_waitcnt lgkmcnt(2)\n"
  "v_mfma_f32_16x16x32_bf16 v[96:99], a[0:3], a[144:147], 0\n"
  "ds_read_b128 a[160:163], v16 offset:10240\n"
  "s_add_u32 s51, s51, 24576\n"
  "v_mfma_f32_16x16x32_bf16 v[100:103], a[8:11], a[144:147], 0\n"
  "ds_read_b128 a[164:167], v17 offset:10240\n"
  "s_cmp_ge_u32 s51, s52\n"
  "s_cselect_b32 s26, 73728, 0\n"
  "v_mfma_f32_16x16x32_bf16 v[104:107], a[16:19], a[144:147], 0\n"
  "s_sub_u32 s51, s51, s26\n"
  "v_mfma_f32_16x16x32_bf16 v[108:111], a[24:27], a[144:147], 0\n"
  "global_load_dwordx2 a[200:201], v25, s[14:15] sc1\n"
  "v_mfma_f32_16x16x32_bf16 v[112:115], a[144:147], a[32:35], 0\n"
  "global_load_dword v27, v30, s[86:87]\n"
  "v_mfma_f32_16x16x32_bf16 v[116:119], a[144:147], a[40:43], 0\n"
  "global_load_dword v27, v25, s[12:13]\n"
  "v_mfma_f32_16x16x32_bf16 v[96:99], a[4:7], a[148:151], v[96:99]\n"
  "v_mfma_f32_16x16x32_bf16 v[100:103], a[12:15], a[148:151], v[100:103]\n"
  "s_add_u32 m0, s51, 0\n"
  "v_mfma_f32_16x16x32_bf16 v[104:107], a[20:23], a[148:151], v[104:107]\n"
  "buffer_load_dwordx4 v9, s[44:47], s49 offen offset:0 lds\n"
  "v_mfma_f32_16x16x32_bf16 v[108:111], a[28:31], a[148:151], v[108:111]\n"
  "v_mfma_f32_16x16x32_bf16 v[112:115], a[148:151], a[36:39], v[112:115]\n"
  "v_mfma_f32_16x16x32_bf16 v[116:119], a[148:151], a[44:47], v[116:119]\n"
  "s_waitcnt lgkmcnt(2)\n"
  "v_mfma_f32_16x16x32_bf16 v[120:123], a[0:3], a[152:155], 0\n"
  "ds_read_b128 a[144:147], v16 offset:12288\n"
  "v_mfma_f32_16x16x32_bf16 v[124:127], a[8:11], a[152:155], 0\n"
  "ds_read_b128 a[148:151], v17 offset:12288\n"
  "v_mfma_f32_16x16x32_bf16 v[128:131], a[16:19], a[152:155], 0\n"
  "buffer_load_dwordx4 v10, s[44:47], s49 offen offset:1024 lds\n"
  "v_mfma_f32_16x16x32_bf16 v[132:135], a[24:27], a[152:155], 0\n"
  "v_mfma_f32_16x16x32_bf16 v[136:139], a[152:155], a[32:35], 0\n"
  "v_mfma_f32_16x16x32_bf16 v[140:143], a[152:155], a[40:43], 0\n"
  "v_mfma_f32_16x16x32_bf16 v[120:123], a[4:7], a[156:159], v[120:123]\n"
  "v_mfma_f32_16x16x32_bf16 v[124:127], a[12:15], a[156:159], v[124:127]\n"
  "s_add_u32 m0, s51, 8192\n"
  "v_mfma_f32_16x16x32_bf16 v[128:131], a[20:23], a[156:159], v[128:131]\n"
  "buffer_load_dwordx4 v11, s[44:47], s49 offen offset:0 lds\n"
  "v_mfma_f32_16x16x32_bf16 v[132:135], a[28:31], a[156:159], v[132:135]\n"
  "v_mfma_f32_16x16x32_bf16 v[136:139], a[156:159], a[36:39], v[136:139]\n"
  "v_mfma_f32_16x16x32_bf16 v[140:143], a[156:159], a[44:47], v[140:143]\n"
  "s_waitcnt vmcnt(21)\n"
  "s_barrier\n"
  "s_waitcnt lgkmcnt(2)\n"
  "v_mfma_f32_16x16x32_bf16 v[144:147], a[0:3], a[160:163], 0\n"
  "ds_read_b128 a[152:155], v16 offset:14336\n"
  "v_mfma_f32_16x16x32_bf16 v[148:151], a[8:11], a[160:163], 0\n"
  "ds_read_b128 a[156:159], v17 offset:14336\n"
  "v_mfma_f32_16x16x32_bf16 v[152:155], a[16:19], a[160:163], 0\n"
  "buffer_load_dwordx4 v12, s[44:47], s49 offen offset:1024 lds\n"
  "v_mfma_f32_16x16x32_bf16 v[156:159], a[24:27], a[160:163], 0\n"
  "ds_read_b64_tr_b16 a[48:49], v18 offset:0\n"
  "v_mfma_f32_16x16x32_bf16 v[160:163], a[160:163], a[32:35], 0\n"
  "ds_read_b64_tr_b16 a[50:51], v18 offset:512\n"
  "v_mfma_f32_16x16x32_bf16 v[164:167], a[160:163], a[40:43], 0\n"
  "ds_read_b64_tr_b16 a[56:57], v19 offset:0\n"
  "v_mfma_f32_16x16x32_bf16 v[144:147], a[4:7], a[164:167], v[144:147]\n"
  "ds_read_b64_tr_b16 a[58:59], v19 offset:512\n"
  "v_mfma_f32_16x16x32_bf16 v[148:151], a[12:15], a[164:167], v[148:151]\n"
  "s_add_u32 m0, s51, 16384\n"
  "ds_read_b64_tr_b16 a[64:65], v18 offset:8192\n"
  "v_mfma_f32_16x16x32_bf16 v[152:155], a[20:23], a[164:167], v[152:155]\n"
  "buffer_load_dwordx4 v13, s[44:47], s49 offen offset:0 lds\n"
  "v_mfma_f32_16x16x32_bf16 v[156:159], a[28:31], a[164:167], v[156:159]\n"
  "ds_read_b64_tr_b16 a[66:67], v18 offset:8704\n"
  "v_mfma_f32_16x16x32_bf16 v[160:163], a[164:167], a[36:39], v[160:163]\n"
  "ds_read_b64_tr_b16 a[72:73], v19 offset:8192\n"
  "v_mfma_f32_16x16x32_bf16 v[164:167], a[164:167], a[44:47], v[164:167]\n"
  "ds_read_b64_tr_b16 a[74:75], v19 offset:8704\n"
  "s_waitcnt lgkmcnt(10)\n"
  "v_mfma_f32_16x16x32_bf16 v[168:171], a[0:3], a[144:147], 0\n"
  "ds_read_b128 a[160:163], v16 offset:16384\n"
  "v_mfma_f32_16x16x32_bf16 v[172:175], a[8:11], a[144:147], 0\n"
  "ds_read_b128 a[164:167], v17 offset:16384\n"
  "v_mfma_f32_16x16x32_bf16 v[176:179], a[16:19], a[144:147], 0\n"
  "buffer_load_dwordx4 v14, s[44:47], s49 offen offset:1024 lds\n"
  "v_mfma_f32_16x16x32_bf16 v[180:183], a[24:27], a[144:147], 0\n"
  "s_add_u32 s49, s49, 393216\n"
  "ds_read_b64_tr_b16 a[80:81], v18 offset:16384\n"
  "v_mfma_f32_16x16x32_bf16 v[184:187], a[144:147], a[32:35], 0\n"
  "s_cmp_eq_u32 s49, 6291456\n"
  "s_cselect_b32 s49, 0, s49\n"
  "ds_read_b64_tr_b16 a[82:83], v18 offset:16896\n"
  "v_mfma_f32_16x16x32_bf16 v[188:191], a[144:147], a[40:43], 0\n"
  "ds_read_b64_tr_b16 a[88:89], v19 offset:16384\n"
  "v_mfma_f32_16x16x32_bf16 v[168:171], a[4:7], a[148:151], v[168:171]\n"
  "s_add_u32 s51, s51, 24576\n"
  "ds_read_b64_tr_b16 a[90:91], v19 offset:16896\n"
  "v_mfma_f32_16x16x32_bf16 v[172:175], a[12:15], a[148:151], v[172:175]\n"
  "ds_read_b64_tr_b16 a[52:53], v18 offset:4096\n"
  "v_mfma_f32_16x16x32_bf16 v[176:179], a[20:23], a[148:151], v[176:179]\n"
  "s_cmp_ge_u32 s51, s52\n"
  "s_cselect_b32 s26, 73728, 0\n"
  "ds_read_b64_tr_b16 a[54:55], v18 offset:4608\n"
  "v_mfma_f32_16x16x32_bf16 v[180:183], a[28:31], a[148:151], v[180:183]\n"
  "ds_read_b64_tr_b16 a[60:61], v19 offset:4096\n"
  "ds_read_b64_tr_b16 a[62:63], v19 offset:4608\n"
  "ds_read_b64_tr_b16 a[68:69], v18 offset:12288\n"
  "ds_read_b64_tr_b16 a[70:71], v18 offset:12800\n"
  "ds_read_b64_tr_b16 a[76:77], v19 offset:12288\n"
  "ds_read_b64_tr_b16 a[78:79], v19 offset:12800\n"
  "ds_read_b64_tr_b16 a[84:85], v18 offset:20480\n"
  "ds_read_b64_tr_b16 a[86:87], v18 offset:20992\n"
  "ds_read_b64_tr_b16 a[92:93], v19 offset:20480\n"
  "ds_read_b64_tr_b16 a[94:95], v19 offset:20992\n"
  "v_mfma_f32_16x16x32_bf16 v[184:187], a[148:151], a[36:39], v[184:187]\n"
  "s_sub_u32 s51, s51, s26\n"
  "v_mfma_f32_16x16x32_bf16 v[188:191], a[148:151], a[44:47], v[188:191]\n"
  "s_waitcnt vmcnt(9) lgkmcnt(0)\n"
  "s_barrier\n"
  "v_mfma_f32_16x16x32_bf16 v[192:195], a[0:3], a[152:155], 0\n"
  "ds_read_b128 a[144:147], v16 offset:34816\n"
  "v_mfma_f32_16x16x32_bf16 v[196:199], a[8:11], a[152:155], 0\n"
  "ds_read_b128 a[148:151], v17 offset:34816\n"
  "v_mfma_f32_16x16x32_bf16 v[200:203], a[16:19], a[152:155], 0\n"
  "s_add_u32 m0, s50, 0\n"
  "v_mfma_f32_16x16x32_bf16 v[204:207], a[24:27], a[152:155], 0\n"
  "buffer_load_dwordx4 v0, s[40:43], s48 offen offset:0 lds\n"
  "v_mfma_f32_16x16x32_bf16 v[208:211], a[152:155], a[32:35], 0\n"
  "s_cmp_eq_u32 s56, 0\n"
  "s_cselect_b32 s53, s70, 24576\n"
  "v_mfma_f32_16x16x32_bf16 v[212:215], a[152:155], a[40:43], 0\n"
  "v_mfma_f32_16x16x32_bf16 v[192:195], a[4:7], a[156:159], v[192:195]\n"
  "buffer_load_dwordx4 v1, s[40:43], s48 offen offset:1024 lds\n"
  "v_mfma_f32_16x16x32_bf16 v[196:199], a[12:15], a[156:159], v[196:199]\n"
  "s_cmp_eq_u32 s56, 0\n"
  "s_cselect_b32 s56, 3, s56\n"
  "v_mfma_f32_16x16x32_bf16 v[200:203], a[20:23], a[156:159], v[200:203]\n"
  "s_sub_u32 s56, s56, 1\n"
  "v_mfma_f32_16x16x32_bf16 v[204:207], a[28:31], a[156:159], v[204:207]\n"
  "buffer_load_dwordx4 v2, s[40:43], s48 offen offset:2048 lds\n"
  "v_mfma_f32_16x16x32_bf16 v[208:211], a[156:159], a[36:39], v[208:211]\n"
  "v_add_u32 v18, s53, v18\n"
  "v_mfma_f32_16x16x32_bf16 v[212:215], a[156:159], a[44:47], v[212:215]\n"
  "v_mfma_f32_16x16x32_bf16 v[216:219], a[0:3], a[160:163], 0\n"
  "ds_read_b128 a[152:155], v16 offset:36864\n"
  "v_mfma_f32_16x16x32_bf16 v[220:223], a[8:11], a[160:163], 0\n"
  "ds_read_b128 a[156:159], v17 offset:36864\n"
  "v_mfma_f32_16x16x32_bf16 v[224:227], a[16:19], a[160:163], 0\n"
  "buffer_load_dwordx4 v3, s[40:43], s48 offen offset:3072 lds\n"
  "v_mfma_f32_16x16x32_bf16 v[228:231], a[24:27], a[160:163], 0\n"
  "s_add_u32 m0, s50, 4096\n"
  "v_mfma_f32_16x16x32_bf16 v[232:235], a[160:163], a[32:35], 0\n"
  "buffer_load_dwordx4 v4, s[40:43], s48 offen offset:0 lds\n"
  "v_mfma_f32_16x16x32_bf16 v[236:239], a[160:163], a[40:43], 0\n"
  "v_add_u32 v19, s53, v19\n"
  "v_mfma_f32_16x16x32_bf16 v[216:219], a[4:7], a[164:167], v[216:219]\n"
  "v_mfma_f32_16x16x32_bf16 v[220:223], a[12:15], a[164:167], v[220:223]\n"
  "buffer_load_dwordx4 v5, s[40:43], s48 offen offset:1024 lds\n"
  "v_mfma_f32_16x16x32_bf16 v[224:227], a[20:23], a[164:167], v[224:227]\n"
  "v_mfma_f32_16x16x32_bf16 v[228:231], a[28:31], a[164:167], v[228:231]\n"
  "v_mfma_f32_16x16x32_bf16 v[232:235], a[164:167], a[36:39], v[232:235]\n"
  "buffer_load_dwordx4 v6, s[40:43], s48 offen offset:2048 lds\n"
  "v_mfma_f32_16x16x32_bf16 v[236:239], a[164:167], a[44:47], v[236:239]\n"
  "s_getpc_b64 s[82:83]\n"
  "K1A_PC2:\n"
  "s_sub_u32 s82, s82, K1A_PC2-K0_PC\n"
  "s_subb_u32 s83, s83, 0\n"
  "s_lshr_b32 s69, s38, 3\n"
  "v_and_b32 v31, 15, v28\n"
  "v_lshl_or_b32 v31, s69, 4, v31\n"
  "v_lshlrev_b32 v31, 7, v31\n"
  "v_min_u32 v31, K2_CODE_END-K0_PC-4, v31\n"
  "s_mov_b32 s54, 6\n"
  ".p2align 6\n"
  "s_nop 0\n"
  "K1A_LOOP_%=:\n"
  "s_waitcnt lgkmcnt(2)\n"
  "v_mfma_f32_16x16x32_bf16 v[32:35], a[64:67], a[144:147], v[32:35]\n"
  "ds_read_b128 a[160:163], v16 offset:38912\n"
  "v_mfma_f32_16x16x32_bf16 v[36:39], a[72:75], a[144:147], v[36:39]\n"
  "ds_read_b128 a[164:167], v17 offset:38912\n"
  "v_mfma_f32_16x16x32_bf16 v[40:43], a[144:147], a[80:83], v[40:43]\n"
  "buffer_load_dwordx4 v7, s[40:43], s48 offen offset:3072 lds\n"
  "v_mfma_f32_16x16x32_bf16 v[44:47], a[144:147], a[88:91], v[44:47]\n"
  "s_add_u32 m0, s57, 0\n"
  "ds_read_b64_tr_b16 a[0:1], v18 offset:0\n"
  "v_mfma_f32_16x16x32_bf16 v[32:35], a[68:71], a[148:151], v[32:35]\n"
  "buffer_load_dwordx4 v8, s[60:63], s48 offen lds\n"
  "v_mfma_f32_16x16x32_bf16 v[36:39], a[76:79], a[148:151], v[36:39]\n"
  "s_add_u32 s48, s48, 128\n"
  "ds_read_b64_tr_b16 a[2:3], v18 offset:512\n"
  "v_mfma_f32_16x16x32_bf16 v[40:43], a[148:151], a[84:87], v[40:43]\n"
  "s_and_b32 s48, s48, 2047\n"
  "v_mfma_f32_16x16x32_bf16 v[44:47], a[148:151], a[92:95], v[44:47]\n"
  "ds_read_b64_tr_b16 a[8:9], v19 offset:0\n"
  "s_waitcnt lgkmcnt(5)\n"
  "v_mfma_f32_16x16x32_bf16 v[48:51], a[48:51], a[152:155], v[48:51]\n"
  "ds_read_b128 a[144:147], v16 offset:40960\n"
  "v_mfma_f32_16x16x32_bf16 v[52:55], a[56:59], a[152:155], v[52:55]\n"
  "ds_read_b128 a[148:151], v17 offset:40960\n"
  "v_mfma_f32_16x16x32_bf16 v[56:59], a[64:67], a[152:155], v[56:59]\n"
  "s_add_u32 m0, s51, 0\n"
  "ds_read_b64_tr_b16 a[10:11], v19 offset:512\n"
  "v_mfma_f32_16x16x32_bf16 v[60:63], a[72:75], a[152:155], v[60:63]\n"
  "buffer_load_dwordx4 v9, s[44:47], s49 offen offset:0 lds\n"
  "v_mfma_f32_16x16x32_bf16 v[64:67], a[152:155], a[80:83], v[64:67]\n"
  "v_mfma_f32_16x16x32_bf16 v[68:71], a[152:155], a[88:91], v[68:71]\n"
  "buffer_load_dword v27, v15, s[64:67], s68 offen\n"
  "v_mfma_f32_16x16x32_bf16 v[48:51], a[52:55], a[156:159], v[48:51]\n"
  "s_sub_u32 s68, s68, 6144\n"
  "ds_read_b64_tr_b16 a[16:17], v18 offset:8192\n"
  "v_mfma_f32_16x16x32_bf16 v[52:55], a[60:63], a[156:159], v[52:55]\n"
  "v_mfma_f32_16x16x32_bf16 v[56:59], a[68:71], a[156:159], v[56:59]\n"
  "ds_read_b64_tr_b16 a[18:19], v18 offset:8704\n"
  "v_mfma_f32_16x16x32_bf16 v[60:63], a[76:79], a[156:159], v[60:63]\n"
  "global_load_dword v27, v31, s[82:83]\n"
  "v_mfma_f32_16x16x32_bf16 v[64:67], a[156:159], a[84:87], v[64:67]\n"
  "v_mfma_f32_16x16x32_bf16 v[68:71], a[156:159], a[92:95], v[68:71]\n"
  "ds_read_b64_tr_b16 a[24:25], v19 offset:8192\n"
  "s_waitcnt lgkmcnt(9)\n"
  "v_mfma_f32_16x16x32_bf16 v[72:75], a[48:51], a[160:163], v[72:75]\n"
  "ds_read_b128 a[152:155], v16 offset:43008\n"
  "v_mfma_f32_16x16x32_bf16 v[76:79], a[56:59], a[160:163], v[76:79]\n"
  "ds_read_b128 a[156:159], v17 offset:43008\n"
  "v_mfma_f32_16x16x32_bf16 v[80:83], a[64:67], a[160:163], v[80:83]\n"
  "buffer_load_dwordx4 v10, s[44:47], s49 offen offset:1024 lds\n"
  "v_mfma_f32_16x16x32_bf16 v[84:87], a[72:75], a[160:163], v[84:87]\n"
  "ds_read_b64_tr_b16 a[26:27], v19 offset:8704\n"
  "v_mfma_f32_16x16x32_bf16 v[88:91], a[160:163], a[80:83], v[88:91]\n"
  "v_mfma_f32_16x16x32_bf16 v[92:95], a[160:163], a[88:91], v[92:95]\n"
  "ds_read_b64_tr_b16 a[32:33], v18 offset:16384\n"
  "v_mfma_f32_16x16x32_bf16 v[72:75], a[52:55], a[164:167], v[72:75]\n"
  "v_mfma_f32_16x16x32_bf16 v[76:79], a[60:63], a[164:167], v[76:79]\n"
  "ds_read_b64_tr_b16 a[34:35], v18 offset:16896\n"
  "v_mfma_f32_16x16x32_bf16 v[80:83], a[68:71], a[164:167], v[80:83]\n"
  "v_mfma_f32_16x16x32_bf16 v[84:87], a[76:79], a[164:167], v[84:87]\n"
  "v_mfma_f32_16x16x32_bf16 v[88:91], a[164:167], a[84:87], v[88:91]\n"
  "ds_read_b64_tr_b16 a[40:41], v19 offset:16384\n"
  "v_mfma_f32_16x16x32_bf16 v[92:95], a[164:167], a[92:95], v[92:95]\n"
  "s_waitcnt lgkmcnt(10)\n"
  "v_mfma_f32_16x16x32_bf16 v[96:99], a[48:51], a[144:147], v[96:99]\n"
  "ds_read_b128 a[160:163], v16 offset:45056\n"
  "v_mfma_f32_16x16x32_bf16 v[100:103], a[56:59], a[144:147], v[100:103]\n"
  "ds_read_b128 a[164:167], v17 offset:45056\n"
  "s_add_u32 m0, s51, 8192\n"
  "v_mfma_f32_16x16x32_bf16 v[104:107], a[64:67], a[144:147], v[104:107]\n"
  "buffer_load_dwordx4 v11, s[44:47], s49 offen offset:0 lds\n"
  "v_mfma_f32_16x16x32_bf16 v[108:111], a[72:75], a[144:147], v[108:111]\n"
  "ds_read_b64_tr_b16 a[42:43], v19 offset:16896\n"
  "v_mfma_f32_16x16x32_bf16 v[112:115], a[144:147], a[80:83], v[112:115]\n"
  "ds_read_b64_tr_b16 a[4:5], v18 offset:4096\n"
  "v_mfma_f32_16x16x32_bf16 v[116:119], a[144:147], a[88:91], v[116:119]\n"
  "v_mfma_f32_16x16x32_bf16 v[96:99], a[52:55], a[148:151], v[96:99]\n"
  "v_mfma_f32_16x16x32_bf16 v[100:103], a[60:63], a[148:151], v[100:103]\n"
  "ds_read_b64_tr_b16 a[6:7], v18 offset:4608\n"
  "v_mfma_f32_16x16x32_bf16 v[104:107], a[68:71], a[148:151], v[104:107]\n"
  "v_mfma_f32_16x16x32_bf16 v[108:111], a[76:79], a[148:151], v[108:111]\n"
  "v_mfma_f32_16x16x32_bf16 v[112:115], a[148:151], a[84:87], v[112:115]\n"
  "ds_read_b64_tr_b16 a[12:13], v19 offset:4096\n"
  "v_mfma_f32_16x16x32_bf16 v[116:119], a[148:151], a[92:95], v[116:119]\n"
  "s_waitcnt lgkmcnt(10)\n"
  "v_mfma_f32_16x16x32_bf16 v[120:123], a[48:51], a[152:155], v[120:123]\n"
  "ds_read_b128 a[144:147], v16 offset:47104\n"
  "v_mfma_f32_16x16x32_bf16 v[124:127], a[56:59], a[152:155], v[124:127]\n"
  "ds_read_b128 a[148:151], v17 offset:47104\n"
  "v_mfma_f32_16x16x32_bf16 v[128:131], a[64:67], a[152:155], v[128:131]\n"
  "buffer_load_dwordx4 v12, s[44:47], s49 offen offset:1024 lds\n"
  "v_mfma_f32_16x16x32_bf16 v[132:135], a[72:75], a[152:155], v[132:135]\n"
  "ds_read_b64_tr_b16 a[14:15], v19 offset:4608\n"
  "v_mfma_f32_16x16x32_bf16 v[136:139], a[152:155], a[80:83], v[136:139]\n"
  "ds_read_b64_tr_b16 a[20:21], v18 offset:12288\n"
  "v_mfma_f32_16x16x32_bf16 v[140:143], a[152:155], a[88:91], v[140:143]\n"
  "v_mfma_f32_16x16x32_bf16 v[120:123], a[52:55], a[156:159], v[120:123]\n"
  "ds_read_b64_tr_b16 a[22:23], v18 offset:12800\n"
  "v_mfma_f32_16x16x32_bf16 v[124:127], a[60:63], a[156:159], v[124:127]\n"
  "v_mfma_f32_16x16x32_bf16 v[128:131], a[68:71], a[156:159], v[128:131]\n"
  "v_mfma_f32_16x16x32_bf16 v[132:135], a[76:79], a[156:159], v[132:135]\n"
  "ds_read_b64_tr_b16 a[28:29], v19 offset:12288\n"
  "v_mfma_f32_16x16x32_bf16 v[136:139], a[156:159], a[84:87], v[136:139]\n"
  "v_mfma_f32_16x16x32_bf16 v[140:143], a[156:159], a[92:95], v[140:143]\n"
  "s_waitcnt lgkmcnt(10)\n"
  "v_mfma_f32_16x16x32_bf16 v[144:147], a[48:51], a[160:163], v[144:147]\n"
  "ds_read_b128 a[152:155], v16 offset:49152\n"
  "v_mfma_f32_16x16x32_bf16 v[148:151], a[56:59], a[160:163], v[148:151]\n"
  "ds_read_b128 a[156:159], v17 offset:49152\n"
  "s_add_u32 m0, s51, 16384\n"
  "v_mfma_f32_16x16x32_bf16 v[152:155], a[64:67], a[160:163], v[152:155]\n"
  "buffer_load_dwordx4 v13, s[44:47], s49 offen offset:0 lds\n"
  "v_mfma_f32_16x16x32_bf16 v[156:159], a[72:75], a[160:163], v[156:159]\n"
  "ds_read_b64_tr_b16 a[30:31], v19 offset:12800\n"
  "v_mfma_f32_16x16x32_bf16 v[160:163], a[160:163], a[80:83], v[160:163]\n"
  "ds_read_b64_tr_b16 a[36:37], v18 offset:20480\n"
  "v_mfma_f32_16x16x32_bf16 v[164:167], a[160:163], a[88:91], v[164:167]\n"
  "v_mfma_f32_16x16x32_bf16 v[144:147], a[52:55], a[164:167], v[144:147]\n"
  "ds_read_b64_tr_b16 a[38:39], v18 offset:20992\n"
  "v_mfma_f32_16x16x32_bf16 v[148:151], a[60:63], a[164:167], v[148:151]\n"
  "v_mfma_f32_16x16x32_bf16 v[152:155], a[68:71], a[164:167], v[152:155]\n"
  "v_mfma_f32_16x16x32_bf16 v[156:159], a[76:79], a[164:167], v[156:159]\n"
  "ds_read_b64_tr_b16 a[44:45], v19 offset:20480\n"
  "v_mfma_f32_16x16x32_bf16 v[160:163], a[164:167], a[84:87], v[160:163]\n"
  "v_mfma_f32_16x16x32_bf16 v[164:167], a[164:167], a[92:95], v[164:167]\n"
  "s_waitcnt lgkmcnt(10)\n"
  "v_mfma_f32_16x16x32_bf16 v[168:171], a[48:51], a[144:147], v[168:171]\n"
  "ds_read_b128 a[160:163], v16 offset:51200\n"
  "v_mfma_f32_16x16x32_bf16 v[172:175], a[56:59], a[144:147], v[172:175]\n"
  "ds_read_b128 a[164:167], v17 offset:51200\n"
  "v_mfma_f32_16x16x32_bf16 v[176:179], a[64:67], a[144:147], v[176:179]\n"
  "buffer_load_dwordx4 v14, s[44:47], s49 offen offset:1024 lds\n"
  "v_mfma_f32_16x16x32_bf16 v[180:183], a[72:75], a[144:147], v[180:183]\n"
  "s_add_u32 s49, s49, 393216\n"
  "ds_read_b64_tr_b16 a[46:47], v19 offset:20992\n"
  "v_mfma_f32_16x16x32_bf16 v[184:187], a[144:147], a[80:83], v[184:187]\n"
  "s_cmp_eq_u32 s49, 6291456\n"
  "s_cselect_b32 s49, 0, s49\n"
  "v_mfma_f32_16x16x32_bf16 v[188:191], a[144:147], a[88:91], v[188:191]\n"
  "v_mfma_f32_16x16x32_bf16 v[168:171], a[52:55], a[148:151], v[168:171]\n"
  "s_add_u32 s51, s51, 24576\n"
  "v_mfma_f32_16x16x32_bf16 v[172:175], a[60:63], a[148:151], v[172:175]\n"
  "v_mfma_f32_16x16x32_bf16 v[176:179], a[68:71], a[148:151], v[176:179]\n"
  "s_cmp_ge_u32 s51, s52\n"
  "s_cselect_b32 s26, 73728, 0\n"
  "v_mfma_f32_16x16x32_bf16 v[180:183], a[76:79], a[148:151], v[180:183]\n"
  "v_mfma_f32_16x16x32_bf16 v[184:187], a[148:151], a[84:87], v[184:187]\n"
  "s_sub_u32 s51, s51, s26\n"
  "v_mfma_f32_16x16x32_bf16 v[188:191], a[148:151], a[92:95], v[188:191]\n"
  "s_waitcnt vmcnt(8) lgkmcnt(0)\n"
  "s_barrier\n"
  "v_mfma_f32_16x16x32_bf16 v[192:195], a[48:51], a[152:155], v[192:195]\n"
  "ds_read_b128 a[144:147], v16 offset:0\n"
  "v_mfma_f32_16x16x32_bf16 v[196:199], a[56:59], a[152:155], v[196:199]\n"
  "ds_read_b128 a[148:151], v17 offset:0\n"
  "v_mfma_f32_16x16x32_bf16 v[200:203], a[64:67], a[152:155], v[200:203]\n"
  "s_add_u32 m0, s50, 34816\n"
  "v_mfma_f32_16x16x32_bf16 v[204:207], a[72:75], a[152:155], v[204:207]\n"
  "buffer_load_dwordx4 v0, s[40:43], s48 offen offset:0 lds\n"
  "v_mfma_f32_16x16x32_bf16 v[208:211], a[152:155], a[80:83], v[208:211]\n"
  "s_cmp_eq_u32 s56, 0\n"
  "s_cselect_b32 s53, s70, 24576\n"
  "v_mfma_f32_16x16x32_bf16 v[212:215], a[152:155], a[88:91], v[212:215]\n"
  "v_mfma_f32_16x16x32_bf16 v[192:195], a[52:55], a[156:159], v[192:195]\n"
  "buffer_load_dwordx4 v1, s[40:43], s48 offen offset:1024 lds\n"
  "v_mfma_f32_16x16x32_bf16 v[196:199], a[60:63], a[156:159], v[196:199]\n"
  "s_cmp_eq_u32 s56, 0\n"
  "s_cselect_b32 s56, 3, s56\n"
  "v_mfma_f32_16x16x32_bf16 v[200:203], a[68:71], a[156:159], v[200:203]\n"
  "s_sub_u32 s56, s56, 1\n"
  "v_mfma_f32_16x16x32_bf16 v[204:207], a[76:79], a[156:159], v[204:207]\n"
  "buffer_load_dwordx4 v2, s[40:43], s48 offen offset:2048 lds\n"
  "v_mfma_f32_16x16x32_bf16 v[208:211], a[156:159], a[84:87], v[208:211]\n"
  "v_add_u32 v18, s53, v18\n"
  "v_mfma_f32_16x16x32_bf16 v[212:215], a[156:159], a[92:95], v[212:215]\n"
  "v_mfma_f32_16x16x32_bf16 v[216:219], a[48:51], a[160:163], v[216:219]\n"
  "ds_read_b128 a[152:155], v16 offset:2048\n"
  "v_mfma_f32_16x16x32_bf16 v[220:223], a[56:59], a[160:163], v[220:223]\n"
  "ds_read_b128 a[156:159], v17 offset:2048\n"
  "v_mfma_f32_16x16x32_bf16 v[224:227], a[64:67], a[160:163], v[224:227]\n"
  "buffer_load_dwordx4 v3, s[40:43], s48 offen offset:3072 lds\n"
  "v_mfma_f32_16x16x32_bf16 v[228:231], a[72:75], a[160:163], v[228:231]\n"
  "s_add_u32 m0, s50, 38912\n"
  "v_mfma_f32_16x16x32_bf16 v[232:235], a[160:163], a[80:83], v[232:235]\n"
  "buffer_load_dwordx4 v4, s[40:43], s48 offen offset:0 lds\n"
  "v_mfma_f32_16x16x32_bf16 v[236:239], a[160:163], a[88:91], v[236:239]\n"
  "v_add_u32 v19, s53, v19\n"
  "v_mfma_f32_16x16x32_bf16 v[216:219], a[52:55], a[164:167], v[216:219]\n"
  "v_mfma_f32_16x16x32_bf16 v[220:223], a[60:63], a[164:167], v[220:223]\n"
  "buffer_load_dwordx4 v5, s[40:43], s48 offen offset:1024 lds\n"
  "v_mfma_f32_16x16x32_bf16 v[224:227], a[68:71], a[164:167], v[224:227]\n"
  "v_mfma_f32_16x16x32_bf16 v[228:231], a[76:79], a[164:167], v[228:231]\n"
  "v_mfma_f32_16x16x32_bf16 v[232:235], a[164:167], a[84:87], v[232:235]\n"
  "buffer_load_dwordx4 v6, s[40:43], s48 offen offset:2048 lds\n"
  "v_mfma_f32_16x16x32_bf16 v[236:239], a[164:167], a[92:95], v[236:239]\n"
  "s_waitcnt lgkmcnt(2)\n"
  "v_mfma_f32_16x16x32_bf16 v[32:35], a[16:19], a[144:147], v[32:35]\n"
  "ds_read_b128 a[160:163], v16 offset:4096\n"
  "v_mfma_f32_16x16x32_bf16 v[36:39], a[24:27], a[144:147], v[36:39]\n"
  "ds_read_b128 a[164:167], v17 offset:4096\n"
  "v_mfma_f32_16x16x32_bf16 v[40:43], a[144:147], a[32:35], v[40:43]\n"
  "buffer_load_dwordx4 v7, s[40:43], s48 offen offset:3072 lds\n"
  "v_mfma_f32_16x16x32_bf16 v[44:47], a[144:147], a[40:43], v[44:47]\n"
  "s_add_u32 m0, s57, 34816\n"
  "ds_read_b64_tr_b16 a[48:49], v18 offset:0\n"
  "v_mfma_f32_16x16x32_bf16 v[32:35], a[20:23], a[148:151], v[32:35]\n"
  "buffer_load_dwordx4 v8, s[60:63], s48 offen lds\n"
  "v_mfma_f32_16x16x32_bf16 v[36:39], a[28:31], a[148:151], v[36:39]\n"
  "s_add_u32 s48, s48, 128\n"
  "ds_read_b64_tr_b16 a[50:51], v18 offset:512\n"
  "v_mfma_f32_16x16x32_bf16 v[40:43], a[148:151], a[36:39], v[40:43]\n"
  "s_and_b32 s48, s48, 2047\n"
  "v_mfma_f32_16x16x32_bf16 v[44:47], a[148:151], a[44:47], v[44:47]\n"
  "ds_read_b64_tr_b16 a[56:57], v19 offset:0\n"
  "s_waitcnt lgkmcnt(5)\n"
  "v_mfma_f32_16x16x32_bf16 v[48:51], a[0:3], a[152:155], v[48:51]\n"
  "ds_read_b128 a[144:147], v16 offset:6144\n"
  "v_mfma_f32_16x16x32_bf16 v[52:55], a[8:11], a[152:155], v[52:55]\n"
  "ds_read_b128 a[148:151], v17 offset:6144\n"
  "v_mfma_f32_16x16x32_bf16 v[56:59], a[16:19], a[152:155], v[56:59]\n"
  "s_add_u32 m0, s51, 0\n"
  "ds_read_b64_tr_b16 a[58:59], v19 offset:512\n"
  "v_mfma_f32_16x16x32_bf16 v[60:63], a[24:27], a[152:155], v[60:63]\n"
  "buffer_load_dwordx4 v9, s[44:47], s49 offen offset:0 lds\n"
  "v_mfma_f32_16x16x32_bf16 v[64:67], a[152:155], a[32:35], v[64:67]\n"
  "v_mfma_f32_16x16x32_bf16 v[68:71], a[152:155], a[40:43], v[68:71]\n"
  "buffer_load_dword v27, v15, s[64:67], s68 offen\n"
  "v_mfma_f32_16x16x32_bf16 v[48:51], a[4:7], a[156:159], v[48:51]\n"
  "s_sub_u32 s68, s68, 6144\n"
  "ds_read_b64_tr_b16 a[64:65], v18 offset:8192\n"
  "v_mfma_f32_16x16x32_bf16 v[52:55], a[12:15], a[156:159], v[52:55]\n"
  "v_mfma_f32_16x16x32_bf16 v[56:59], a[20:23], a[156:159], v[56:59]\n"
  "ds_read_b64_tr_b16 a[66:67], v18 offset:8704\n"
  "v_mfma_f32_16x16x32_bf16 v[60:63], a[28:31], a[156:159], v[60:63]\n"
  "v_mfma_f32_16x16x32_bf16 v[64:67], a[156:159], a[36:39], v[64:67]\n"
  "v_mfma_f32_16x16x32_bf16 v[68:71], a[156:159], a[44:47], v[68:71]\n"
  "ds_read_b64_tr_b16 a[72:73], v19 offset:8192\n"
  "s_waitcnt lgkmcnt(9)\n"
  "v_mfma_f32_16x16x32_bf16 v[72:75], a[0:3], a[160:163], v[72:75]\n"
  "ds_read_b128 a[152:155], v16 offset:8192\n"
  "v_mfma_f32_16x16x32_bf16 v[76:79], a[8:11], a[160:163], v[76:79]\n"
  "ds_read_b128 a[156:159], v17 offset:8192\n"
  "v_mfma_f32_16x16x32_bf16 v[80:83], a[16:19], a[160:163], v[80:83]\n"
  "buffer_load_dwordx4 v10, s[44:47], s49 offen offset:1024 lds\n"
  "v_mfma_f32_16x16x32_bf16 v[84:87], a[24:27], a[160:163], v[84:87]\n"
  "ds_read_b64_tr_b16 a[74:75], v19 offset:8704\n"
  "v_mfma_f32_16x16x32_bf16 v[88:91], a[160:163], a[32:35], v[88:91]\n"
  "v_mfma_f32_16x16x32_bf16 v[92:95], a[160:163], a[40:43], v[92:95]\n"
  "ds_read_b64_tr_b16 a[80:81], v18 offset:16384\n"
  "v_mfma_f32_16x16x32_bf16 v[72:75], a[4:7], a[164:167], v[72:75]\n"
  "v_mfma_f32_16x16x32_bf16 v[76:79], a[12:15], a[164:167], v[76:79]\n"
  "ds_read_b64_tr_b16 a[82:83], v18 offset:16896\n"
  "v_mfma_f32_16x16x32_bf16 v[80:83], a[20:23], a[164:167], v[80:83]\n"
  "v_mfma_f32_16x16x32_bf16 v[84:87], a[28:31], a[164:167], v[84:87]\n"
  "v_mfma_f32_16x16x32_bf16 v[88:91], a[164:167], a[36:39], v[88:91]\n"
  "ds_read_b64_tr_b16 a[88:89], v19 offset:16384\n"
  "v_mfma_f32_16x16x32_bf16 v[92:95], a[164:167], a[44:47], v[92:95]\n"
  "s_waitcnt lgkmcnt(10)\n"
  "v_mfma_f32_16x16x32_bf16 v[96:99], a[0:3], a[144:147], v[96:99]\n"
  "ds_read_b128 a[160:163], v16 offset:10240\n"
  "v_mfma_f32_16x16x32_bf16 v[100:103], a[8:11], a[144:147], v[100:103]\n"
  "ds_read_b128 a[164:167], v17 offset:10240\n"
  "s_add_u32 m0, s51, 8192\n"
  "v_mfma_f32_16x16x32_bf16 v[104:107], a[16:19], a[144:147], v[104:107]\n"
  "buffer_load_dwordx4 v11, s[44:47], s49 offen offset:0 lds\n"
  "v_mfma_f32_16x16x32_bf16 v[108:111], a[24:27], a[144:147], v[108:111]\n"
  "ds_read_b64_tr_b16 a[90:91], v19 offset:16896\n"
  "v_mfma_f32_16x16x32_bf16 v[112:115], a[144:147], a[32:35], v[112:115]\n"
  "ds_read_b64_tr_b16 a[52:53], v18 offset:4096\n"
  "v_mfma_f32_16x16x32_bf16 v[116:119], a[144:147], a[40:43], v[116:119]\n"
  "v_mfma_f32_16x16x32_bf16 v[96:99], a[4:7], a[148:151], v[96:99]\n"
  "v_mfma_f32_16x16x32_bf16 v[100:103], a[12:15], a[148:151], v[100:103]\n"
  "ds_read_b64_tr_b16 a[54:55], v18 offset:4608\n"
  "v_mfma_f32_16x16x32_bf16 v[104:107], a[20:23], a[148:151], v[104:107]\n"
  "v_mfma_f32_16x16x32_bf16 v[108:111], a[28:31], a[148:151], v[108:111]\n"
  "v_mfma_f32_16x16x32_bf16 v[112:115], a[148:151], a[36:39], v[112:115]\n"
  "ds_read_b64_tr_b16 a[60:61], v19 offset:4096\n"
  "v_mfma_f32_16x16x32_bf16 v[116:119], a[148:151], a[44:47], v[116:119]\n"
  "s_waitcnt lgkmcnt(10)\n"
  "v_mfma_f32_16x16x32_bf16 v[120:123], a[0:3], a[152:155], v[120:123]\n"
  "ds_read_b128 a[144:147], v16 offset:12288\n"
  "v_mfma_f32_16x16x32_bf16 v[124:127], a[8:11], a[152:155], v[124:127]\n"
  "ds_read_b128 a[148:151], v17 offset:12288\n"
  "v_mfma_f32_16x16x32_bf16 v[128:131], a[16:19], a[152:155], v[128:131]\n"
  "buffer_load_dwordx4 v12, s[44:47], s49 offen offset:1024 lds\n"
  "v_mfma_f32_16x16x32_bf16 v[132:135], a[24:27], a[152:155], v[132:135]\n"
  "ds_read_b64_tr_b16 a[62:63], v19 offset:4608\n"
  "v_mfma_f32_16x16x32_bf16 v[136:139], a[152:155], a[32:35], v[136:139]\n"
  "ds_read_b64_tr_b16 a[68:69], v18 offset:12288\n"
  "v_mfma_f32_16x16x32_bf16 v[140:143], a[152:155], a[40:43], v[140:143]\n"
  "v_mfma_f32_16x16x32_bf16 v[120:123], a[4:7], a[156:159], v[120:123]\n"
  "ds_read_b64_tr_b16 a[70:71], v18 offset:12800\n"
  "v_mfma_f32_16x16x32_bf16 v[124:127], a[12:15], a[156:159], v[124:127]\n"
  "v_mfma_f32_16x16x32_bf16 v[128:131], a[20:23], a[156:159], v[128:131]\n"
  "v_mfma_f32_16x16x32_bf16 v[132:135], a[28:31], a[156:159], v[132:135]\n"
  "ds_read_b64_tr_b16 a[76:77], v19 offset:12288\n"
  "v_mfma_f32_16x16x32_bf16 v[136:139], a[156:159], a[36:39], v[136:139]\n"
  "v_mfma_f32_16x16x32_bf16 v[140:143], a[156:159], a[44:47], v[140:143]\n"
  "s_waitcnt lgkmcnt(10)\n"
  "v_mfma_f32_16x16x32_bf16 v[144:147], a[0:3], a[160:163], v[144:147]\n"
  "ds_read_b128 a[152:155], v16 offset:14336\n"
  "v_mfma_f32_16x16x32_bf16 v[148:151], a[8:11], a[160:163], v[148:151]\n"
  "ds_read_b128 a[156:159], v17 offset:14336\n"
  "s_add_u32 m0, s51, 16384\n"
  "v_mfma_f32_16x16x32_bf16 v[152:155], a[16:19], a[160:163], v[152:155]\n"
  "buffer_load_dwordx4 v13, s[44:47], s49 offen offset:0 lds\n"
  "v_mfma_f32_16x16x32_bf16 v[156:159], a[24:27], a[160:163], v[156:159]\n"
  "ds_read_b64_tr_b16 a[78:79], v19 offset:12800\n"
  "v_mfma_f32_16x16x32_bf16 v[160:163], a[160:163], a[32:35], v[160:163]\n"
  "ds_read_b64_tr_b16 a[84:85], v18 offset:20480\n"
  "v_mfma_f32_16x16x32_bf16 v[164:167], a[160:163], a[40:43], v[164:167]\n"
  "v_mfma_f32_16x16x32_bf16 v[144:147], a[4:7], a[164:167], v[144:147]\n"
  "ds_read_b64_tr_b16 a[86:87], v18 offset:20992\n"
  "v_mfma_f32_16x16x32_bf16 v[148:151], a[12:15], a[164:167], v[148:151]\n"
  "v_mfma_f32_16x16x32_bf16 v[152:155], a[20:23], a[164:167], v[152:155]\n"
  "v_mfma_f32_16x16x32_bf16 v[156:159], a[28:31], a[164:167], v[156:159]\n"
  "ds_read_b64_tr_b16 a[92:93], v19 offset:20480\n"
  "v_mfma_f32_16x16x32_bf16 v[160:163], a[164:167], a[36:39], v[160:163]\n"
  "v_mfma_f32_16x16x32_bf16 v[164:167], a[164:167], a[44:47], v[164:167]\n"
  "s_waitcnt lgkmcnt(10)\n"
  "v_mfma_f32_16x16x32_bf16 v[168:171], a[0:3], a[144:147], v[168:171]\n"
  "ds_read_b128 a[160:163], v16 offset:16384\n"
  "v_mfma_f32_16x16x32_bf16 v[172:175], a[8:11], a[144:147], v[172:175]\n"
  "ds_read_b128 a[164:167], v17 offset:16384\n"
  "v_mfma_f32_16x16x32_bf16 v[176:179], a[16:19], a[144:147], v[176:179]\n"
  "buffer_load_dwordx4 v14, s[44:47], s49 offen offset:1024 lds\n"
  "v_mfma_f32_16x16x32_bf16 v[180:183], a[24:27], a[144:147], v[180:183]\n"
  "s_add_u32 s49, s49, 393216\n"
  "ds_read_b64_tr_b16 a[94:95], v19 offset:20992\n"
  "v_mfma_f32_16x16x32_bf16 v[184:187], a[144:147], a[32:35], v[184:187]\n"
  "s_cmp_eq_u32 s49, 6291456\n"
  "s_cselect_b32 s49, 0, s49\n"
  "v_mfma_f32_16x16x32_bf16 v[188:191], a[144:147], a[40:43], v[188:191]\n"
  "v_mfma_f32_16x16x32_bf16 v[168:171], a[4:7], a[148:151], v[168:171]\n"
  "s_add_u32 s51, s51, 24576\n"
  "v_mfma_f32_16x16x32_bf16 v[172:175], a[12:15], a[148:151], v[172:175]\n"
  "v_mfma_f32_16x16x32_bf16 v[176:179], a[20:23], a[148:151], v[176:179]\n"
  "s_cmp_ge_u32 s51, s52\n"
  "s_cselect_b32 s26, 73728, 0\n"
  "v_mfma_f32_16x16x32_bf16 v[180:183], a[28:31], a[148:151], v[180:183]\n"
  "v_mfma_f32_16x16x32_bf16 v[184:187], a[148:151], a[36:39], v[184:187]\n"
  "s_sub_u32 s51, s51, s26\n"
  "v_mfma_f32_16x16x32_bf16 v[188:191], a[148:151], a[44:47], v[188:191]\n"
  "s_waitcnt vmcnt(7) lgkmcnt(0)\n"
  "s_barrier\n"
  "v_mfma_f32_16x16x32_bf16 v[192:195], a[0:3], a[152:155], v[192:195]\n"
  "ds_read_b128 a[144:147], v16 offset:34816\n"
  "v_mfma_f32_16x16x32_bf16 v[196:199], a[8:11], a[152:155], v[196:199]\n"
  "ds_read_b128 a[148:151], v17 offset:34816\n"
  "v_mfma_f32_16x16x32_bf16 v[200:203], a[16:19], a[152:155], v[200:203]\n"
  "s_add_u32 m0, s50, 0\n"
  "v_mfma_f32_16x16x32_bf16 v[204:207], a[24:27], a[152:155], v[204:207]\n"
  "buffer_load_dwordx4 v0, s[40:43], s48 offen offset:0 lds\n"
  "v_mfma_f32_16x16x32_bf16 v[208:211], a[152:155], a[32:35], v[208:211]\n"
  "s_cmp_eq_u32 s56, 0\n"
  "s_cselect_b32 s53, s70, 24576\n"
  "v_mfma_f32_16x16x32_bf16 v[212:215], a[152:155], a[40:43], v[212:215]\n"
  "v_mfma_f32_16x16x32_bf16 v[192:195], a[4:7], a[156:159], v[192:195]\n"
  "buffer_load_dwordx4 v1, s[40:43], s48 offen offset:1024 lds\n"
  "v_mfma_f32_16x16x32_bf16 v[196:199], a[12:15], a[156:159], v[196:199]\n"
  "s_cmp_eq_u32 s56, 0\n"
  "s_cselect_b32 s56, 3, s56\n"
  "v_mfma_f32_16x16x32_bf16 v[200:203], a[20:23], a[156:159], v[200:203]\n"
  "s_sub_u32 s56, s56, 1\n"
  "v_mfma_f32_16x16x32_bf16 v[204:207], a[28:31], a[156:159], v[204:207]\n"
  "buffer_load_dwordx4 v2, s[40:43], s48 offen offset:2048 lds\n"
  "v_mfma_f32_16x16x32_bf16 v[208:211], a[156:159], a[36:39], v[208:211]\n"
  "v_add_u32 v18, s53, v18\n"
  "v_mfma_f32_16x16x32_bf16 v[212:215], a[156:159], a[44:47], v[212:215]\n"
  "v_mfma_f32_16x16x32_bf16 v[216:219], a[0:3], a[160:163], v[216:219]\n"
  "ds_read_b128 a[152:155], v16 offset:36864\n"
  "v_mfma_f32_16x16x32_bf16 v[220:223], a[8:11], a[160:163], v[220:223]\n"
  "ds_read_b128 a[156:159], v17 offset:36864\n"
  "v_mfma_f32_16x16x32_bf16 v[224:227], a[16:19], a[160:163], v[224:227]\n"
  "buffer_load_dwordx4 v3, s[40:43], s48 offen offset:3072 lds\n"
  "v_mfma_f32_16x16x32_bf16 v[228:231], a[24:27], a[160:163], v[228:231]\n"
  "s_add_u32 m0, s50, 4096\n"
  "v_mfma_f32_16x16x32_bf16 v[232:235], a[160:163], a[32:35], v[232:235]\n"
  "buffer_load_dwordx4 v4, s[40:43], s48 offen offset:0 lds\n"
  "v_mfma_f32_16x16x32_bf16 v[236:239], a[160:163], a[40:43], v[236:239]\n"
  "v_add_u32 v19, s53, v19\n"
  "v_mfma_f32_16x16x32_bf16 v[216:219], a[4:7], a[164:167], v[216:219]\n"
  "v_mfma_f32_16x16x32_bf16 v[220:223], a[12:15], a[164:167], v[220:223]\n"
  "buffer_load_dwordx4 v5, s[40:43], s48 offen offset:1024 lds\n"
  "v_mfma_f32_16x16x32_bf16 v[224:227], a[20:23], a[164:167], v[224:227]\n"
  "v_mfma_f32_16x16x32_bf16 v[228:231], a[28:31], a[164:167], v[228:231]\n"
  "v_mfma_f32_16x16x32_bf16 v[232:235], a[164:167], a[36:39], v[232:235]\n"
  "buffer_load_dwordx4 v6, s[40:43], s48 offen offset:2048 lds\n"
  "v_mfma_f32_16x16x32_bf16 v[236:239], a[164:167], a[44:47], v[236:239]\n"
  "s_sub_u32 s54, s54, 1\n"
  "s_cmp_lg_u32 s54, 0\n"
  "s_cbranch_scc1 K1A_LOOP_%=\n"
  "s_waitcnt lgkmcnt(2)\n"
  "v_mfma_f32_16x16x32_bf16 v[32:35], a[64:67], a[144:147], v[32:35]\n"
  "ds_read_b128 a[160:163], v16 offset:38912\n"
  "v_mfma_f32_16x16x32_bf16 v[36:39], a[72:75], a[144:147], v[36:39]\n"
  "ds_read_b128 a[164:167], v17 offset:38912\n"
  "v_mfma_f32_16x16x32_bf16 v[40:43], a[144:147], a[80:83], v[40:43]\n"
  "buffer_load_dwordx4 v7, s[40:43], s48 offen offset:3072 lds\n"
  "v_mfma_f32_16x16x32_bf16 v[44:47], a[144:147], a[88:91], v[44:47]\n"
  "s_add_u32 m0, s57, 0\n"
  "ds_read_b64_tr_b16 a[0:1], v18 offset:0\n"
  "v_mfma_f32_16x16x32_bf16 v[32:35], a[68:71], a[148:151], v[32:35]\n"
  "buffer_load_dwordx4 v8, s[60:63], s48 offen lds\n"
  "v_mfma_f32_16x16x32_bf16 v[36:39], a[76:79], a[148:151], v[36:39]\n"
  "s_add_u32 s48, s48, 128\n"
  "ds_read_b64_tr_b16 a[2:3], v18 offset:512\n"
  "v_mfma_f32_16x16x32_bf16 v[40:43], a[148:151], a[84:87], v[40:43]\n"
  "s_and_b32 s48, s48, 2047\n"
  "s_add_u32 m0, s50, 118784\n"
  "ds_read_b64_tr_b16 a[8:9], v19 offset:0\n"
  "v_mfma_f32_16x16x32_bf16 v[44:47], a[148:151], a[92:95], v[44:47]\n"
  "buffer_load_dwordx4 v0, s[40:43], s48 offen offset:0 lds\n"
  "s_waitcnt lgkmcnt(5)\n"
  "v_mfma_f32_16x16x32_bf16 v[48:51], a[48:51], a[152:155], v[48:51]\n"
  "ds_read_b128 a[168:171], v16 offset:40960\n"
  "v_mfma_f32_16x16x32_bf16 v[52:55], a[56:59], a[152:155], v[52:55]\n"
  "ds_read_b128 a[172:175], v17 offset:40960\n"
  "v_mfma_f32_16x16x32_bf16 v[56:59], a[64:67], a[152:155], v[56:59]\n"
  "buffer_load_dwordx4 v1, s[40:43], s48 offen offset:1024 lds\n"
  "v_mfma_f32_16x16x32_bf16 v[60:63], a[72:75], a[152:155], v[60:63]\n"
  "ds_read_b64_tr_b16 a[10:11], v19 offset:512\n"
  "v_add_u32 v9, 118784, v16\n"
  "v_add_u32 v10, 118784, v17\n"
  "v_mfma_f32_16x16x32_bf16 v[64:67], a[152:155], a[80:83], v[64:67]\n"
  "ds_read_b64_tr_b16 a[16:17], v18 offset:8192\n"
  "v_lshlrev_b32 v29, 7, v28\n"
  "v_mfma_f32_16x16x32_bf16 v[68:71], a[152:155], a[88:91], v[68:71]\n"
  "buffer_load_dwordx4 v2, s[40:43], s48 offen offset:2048 lds\n"
  "v_mfma_f32_16x16x32_bf16 v[48:51], a[52:55], a[156:159], v[48:51]\n"
  "ds_read_b64_tr_b16 a[18:19], v18 offset:8704\n"
  "v_mfma_f32_16x16x32_bf16 v[52:55], a[60:63], a[156:159], v[52:55]\n"
  "ds_read_b64_tr_b16 a[24:25], v19 offset:8192\n"
  "v_mfma_f32_16x16x32_bf16 v[56:59], a[68:71], a[156:159], v[56:59]\n"
  "buffer_load_dwordx4 v3, s[40:43], s48 offen offset:3072 lds\n"
  "v_mfma_f32_16x16x32_bf16 v[60:63], a[76:79], a[156:159], v[60:63]\n"
  "ds_read_b64_tr_b16 a[26:27], v19 offset:8704\n"
  "v_mfma_f32_16x16x32_bf16 v[64:67], a[156:159], a[84:87], v[64:67]\n"
  "s_add_u32 m0, s50, 122880\n"
  "ds_read_b64_tr_b16 a[32:33], v18 offset:16384\n"
  "v_mfma_f32_16x16x32_bf16 v[68:71], a[156:159], a[92:95], v[68:71]\n"
  "buffer_load_dwordx4 v4, s[40:43], s48 offen offset:0 lds\n"
  "s_waitcnt lgkmcnt(11)\n"
  "v_mfma_f32_16x16x32_bf16 v[72:75], a[48:51], a[160:163], v[72:75]\n"
  "ds_read_b128 a[144:147], v16 offset:43008\n"
  "v_mfma_f32_16x16x32_bf16 v[76:79], a[56:59], a[160:163], v[76:79]\n"
  "ds_read_b128 a[148:151], v17 offset:43008\n"
  "v_mfma_f32_16x16x32_bf16 v[80:83], a[64:67], a[160:163], v[80:83]\n"
  "buffer_load_dwordx4 v5, s[40:43], s48 offen offset:1024 lds\n"
  "v_mfma_f32_16x16x32_bf16 v[84:87], a[72:75], a[160:163], v[84:87]\n"
  "ds_read_b64_tr_b16 a[34:35], v18 offset:16896\n"
  "v_mfma_f32_16x16x32_bf16 v[88:91], a[160:163], a[80:83], v[88:91]\n"
  "ds_read_b64_tr_b16 a[40:41], v19 offset:16384\n"
  "v_mfma_f32_16x16x32_bf16 v[92:95], a[160:163], a[88:91], v[92:95]\n"
  "buffer_load_dwordx4 v6, s[40:43], s48 offen offset:2048 lds\n"
  "v_mfma_f32_16x16x32_bf16 v[72:75], a[52:55], a[164:167], v[72:75]\n"
  "ds_read_b64_tr_b16 a[42:43], v19 offset:16896\n"
  "v_mfma_f32_16x16x32_bf16 v[76:79], a[60:63], a[164:167], v[76:79]\n"
  "ds_read_b64_tr_b16 a[4:5], v18 offset:4096\n"
  "v_mfma_f32_16x16x32_bf16 v[80:83], a[68:71], a[164:167], v[80:83]\n"
  "buffer_load_dwordx4 v7, s[40:43], s48 offen offset:3072 lds\n"
  "v_mfma_f32_16x16x32_bf16 v[84:87], a[76:79], a[164:167], v[84:87]\n"
  "ds_read_b64_tr_b16 a[6:7], v18 offset:4608\n"
  "v_mfma_f32_16x16x32_bf16 v[88:91], a[164:167], a[84:87], v[88:91]\n"
  "s_add_u32 m0, s57, 118784\n"
  "v_mfma_f32_16x16x32_bf16 v[92:95], a[164:167], a[92:95], v[92:95]\n"
  "buffer_load_dwordx4 v8, s[60:63], s48 offen lds\n"
  "s_waitcnt lgkmcnt(13)\n"
  "v_mfma_f32_16x16x32_bf16 v[96:99], a[48:51], a[168:171], v[96:99]\n"
  "ds_read_b128 a[152:155], v16 offset:45056\n"
  "v_mfma_f32_16x16x32_bf16 v[100:103], a[56:59], a[168:171], v[100:103]\n"
  "ds_read_b128 a[156:159], v17 offset:45056\n"
  "v_and_b32 v22, 15, v28\n"
  "v_lshrrev_b32 v23, 4, v28\n"
  "v_mfma_f32_16x16x32_bf16 v[104:107], a[64:67], a[168:171], v[104:107]\n"
  "ds_read_b64_tr_b16 a[12:13], v19 offset:4096\n"
  "v_lshlrev_b32 v23, 2, v23\n"
  "v_sub_u32 v22, v22, v23\n"
  "v_mfma_f32_16x16x32_bf16 v[108:111], a[72:75], a[168:171], v[108:111]\n"
  "ds_read_b64_tr_b16 a[14:15], v19 offset:4608\n"
  "v_mov_b32 v24, 0xf149f2ca\n"
  "s_lshr_b32 s69, s59, 1\n"
  "v_mfma_f32_16x16x32_bf16 v[112:115], a[168:171], a[80:83], v[112:115]\n"
  "ds_read_b64_tr_b16 a[20:21], v18 offset:12288\n"
  "s_add_u32 s69, s69, s98\n"
  "s_cmp_eq_u32 s69, 0\n"
  "s_cselect_b32 s69, 0xffffffc0, 0\n"
  "v_mfma_f32_16x16x32_bf16 v[116:119], a[168:171], a[88:91], v[116:119]\n"
  "ds_read_b64_tr_b16 a[22:23], v18 offset:12800\n"
  "s_mov_b32 s71, 0x3e8293ee\n"
  "v_lshlrev_b32 v26, 4, v28\n"
  "v_mfma_f32_16x16x32_bf16 v[96:99], a[52:55], a[172:175], v[96:99]\n"
  "v_subrev_u32 v23, 0, v22\n"
  "v_and_b32 v25, 0xfffffff1, v23\n"
  "v_mfma_f32_16x16x32_bf16 v[100:103], a[60:63], a[172:175], v[100:103]\n"
  "ds_read_b64_tr_b16 a[28:29], v19 offset:12288\n"
  "v_cmp_eq_u32 vcc, 0, v25\n"
  "v_mfma_f32_16x16x32_bf16 v[104:107], a[68:71], a[172:175], v[104:107]\n"
  "v_add_u32 v23, 16, v23\n"
  "v_cndmask_b32_e64 v244, v24, 0, vcc\n"
  "v_mfma_f32_16x16x32_bf16 v[108:111], a[76:79], a[172:175], v[108:111]\n"
  "ds_read_b64_tr_b16 a[30:31], v19 offset:12800\n"
  "v_and_b32 v25, 0xfffffff1, v23\n"
  "v_cmp_eq_u32 vcc, 0, v25\n"
  "v_mfma_f32_16x16x32_bf16 v[112:115], a[172:175], a[84:87], v[112:115]\n"
  "v_or_b32 v25, s69, v25\n"
  "v_cndmask_b32_e64 v240, v24, 0, vcc\n"
  "v_mfma_f32_16x16x32_bf16 v[116:119], a[172:175], a[92:95], v[116:119]\n"
  "ds_read_b64_tr_b16 a[36:37], v18 offset:20480\n"
  "v_cmp_eq_u32 vcc, 0, v25\n"
  "s_waitcnt lgkmcnt(14)\n"
  "v_mfma_f32_16x16x32_bf16 v[120:123], a[48:51], a[144:147], v[120:123]\n"
  "ds_read_b128 a[160:163], v16 offset:47104\n"
  "v_mov_b32 v12, 0x3f803f80\n"
  "v_cndmask_b32_e64 v248, v24, 0, vcc\n"
  "v_mfma_f32_16x16x32_bf16 v[124:127], a[56:59], a[144:147], v[124:127]\n"
  "ds_read_b128 a[164:167], v17 offset:47104\n"
  "v_subrev_u32 v23, 1, v22\n"
  "v_and_b32 v25, 0xfffffff1, v23\n"
  "v_mfma_f32_16x16x32_bf16 v[128:131], a[64:67], a[144:147], v[128:131]\n"
  "ds_read_b64_tr_b16 a[38:39], v18 offset:20992\n"
  "v_cmp_eq_u32 vcc, 0, v25\n"
  "v_mfma_f32_16x16x32_bf16 v[132:135], a[72:75], a[144:147], v[132:135]\n"
  "ds_read_b64_tr_b16 a[44:45], v19 offset:20480\n"
  "v_add_u32 v23, 16, v23\n"
  "v_cndmask_b32_e64 v245, v24, 0, vcc\n"
  "v_mfma_f32_16x16x32_bf16 v[136:139], a[144:147], a[80:83], v[136:139]\n"
  "v_and_b32 v25, 0xfffffff1, v23\n"
  "v_cmp_eq_u32 vcc, 0, v25\n"
  "v_mfma_f32_16x16x32_bf16 v[140:143], a[144:147], a[88:91], v[140:143]\n"
  "ds_read_b64_tr_b16 a[46:47], v19 offset:20992\n"
  "v_or_b32 v25, s69, v25\n"
  "v_cndmask_b32_e64 v241, v24, 0, vcc\n"
  "v_mfma_f32_16x16x32_bf16 v[120:123], a[52:55], a[148:151], v[120:123]\n"
  "s_cmp_eq_u32 s56, 0\n"
  "s_cselect_b32 s53, s70, 24576\n"
  "v_cmp_eq_u32 vcc, 0, v25\n"
  "v_mfma_f32_16x16x32_bf16 v[124:127], a[60:63], a[148:151], v[124:127]\n"
  "v_mov_b32 v13, 0x3f803f80\n"
  "v_cndmask_b32_e64 v249, v24, 0, vcc\n"
  "v_mfma_f32_16x16x32_bf16 v[128:131], a[68:71], a[148:151], v[128:131]\n"
  "v_subrev_u32 v23, 2, v22\n"
  "v_and_b32 v25, 0xfffffff1, v23\n"
  "v_mfma_f32_16x16x32_bf16 v[132:135], a[76:79], a[148:151], v[132:135]\n"
  "s_cmp_eq_u32 s56, 0\n"
  "s_cselect_b32 s56, 3, s56\n"
  "v_cmp_eq_u32 vcc, 0, v25\n"
  "v_mfma_f32_16x16x32_bf16 v[136:139], a[148:151], a[84:87], v[136:139]\n"
  "v_add_u32 v23, 16, v23\n"
  "v_cndmask_b32_e64 v246, v24, 0, vcc\n"
  "v_mfma_f32_16x16x32_bf16 v[140:143], a[148:151], a[92:95], v[140:143]\n"
  "v_and_b32 v25, 0xfffffff1, v23\n"
  "v_cmp_eq_u32 vcc, 0, v25\n"
  "s_waitcnt lgkmcnt(12)\n"
  "v_mfma_f32_16x16x32_bf16 v[144:147], a[48:51], a[152:155], v[144:147]\n"
  "v_or_b32 v25, s69, v25\n"
  "v_cndmask_b32_e64 v242, v24, 0, vcc\n"
  "v_mfma_f32_16x16x32_bf16 v[148:151], a[56:59], a[152:155], v[148:151]\n"
  "s_sub_u32 s56, s56, 1\n"
  "v_cmp_eq_u32 vcc, 0, v25\n"
  "v_mfma_f32_16x16x32_bf16 v[152:155], a[64:67], a[152:155], v[152:155]\n"
  "v_mov_b32 v14, 0x3f803f80\n"
  "v_cndmask_b32_e64 v250, v24, 0, vcc\n"
  "v_mfma_f32_16x16x32_bf16 v[156:159], a[72:75], a[152:155], v[156:159]\n"
  "v_subrev_u32 v23, 3, v22\n"
  "v_and_b32 v25, 0xfffffff1, v23\n"
  "v_mfma_f32_16x16x32_bf16 v[160:163], a[152:155], a[80:83], v[160:163]\n"
  "v_add_u32 v18, s53, v18\n"
  "v_cmp_eq_u32 vcc, 0, v25\n"
  "v_mfma_f32_16x16x32_bf16 v[164:167], a[152:155], a[88:91], v[164:167]\n"
  "v_add_u32 v23, 16, v23\n"
  "v_cndmask_b32_e64 v247, v24, 0, vcc\n"
  "v_mfma_f32_16x16x32_bf16 v[144:147], a[52:55], a[156:159], v[144:147]\n"
  "v_and_b32 v25, 0xfffffff1, v23\n"
  "v_cmp_eq_u32 vcc, 0, v25\n"
  "v_mfma_f32_16x16x32_bf16 v[148:151], a[60:63], a[156:159], v[148:151]\n"
  "v_or_b32 v25, s69, v25\n"
  "v_cndmask_b32_e64 v243, v24, 0, vcc\n"
  "v_mfma_f32_16x16x32_bf16 v[152:155], a[68:71], a[156:159], v[152:155]\n"
  "v_add_u32 v19, s53, v19\n"
  "v_cmp_eq_u32 vcc, 0, v25\n"
  "v_mfma_f32_16x16x32_bf16 v[156:159], a[76:79], a[156:159], v[156:159]\n"
  "v_mov_b32 v15, 0x3f803f80\n"
  "v_cndmask_b32_e64 v251, v24, 0, vcc\n"
  "v_mfma_f32_16x16x32_bf16 v[160:163], a[156:159], a[84:87], v[160:163]\n"
  "v_mov_b32 v25, 0xffff\n"
  "v_mov_b32 v23, 0xffff0000\n"
  "v_mfma_f32_16x16x32_bf16 v[164:167], a[156:159], a[92:95], v[164:167]\n"
  "v_cmp_eq_u32 vcc, 0, v240\n"
  "s_waitcnt vmcnt(0)\n"
  "s_barrier\n"
  "s_waitcnt lgkmcnt(3)\n"
  "v_mfma_f32_16x16x32_bf16 v[168:171], a[48:51], a[160:163], v[168:171]\n"
  "ds_read_b128 a[168:171], v16 offset:0\n"
  "ds_read_b128 a[144:147], v9 offset:0\n"
  "s_nop 0\n"
  "v_cndmask_b32_e64 v8, 0, v25, vcc\n"
  "v_mfma_f32_16x16x32_bf16 v[172:175], a[56:59], a[160:163], v[172:175]\n"
  "ds_read_b128 a[172:175], v17 offset:0\n"
  "ds_read_b128 a[148:151], v10 offset:0\n"
  "v_cmp_eq_u32 vcc, 0, v241\n"
  "v_mfma_f32_16x16x32_bf16 v[176:179], a[64:67], a[160:163], v[176:179]\n"
  "ds_read_b64_tr_b16 a[96:97], v18 offset:0\n"
  "s_nop 0\n"
  "v_cndmask_b32 v8, v8, v23, vcc\n"
  "v_mfma_f32_16x16x32_bf16 v[180:183], a[72:75], a[160:163], v[180:183]\n"
  "ds_read_b64_tr_b16 a[98:99], v18 offset:512\n"
  "v_cmp_eq_u32 vcc, 0, v242\n"
  "v_mfma_f32_16x16x32_bf16 v[184:187], a[160:163], a[80:83], v[184:187]\n"
  "ds_read_b64_tr_b16 a[104:105], v19 offset:0\n"
  "s_nop 0\n"
  "v_cndmask_b32_e64 v11, 0, v25, vcc\n"
  "v_mfma_f32_16x16x32_bf16 v[188:191], a[160:163], a[88:91], v[188:191]\n"
  "ds_read_b64_tr_b16 a[106:107], v19 offset:512\n"
  "v_cmp_eq_u32 vcc, 0, v243\n"
  "v_mfma_f32_16x16x32_bf16 v[168:171], a[52:55], a[164:167], v[168:171]\n"
  "ds_read_b64_tr_b16 a[112:113], v18 offset:8192\n"
  "s_nop 0\n"
  "v_cndmask_b32 v11, v11, v23, vcc\n"
  "v_mfma_f32_16x16x32_bf16 v[172:175], a[60:63], a[164:167], v[172:175]\n"
  "ds_read_b64_tr_b16 a[114:115], v18 offset:8704\n"
  "v_cmp_eq_u32 vcc, 0, v244\n"
  "v_mfma_f32_16x16x32_bf16 v[176:179], a[68:71], a[164:167], v[176:179]\n"
  "ds_read_b64_tr_b16 a[120:121], v19 offset:8192\n"
  "s_nop 0\n"
  "v_cndmask_b32_e64 v20, 0, v25, vcc\n"
  "v_mfma_f32_16x16x32_bf16 v[180:183], a[76:79], a[164:167], v[180:183]\n"
  "ds_read_b64_tr_b16 a[122:123], v19 offset:8704\n"
  "v_cmp_eq_u32 vcc, 0, v245\n"
  "v_mfma_f32_16x16x32_bf16 v[184:187], a[164:167], a[84:87], v[184:187]\n"
  "ds_read_b64_tr_b16 a[128:129], v18 offset:16384\n"
  "s_nop 0\n"
  "v_cndmask_b32 v20, v20, v23, vcc\n"
  "v_mfma_f32_16x16x32_bf16 v[188:191], a[164:167], a[92:95], v[188:191]\n"
  "ds_read_b64_tr_b16 a[130:131], v18 offset:16896\n"
  "v_cmp_eq_u32 vcc, 0, v246\n"
  "s_waitcnt lgkmcnt(11)\n"
  "v_mfma_f32_16x16x32_bf16 v[32:35], a[16:19], a[168:171], v[32:35]\n"
  "ds_read_b128 a[152:155], v16 offset:49152\n"
  "s_nop 0\n"
  "v_cndmask_b32_e64 v21, 0, v25, vcc\n"
  "v_mfma_f32_16x16x32_bf16 v[36:39], a[24:27], a[168:171], v[36:39]\n"
  "ds_read_b128 a[156:159], v17 offset:49152\n"
  "v_cmp_eq_u32 vcc, 0, v247\n"
  "v_mfma_f32_16x16x32_bf16 v[40:43], a[168:171], a[32:35], v[40:43]\n"
  "ds_read_b64_tr_b16 a[136:137], v19 offset:16384\n"
  "s_nop 0\n"
  "v_cndmask_b32 v21, v21, v23, vcc\n"
  "v_mfma_f32_16x16x32_bf16 v[44:47], a[168:171], a[40:43], v[44:47]\n"
  "ds_read_b64_tr_b16 a[138:139], v19 offset:16896\n"
  "v_mfma_f32_16x16x32_bf16 v[32:35], a[20:23], a[172:175], v[32:35]\n"
  "ds_read_b64_tr_b16 a[100:101], v18 offset:4096\n"
  "v_mfma_f32_16x16x32_bf16 v[36:39], a[28:31], a[172:175], v[36:39]\n"
  "ds_read_b64_tr_b16 a[102:103], v18 offset:4608\n"
  "v_mfma_f32_16x16x32_bf16 v[40:43], a[172:175], a[36:39], v[40:43]\n"
  "ds_read_b64_tr_b16 a[108:109], v19 offset:4096\n"
  "v_mfma_f32_16x16x32_bf16 v[44:47], a[172:175], a[44:47], v[44:47]\n"
  "ds_read_b64_tr_b16 a[110:111], v19 offset:4608\n"
  "ds_read_b64_tr_b16 a[116:117], v18 offset:12288\n"
  "ds_read_b64_tr_b16 a[118:119], v18 offset:12800\n"
  "ds_read_b64_tr_b16 a[124:125], v19 offset:12288\n"
  "ds_read_b64_tr_b16 a[126:127], v19 offset:12800\n"
  "ds_read_b64_tr_b16 a[132:133], v18 offset:20480\n"
  "ds_read_b64_tr_b16 a[134:135], v18 offset:20992\n"
  "ds_read_b64_tr_b16 a[140:141], v19 offset:20480\n"
  "ds_read_b64_tr_b16 a[142:143], v19 offset:20992\n"
  "s_waitcnt lgkmcnt(15)\n"
  "v_mfma_f32_16x16x32_bf16 v[32:35], a[112:115], a[144:147], v[32:35]\n"
  "ds_read_b128 a[160:163], v16 offset:2048\n"
  "s_waitcnt lgkmcnt(7)\n"
  "v_mfma_f32_16x16x32_bf16 v[32:35], a[116:119], a[148:151], v[32:35]\n"
  "ds_read_b128 a[164:167], v17 offset:2048\n"
  "v_mfma_f32_16x16x32_bf16 v[36:39], a[120:123], a[144:147], v[36:39]\n"
  "s_waitcnt lgkmcnt(6)\n"
  "v_mfma_f32_16x16x32_bf16 v[36:39], a[124:127], a[148:151], v[36:39]\n"
  "v_mfma_f32_16x16x32_bf16 v[40:43], a[144:147], a[128:131], v[40:43]\n"
  "s_waitcnt lgkmcnt(4)\n"
  "v_mfma_f32_16x16x32_bf16 v[40:43], a[148:151], a[132:135], v[40:43]\n"
  "v_mfma_f32_16x16x32_bf16 v[44:47], a[144:147], a[136:139], v[44:47]\n"
  "s_waitcnt lgkmcnt(2)\n"
  "v_mfma_f32_16x16x32_bf16 v[44:47], a[148:151], a[140:143], v[44:47]\n"
  "v_mfma_f32_16x16x32_bf16 v[192:195], a[48:51], a[152:155], v[192:195]\n"
  "ds_read_b128 a[168:171], v9 offset:2048\n"
  "v_mfma_f32_16x16x32_bf16 v[196:199], a[56:59], a[152:155], v[196:199]\n"
  "ds_read_b128 a[172:175], v10 offset:2048\n"
  "v_cvt_pk_bf16_f32 v252, v32, v33\n"
  "v_cvt_pk_bf16_f32 v253, v34, v35\n"
  "v_mfma_f32_16x16x32_bf16 v[200:203], a[64:67], a[152:155], v[200:203]\n"
  "v_cvt_pk_bf16_f32 v254, v36, v37\n"
  "v_cvt_pk_bf16_f32 v255, v38, v39\n"
  "v_mfma_f32_16x16x32_bf16 v[204:207], a[72:75], a[152:155], v[204:207]\n"
  "v_cvt_pk_bf16_f32 v0, v40, v41\n"
  "v_cvt_pk_bf16_f32 v1, v42, v43\n"
  "v_mfma_f32_16x16x32_bf16 v[208:211], a[152:155], a[80:83], v[208:211]\n"
  "v_cvt_pk_bf16_f32 v32, v44, v45\n"
  "v_cvt_pk_bf16_f32 v33, v46, v47\n"
  "v_mfma_f32_16x16x32_bf16 v[212:215], a[152:155], a[88:91], v[212:215]\n"
  "v_mfma_f32_16x16x32_bf16 v[192:195], a[52:55], a[156:159], v[192:195]\n"
  "v_mfma_f32_16x16x32_bf16 v[196:199], a[60:63], a[156:159], v[196:199]\n"
  "v_mfma_f32_16x16x32_bf16 v[200:203], a[68:71], a[156:159], v[200:203]\n"
  "v_mfma_f32_16x16x32_bf16 v[204:207], a[76:79], a[156:159], v[204:207]\n"
  "v_mfma_f32_16x16x32_bf16 v[208:211], a[156:159], a[84:87], v[208:211]\n"
  "v_mfma_f32_16x16x32_bf16 v[212:215], a[156:159], a[92:95], v[212:215]\n"
  "s_waitcnt lgkmcnt(2)\n"
  "v_mfma_f32_16x16x32_bf16 v[48:51], a[0:3], a[160:163], v[48:51]\n"
  "ds_read_b128 a[144:147], v16 offset:51200\n"
  "v_mfma_f32_16x16x32_bf16 v[52:55], a[8:11], a[160:163], v[52:55]\n"
  "ds_read_b128 a[148:151], v17 offset:51200\n"
  "v_mfma_f32_16x16x32_bf16 v[56:59], a[16:19], a[160:163], v[56:59]\n"
  "v_mfma_f32_16x16x32_bf16 v[60:63], a[24:27], a[160:163], v[60:63]\n"
  "v_mfma_f32_16x16x32_bf16 v[64:67], a[160:163], a[32:35], v[64:67]\n"
  "v_mfma_f32_16x16x32_bf16 v[68:71], a[160:163], a[40:43], v[68:71]\n"
  "v_mfma_f32_16x16x32_bf16 v[48:51], a[4:7], a[164:167], v[48:51]\n"
  "v_mfma_f32_16x16x32_bf16 v[52:55], a[12:15], a[164:167], v[52:55]\n"
  "v_mfma_f32_16x16x32_bf16 v[56:59], a[20:23], a[164:167], v[56:59]\n"
  "v_mfma_f32_16x16x32_bf16 v[60:63], a[28:31], a[164:167], v[60:63]\n"
  "v_mfma_f32_16x16x32_bf16 v[64:67], a[164:167], a[36:39], v[64:67]\n"
  "v_mfma_f32_16x16x32_bf16 v[68:71], a[164:167], a[44:47], v[68:71]\n"
  "s_waitcnt lgkmcnt(2)\n"
  "v_mfma_f32_16x16x32_bf16 v[48:51], a[96:99], a[168:171], v[48:51]\n"
  "ds_read_b128 a[152:155], v16 offset:4096\n"
  "v_mfma_f32_16x16x32_bf16 v[48:51], a[100:103], a[172:175], v[48:51]\n"
  "ds_read_b128 a[156:159], v17 offset:4096\n"
  "v_mfma_f32_16x16x32_bf16 v[52:55], a[104:107], a[168:171], v[52:55]\n"
  "v_mfma_f32_16x16x32_bf16 v[52:55], a[108:111], a[172:175], v[52:55]\n"
  "v_mfma_f32_16x16x32_bf16 v[56:59], a[112:115], a[168:171], v[56:59]\n"
  "v_mfma_f32_16x16x32_bf16 v[56:59], a[116:119], a[172:175], v[56:59]\n"
  "s_nop 2\n"
  "v_cvt_pk_bf16_f32 v48, v48, v49\n"
  "v_cvt_pk_bf16_f32 v49, v50, v51\n"
  "v_mfma_f32_16x16x32_bf16 v[60:63], a[120:123], a[168:171], v[60:63]\n"
  "v_mfma_f32_16x16x32_bf16 v[60:63], a[124:127], a[172:175], v[60:63]\n"
  "v_cvt_pk_bf16_f32 v50, v52, v53\n"
  "v_cvt_pk_bf16_f32 v51, v54, v55\n"
  "v_mfma_f32_16x16x32_bf16 v[64:67], a[168:171], a[128:131], v[64:67]\n"
  "v_mfma_f32_16x16x32_bf16 v[64:67], a[172:175], a[132:135], v[64:67]\n"
  "v_cvt_pk_bf16_f32 v56, v56, v57\n"
  "v_cvt_pk_bf16_f32 v57, v58, v59\n"
  "v_mfma_f32_16x16x32_bf16 v[68:71], a[168:171], a[136:139], v[68:71]\n"
  "v_mfma_f32_16x16x32_bf16 v[68:71], a[172:175], a[140:143], v[68:71]\n"
  "v_cvt_pk_bf16_f32 v58, v60, v61\n"
  "v_cvt_pk_bf16_f32 v59, v62, v63\n"
  "s_waitcnt lgkmcnt(2)\n"
  "v_mfma_f32_16x16x32_bf16 v[216:219], a[48:51], a[144:147], v[216:219]\n"
  "ds_read_b128 a[160:163], v9 offset:4096\n"
  "v_mfma_f32_16x16x32_bf16 v[52:55], v[252:255], v[48:51], v[248:251]\n"
  "v_mfma_f32_16x16x32_bf16 v[60:63], v[56:59], v[48:51], v[244:247]\n"
  "v_mfma_f32_16x16x32_bf16 v[220:223], a[56:59], a[144:147], v[220:223]\n"
  "ds_read_b128 a[164:167], v10 offset:4096\n"
  "v_cvt_pk_bf16_f32 v2, v64, v65\n"
  "v_cvt_pk_bf16_f32 v3, v66, v67\n"
  "v_mfma_f32_16x16x32_bf16 v[224:227], a[64:67], a[144:147], v[224:227]\n"
  "v_mfma_f32_16x16x32_bf16 v[228:231], a[72:75], a[144:147], v[228:231]\n"
  "v_cvt_pk_bf16_f32 v34, v68, v69\n"
  "v_cvt_pk_bf16_f32 v35, v70, v71\n"
  "v_mfma_f32_16x16x32_bf16 v[232:235], a[144:147], a[80:83], v[232:235]\n"
  "v_max_f32 v52, v52, v53\n"
  "v_max_f32 v54, v54, v55\n"
  "v_mfma_f32_16x16x32_bf16 v[236:239], a[144:147], a[88:91], v[236:239]\n"
  "v_max3_f32 v52, v52, v60, v61\n"
  "v_max3_f32 v54, v54, v62, v63\n"
  "v_mfma_f32_16x16x32_bf16 v[216:219], a[52:55], a[148:151], v[216:219]\n"
  "v_max_f32 v23, v52, v54\n"
  "v_max_f32 v25, v52, v54\n"
  "v_mfma_f32_16x16x32_bf16 v[220:223], a[60:63], a[148:151], v[220:223]\n"
  "s_nop 0\n"
  "v_permlane16_swap_b32 v23, v25\n"
  "v_mfma_f32_16x16x32_bf16 v[224:227], a[68:71], a[148:151], v[224:227]\n"
  "v_max_f32 v23, v23, v25\n"
  "v_mov_b32 v25, v23\n"
  "v_mfma_f32_16x16x32_bf16 v[228:231], a[76:79], a[148:151], v[228:231]\n"
  "s_nop 0\n"
  "v_permlane32_swap_b32 v23, v25\n"
  "v_mfma_f32_16x16x32_bf16 v[232:235], a[148:151], a[84:87], v[232:235]\n"
  "v_max_f32 v23, v23, v25\n"
  "v_mul_f32 v23, s71, v23\n"
  "v_mfma_f32_16x16x32_bf16 v[236:239], a[148:151], a[92:95], v[236:239]\n"
  "v_fma_f32 v52, v52, s71, -v23\n"
  "v_fma_f32 v54, v54, s71, -v23\n"
  "s_waitcnt lgkmcnt(2)\n"
  "v_mfma_f32_16x16x32_bf16 v[72:75], a[0:3], a[152:155], v[72:75]\n"
  "ds_read_b128 a[168:171], v16 offset:6144\n"
  "v_exp_f32 v52, v52\n"
  "v_mfma_f32_16x16x32_bf16 v[76:79], a[8:11], a[152:155], v[76:79]\n"
  "ds_read_b128 a[172:175], v17 offset:6144\n"
  "v_exp_f32 v54, v54\n"
  "v_mfma_f32_16x16x32_bf16 v[80:83], a[16:19], a[152:155], v[80:83]\n"
  "v_cvt_pk_bf16_f32 v52, v52, v52\n"
  "v_cvt_pk_bf16_f32 v54, v54, v54\n"
  "v_mfma_f32_16x16x32_bf16 v[84:87], a[24:27], a[152:155], v[84:87]\n"
  "v_and_b32 v64, v8, v52\n"
  "v_and_b32 v65, v11, v54\n"
  "v_mfma_f32_16x16x32_bf16 v[88:91], a[152:155], a[32:35], v[88:91]\n"
  "v_and_b32 v66, v20, v52\n"
  "v_and_b32 v67, v21, v54\n"
  "v_mfma_f32_16x16x32_bf16 v[92:95], a[152:155], a[40:43], v[92:95]\n"
  "s_nop 0\n"
  "v_mfma_f32_16x16x32_bf16 v[60:63], v[12:15], v[64:67], 0\n"
  "v_mfma_f32_16x16x32_bf16 v[68:71], v[0:3], v[64:67], 0\n"
  "v_mfma_f32_16x16x32_bf16 v[72:75], a[4:7], a[156:159], v[72:75]\n"
  "v_mfma_f32_16x16x32_bf16 v[52:55], v[32:35], v[64:67], 0\n"
  "v_mov_b64 v[4:5], v[2:3]\n"
  "v_mfma_f32_16x16x32_bf16 v[76:79], a[12:15], a[156:159], v[76:79]\n"
  "v_mov_b64 v[36:37], v[34:35]\n"
  "v_mfma_f32_16x16x32_bf16 v[80:83], a[20:23], a[156:159], v[80:83]\n"
  "v_mfma_f32_16x16x32_bf16 v[84:87], a[28:31], a[156:159], v[84:87]\n"
  "v_rcp_f32 v60, v60\n"
  "v_mfma_f32_16x16x32_bf16 v[88:91], a[156:159], a[36:39], v[88:91]\n"
  "v_mul_f32 v68, v60, v68\n"
  "v_mul_f32 v69, v60, v69\n"
  "v_mfma_f32_16x16x32_bf16 v[92:95], a[156:159], a[44:47], v[92:95]\n"
  "v_mul_f32 v70, v60, v70\n"
  "v_mul_f32 v71, v60, v71\n"
  "s_waitcnt lgkmcnt(2)\n"
  "v_mfma_f32_16x16x32_bf16 v[72:75], a[96:99], a[160:163], v[72:75]\n"
  "ds_read_b128 a[144:147], v9 offset:6144\n"
  "v_mul_f32 v52, v60, v52\n"
  "v_mul_f32 v53, v60, v53\n"
  "v_mfma_f32_16x16x32_bf16 v[72:75], a[100:103], a[164:167], v[72:75]\n"
  "ds_read_b128 a[148:151], v10 offset:6144\n"
  "v_mul_f32 v54, v60, v54\n"
  "v_mul_f32 v55, v60, v55\n"
  "v_mfma_f32_16x16x32_bf16 v[76:79], a[104:107], a[160:163], v[76:79]\n"
  "v_cvt_pk_bf16_f32 v48, v68, v69\n"
  "v_cvt_pk_bf16_f32 v49, v70, v71\n"
  "v_mfma_f32_16x16x32_bf16 v[76:79], a[108:111], a[164:167], v[76:79]\n"
  "v_cvt_pk_bf16_f32 v50, v52, v53\n"
  "v_cvt_pk_bf16_f32 v51, v54, v55\n"
  "v_mfma_f32_16x16x32_bf16 v[80:83], a[112:115], a[160:163], v[80:83]\n"
  "buffer_store_dwordx4 v[48:51], v26, s[28:31], s33 offen offset:0 sc1\n"
  "v_mfma_f32_16x16x32_bf16 v[80:83], a[116:119], a[164:167], v[80:83]\n"
  "v_cvt_pk_bf16_f32 v72, v72, v73\n"
  "v_cvt_pk_bf16_f32 v73, v74, v75\n"
  "v_mfma_f32_16x16x32_bf16 v[84:87], a[120:123], a[160:163], v[84:87]\n"
  "v_mfma_f32_16x16x32_bf16 v[84:87], a[124:127], a[164:167], v[84:87]\n"
  "v_cvt_pk_bf16_f32 v74, v76, v77\n"
  "v_cvt_pk_bf16_f32 v75, v78, v79\n"
  "v_mfma_f32_16x16x32_bf16 v[88:91], a[160:163], a[128:131], v[88:91]\n"
  "v_mfma_f32_16x16x32_bf16 v[88:91], a[164:167], a[132:135], v[88:91]\n"
  "v_cvt_pk_bf16_f32 v80, v80, v81\n"
  "v_cvt_pk_bf16_f32 v81, v82, v83\n"
  "v_mfma_f32_16x16x32_bf16 v[92:95], a[160:163], a[136:139], v[92:95]\n"
  "v_mfma_f32_16x16x32_bf16 v[92:95], a[164:167], a[140:143], v[92:95]\n"
  "v_cvt_pk_bf16_f32 v82, v84, v85\n"
  "v_cvt_pk_bf16_f32 v83, v86, v87\n"
  "s_waitcnt lgkmcnt(2)\n"
  "v_mfma_f32_16x16x32_bf16 v[96:99], a[0:3], a[168:171], v[96:99]\n"
  "ds_read_b128 a[152:155], v16 offset:8192\n"
  "v_mfma_f32_16x16x32_bf16 v[76:79], v[56:59], v[72:75], v[240:243]\n"
  "v_mfma_f32_16x16x32_bf16 v[84:87], v[80:83], v[72:75], v[244:247]\n"
  "v_mfma_f32_16x16x32_bf16 v[100:103], a[8:11], a[168:171], v[100:103]\n"
  "ds_read_b128 a[156:159], v17 offset:8192\n"
  "v_cvt_pk_bf16_f32 v6, v88, v89\n"
  "v_cvt_pk_bf16_f32 v7, v90, v91\n"
  "v_mfma_f32_16x16x32_bf16 v[104:107], a[16:19], a[168:171], v[104:107]\n"
  "v_mfma_f32_16x16x32_bf16 v[108:111], a[24:27], a[168:171], v[108:111]\n"
  "v_cvt_pk_bf16_f32 v38, v92, v93\n"
  "v_cvt_pk_bf16_f32 v39, v94, v95\n"
  "v_mfma_f32_16x16x32_bf16 v[112:115], a[168:171], a[32:35], v[112:115]\n"
  "v_max_f32 v76, v76, v77\n"
  "v_max_f32 v78, v78, v79\n"
  "v_mfma_f32_16x16x32_bf16 v[116:119], a[168:171], a[40:43], v[116:119]\n"
  "v_max3_f32 v76, v76, v84, v85\n"
  "v_max3_f32 v78, v78, v86, v87\n"
  "v_mfma_f32_16x16x32_bf16 v[96:99], a[4:7], a[172:175], v[96:99]\n"
  "v_max_f32 v22, v76, v78\n"
  "v_max_f32 v24, v76, v78\n"
  "v_mfma_f32_16x16x32_bf16 v[100:103], a[12:15], a[172:175], v[100:103]\n"
  "s_nop 0\n"
  "v_permlane16_swap_b32 v22, v24\n"
  "v_mfma_f32_16x16x32_bf16 v[104:107], a[20:23], a[172:175], v[104:107]\n"
  "v_max_f32 v22, v22, v24\n"
  "v_mov_b32 v24, v22\n"
  "v_mfma_f32_16x16x32_bf16 v[108:111], a[28:31], a[172:175], v[108:111]\n"
  "s_nop 0\n"
  "v_permlane32_swap_b32 v22, v24\n"
  "v_mfma_f32_16x16x32_bf16 v[112:115], a[172:175], a[36:39], v[112:115]\n"
  "v_max_f32 v22, v22, v24\n"
  "v_mul_f32 v22, s71, v22\n"
  "v_mfma_f32_16x16x32_bf16 v[116:119], a[172:175], a[44:47], v[116:119]\n"
  "v_fma_f32 v76, v76, s71, -v22\n"
  "v_fma_f32 v78, v78, s71, -v22\n"
  "s_waitcnt lgkmcnt(2)\n"
  "v_mfma_f32_16x16x32_bf16 v[96:99], a[96:99], a[144:147], v[96:99]\n"
  "ds_read_b128 a[160:163], v9 offset:8192\n"
  "v_exp_f32 v76, v76\n"
  "v_mfma_f32_16x16x32_bf16 v[96:99], a[100:103], a[148:151], v[96:99]\n"
  "ds_read_b128 a[164:167], v10 offset:8192\n"
  "v_exp_f32 v78, v78\n"
  "v_mfma_f32_16x16x32_bf16 v[100:103], a[104:107], a[144:147], v[100:103]\n"
  "v_cvt_pk_bf16_f32 v76, v76, v76\n"
  "v_cvt_pk_bf16_f32 v78, v78, v78\n"
  "v_mfma_f32_16x16x32_bf16 v[100:103], a[108:111], a[148:151], v[100:103]\n"
  "v_and_b32 v88, v8, v76\n"
  "v_and_b32 v89, v11, v78\n"
  "v_mfma_f32_16x16x32_bf16 v[104:107], a[112:115], a[144:147], v[104:107]\n"
  "v_and_b32 v90, v20, v76\n"
  "v_and_b32 v91, v21, v78\n"
  "v_mfma_f32_16x16x32_bf16 v[104:107], a[116:119], a[148:151], v[104:107]\n"
  "s_nop 0\n"
  "v_mfma_f32_16x16x32_bf16 v[84:87], v[12:15], v[88:91], 0\n"
  "v_mfma_f32_16x16x32_bf16 v[92:95], v[4:7], v[88:91], 0\n"
  "v_mfma_f32_16x16x32_bf16 v[108:111], a[120:123], a[144:147], v[108:111]\n"
  "v_mfma_f32_16x16x32_bf16 v[76:79], v[36:39], v[88:91], 0\n"
  "v_mov_b64 v[0:1], v[6:7]\n"
  "v_mfma_f32_16x16x32_bf16 v[108:111], a[124:127], a[148:151], v[108:111]\n"
  "v_mov_b64 v[32:33], v[38:39]\n"
  "v_cvt_pk_bf16_f32 v96, v96, v97\n"
  "v_mfma_f32_16x16x32_bf16 v[112:115], a[144:147], a[128:131], v[112:115]\n"
  "v_cvt_pk_bf16_f32 v97, v98, v99\n"
  "v_cvt_pk_bf16_f32 v98, v100, v101\n"
  "v_mfma_f32_16x16x32_bf16 v[112:115], a[148:151], a[132:135], v[112:115]\n"
  "v_rcp_f32 v84, v84\n"
  "v_mfma_f32_16x16x32_bf16 v[116:119], a[144:147], a[136:139], v[116:119]\n"
  "v_mul_f32 v92, v84, v92\n"
  "v_mul_f32 v93, v84, v93\n"
  "v_mfma_f32_16x16x32_bf16 v[116:119], a[148:151], a[140:143], v[116:119]\n"
  "v_mul_f32 v94, v84, v94\n"
  "v_mul_f32 v95, v84, v95\n"
  "s_waitcnt lgkmcnt(2)\n"
  "v_mfma_f32_16x16x32_bf16 v[120:123], a[0:3], a[152:155], v[120:123]\n"
  "ds_read_b128 a[168:171], v16 offset:10240\n"
  "v_mul_f32 v76, v84, v76\n"
  "v_mul_f32 v77, v84, v77\n"
  "v_mfma_f32_16x16x32_bf16 v[124:127], a[8:11], a[152:155], v[124:127]\n"
  "ds_read_b128 a[172:175], v17 offset:10240\n"
  "v_mul_f32 v78, v84, v78\n"
  "v_mul_f32 v79, v84, v79\n"
  "v_mfma_f32_16x16x32_bf16 v[128:131], a[16:19], a[152:155], v[128:131]\n"
  "v_cvt_pk_bf16_f32 v72, v92, v93\n"
  "v_cvt_pk_bf16_f32 v73, v94, v95\n"
  "v_mfma_f32_16x16x32_bf16 v[132:135], a[24:27], a[152:155], v[132:135]\n"
  "v_cvt_pk_bf16_f32 v74, v76, v77\n"
  "v_cvt_pk_bf16_f32 v75, v78, v79\n"
  "v_mfma_f32_16x16x32_bf16 v[136:139], a[152:155], a[32:35], v[136:139]\n"
  "buffer_store_dwordx4 v[72:75], v26, s[28:31], s33 offen offset:2048 sc1\n"
  "v_cvt_pk_bf16_f32 v99, v102, v103\n"
  "v_mfma_f32_16x16x32_bf16 v[140:143], a[152:155], a[40:43], v[140:143]\n"
  "v_cvt_pk_bf16_f32 v104, v104, v105\n"
  "v_cvt_pk_bf16_f32 v105, v106, v107\n"
  "v_mfma_f32_16x16x32_bf16 v[120:123], a[4:7], a[156:159], v[120:123]\n"
  "v_cvt_pk_bf16_f32 v106, v108, v109\n"
  "v_cvt_pk_bf16_f32 v107, v110, v111\n"
  "v_mfma_f32_16x16x32_bf16 v[124:127], a[12:15], a[156:159], v[124:127]\n"
  "v_mfma_f32_16x16x32_bf16 v[100:103], v[80:83], v[96:99], v[240:243]\n"
  "v_mfma_f32_16x16x32_bf16 v[108:111], v[104:107], v[96:99], v[244:247]\n"
  "v_mfma_f32_16x16x32_bf16 v[128:131], a[20:23], a[156:159], v[128:131]\n"
  "v_cvt_pk_bf16_f32 v2, v112, v113\n"
  "v_cvt_pk_bf16_f32 v3, v114, v115\n"
  "v_mfma_f32_16x16x32_bf16 v[132:135], a[28:31], a[156:159], v[132:135]\n"
  "v_cvt_pk_bf16_f32 v34, v116, v117\n"
  "v_cvt_pk_bf16_f32 v35, v118, v119\n"
  "v_mfma_f32_16x16x32_bf16 v[136:139], a[156:159], a[36:39], v[136:139]\n"
  "v_max_f32 v100, v100, v101\n"
  "v_max_f32 v102, v102, v103\n"
  "v_mfma_f32_16x16x32_bf16 v[140:143], a[156:159], a[44:47], v[140:143]\n"
  "v_max3_f32 v100, v100, v108, v109\n"
  "v_max3_f32 v102, v102, v110, v111\n"
  "s_waitcnt lgkmcnt(2)\n"
  "v_mfma_f32_16x16x32_bf16 v[120:123], a[96:99], a[160:163], v[120:123]\n"
  "ds_read_b128 a[144:147], v9 offset:10240\n"
  "v_max_f32 v23, v100, v102\n"
  "v_max_f32 v25, v100, v102\n"
  "v_mfma_f32_16x16x32_bf16 v[120:123], a[100:103], a[164:167], v[120:123]\n"
  "ds_read_b128 a[148:151], v10 offset:10240\n"
  "v_permlane16_swap_b32 v23, v25\n"
  "v_mfma_f32_16x16x32_bf16 v[124:127], a[104:107], a[160:163], v[124:127]\n"
  "v_max_f32 v23, v23, v25\n"
  "v_mov_b32 v25, v23\n"
  "v_mfma_f32_16x16x32_bf16 v[124:127], a[108:111], a[164:167], v[124:127]\n"
  "s_nop 0\n"
  "v_permlane32_swap_b32 v23, v25\n"
  "v_mfma_f32_16x16x32_bf16 v[128:131], a[112:115], a[160:163], v[128:131]\n"
  "v_max_f32 v23, v23, v25\n"
  "v_mul_f32 v23, s71, v23\n"
  "v_mfma_f32_16x16x32_bf16 v[128:131], a[116:119], a[164:167], v[128:131]\n"
  "v_fma_f32 v100, v100, s71, -v23\n"
  "v_fma_f32 v102, v102, s71, -v23\n"
  "v_mfma_f32_16x16x32_bf16 v[132:135], a[120:123], a[160:163], v[132:135]\n"
  "v_exp_f32 v100, v100\n"
  "v_mfma_f32_16x16x32_bf16 v[132:135], a[124:127], a[164:167], v[132:135]\n"
  "v_exp_f32 v102, v102\n"
  "v_mfma_f32_16x16x32_bf16 v[136:139], a[160:163], a[128:131], v[136:139]\n"
  "v_cvt_pk_bf16_f32 v100, v100, v100\n"
  "v_cvt_pk_bf16_f32 v102, v102, v102\n"
  "v_mfma_f32_16x16x32_bf16 v[136:139], a[164:167], a[132:135], v[136:139]\n"
  "v_and_b32 v112, v8, v100\n"
  "v_and_b32 v113, v11, v102\n"
  "v_mfma_f32_16x16x32_bf16 v[140:143], a[160:163], a[136:139], v[140:143]\n"
  "v_and_b32 v114, v20, v100\n"
  "v_and_b32 v115, v21, v102\n"
  "v_mfma_f32_16x16x32_bf16 v[140:143], a[164:167], a[140:143], v[140:143]\n"
  "s_nop 0\n"
  "v_mfma_f32_16x16x32_bf16 v[108:111], v[12:15], v[112:115], 0\n"
  "v_mfma_f32_16x16x32_bf16 v[116:119], v[0:3], v[112:115], 0\n"
  "s_waitcnt lgkmcnt(2)\n"
  "v_mfma_f32_16x16x32_bf16 v[144:147], a[0:3], a[168:171], v[144:147]\n"
  "ds_read_b128 a[152:155], v16 offset:12288\n"
  "v_mfma_f32_16x16x32_bf16 v[100:103], v[32:35], v[112:115], 0\n"
  "v_mov_b64 v[4:5], v[2:3]\n"
  "v_mfma_f32_16x16x32_bf16 v[148:151], a[8:11], a[168:171], v[148:151]\n"
  "ds_read_b128 a[156:159], v17 offset:12288\n"
  "v_mov_b64 v[36:37], v[34:35]\n"
  "v_cvt_pk_bf16_f32 v120, v120, v121\n"
  "v_mfma_f32_16x16x32_bf16 v[152:155], a[16:19], a[168:171], v[152:155]\n"
  "v_cvt_pk_bf16_f32 v121, v122, v123\n"
  "v_cvt_pk_bf16_f32 v122, v124, v125\n"
  "v_mfma_f32_16x16x32_bf16 v[156:159], a[24:27], a[168:171], v[156:159]\n"
  "v_rcp_f32 v108, v108\n"
  "v_mfma_f32_16x16x32_bf16 v[160:163], a[168:171], a[32:35], v[160:163]\n"
  "v_mul_f32 v116, v108, v116\n"
  "v_mul_f32 v117, v108, v117\n"
  "v_mfma_f32_16x16x32_bf16 v[164:167], a[168:171], a[40:43], v[164:167]\n"
  "v_mul_f32 v118, v108, v118\n"
  "v_mul_f32 v119, v108, v119\n"
  "v_mfma_f32_16x16x32_bf16 v[144:147], a[4:7], a[172:175], v[144:147]\n"
  "v_mul_f32 v100, v108, v100\n"
  "v_mul_f32 v101, v108, v101\n"
  "v_mfma_f32_16x16x32_bf16 v[148:151], a[12:15], a[172:175], v[148:151]\n"
  "v_mul_f32 v102, v108, v102\n"
  "v_mul_f32 v103, v108, v103\n"
  "v_mfma_f32_16x16x32_bf16 v[152:155], a[20:23], a[172:175], v[152:155]\n"
  "v_cvt_pk_bf16_f32 v96, v116, v117\n"
  "v_cvt_pk_bf16_f32 v97, v118, v119\n"
  "v_mfma_f32_16x16x32_bf16 v[156:159], a[28:31], a[172:175], v[156:159]\n"
  "v_cvt_pk_bf16_f32 v98, v100, v101\n"
  "v_cvt_pk_bf16_f32 v99, v102, v103\n"
  "v_mfma_f32_16x16x32_bf16 v[160:163], a[172:175], a[36:39], v[160:163]\n"
  "buffer_store_dwordx4 v[96:99], v26, s[28:31], s34 offen offset:0 sc1\n"
  "v_cvt_pk_bf16_f32 v123, v126, v127\n"
  "v_mfma_f32_16x16x32_bf16 v[164:167], a[172:175], a[44:47], v[164:167]\n"
  "v_cvt_pk_bf16_f32 v128, v128, v129\n"
  "v_cvt_pk_bf16_f32 v129, v130, v131\n"
  "s_waitcnt lgkmcnt(2)\n"
  "v_mfma_f32_16x16x32_bf16 v[144:147], a[96:99], a[144:147], v[144:147]\n"
  "ds_read_b128 a[160:163], v9 offset:12288\n"
  "v_cvt_pk_bf16_f32 v130, v132, v133\n"
  "v_cvt_pk_bf16_f32 v131, v134, v135\n"
  "v_mfma_f32_16x16x32_bf16 v[144:147], a[100:103], a[148:151], v[144:147]\n"
  "ds_read_b128 a[164:167], v10 offset:12288\n"
  "v_mfma_f32_16x16x32_bf16 v[124:127], v[104:107], v[120:123], v[240:243]\n"
  "v_mfma_f32_16x16x32_bf16 v[132:135], v[128:131], v[120:123], v[244:247]\n"
  "v_mfma_f32_16x16x32_bf16 v[148:151], a[104:107], a[144:147], v[148:151]\n"
  "v_cvt_pk_bf16_f32 v6, v136, v137\n"
  "v_cvt_pk_bf16_f32 v7, v138, v139\n"
  "v_mfma_f32_16x16x32_bf16 v[148:151], a[108:111], a[148:151], v[148:151]\n"
  "v_cvt_pk_bf16_f32 v38, v140, v141\n"
  "v_cvt_pk_bf16_f32 v39, v142, v143\n"
  "v_mfma_f32_16x16x32_bf16 v[152:155], a[112:115], a[144:147], v[152:155]\n"
  "v_max_f32 v124, v124, v125\n"
  "v_max_f32 v126, v126, v127\n"
  "v_mfma_f32_16x16x32_bf16 v[152:155], a[116:119], a[148:151], v[152:155]\n"
  "v_max3_f32 v124, v124, v132, v133\n"
  "v_max3_f32 v126, v126, v134, v135\n"
  "v_mfma_f32_16x16x32_bf16 v[156:159], a[120:123], a[144:147], v[156:159]\n"
  "v_max_f32 v22, v124, v126\n"
  "v_max_f32 v24, v124, v126\n"
  "v_mfma_f32_16x16x32_bf16 v[156:159], a[124:127], a[148:151], v[156:159]\n"
  "s_nop 0\n"
  "v_permlane16_swap_b32 v22, v24\n"
  "v_mfma_f32_16x16x32_bf16 v[160:163], a[144:147], a[128:131], v[160:163]\n"
  "v_max_f32 v22, v22, v24\n"
  "v_mov_b32 v24, v22\n"
  "v_mfma_f32_16x16x32_bf16 v[160:163], a[148:151], a[132:135], v[160:163]\n"
  "s_nop 0\n"
  "v_permlane32_swap_b32 v22, v24\n"
  "v_mfma_f32_16x16x32_bf16 v[164:167], a[144:147], a[136:139], v[164:167]\n"
  "v_max_f32 v22, v22, v24\n"
  "v_mul_f32 v22, s71, v22\n"
  "v_mfma_f32_16x16x32_bf16 v[164:167], a[148:151], a[140:143], v[164:167]\n"
  "v_fma_f32 v124, v124, s71, -v22\n"
  "v_fma_f32 v126, v126, s71, -v22\n"
  "s_waitcnt lgkmcnt(2)\n"
  "v_mfma_f32_16x16x32_bf16 v[168:171], a[0:3], a[152:155], v[168:171]\n"
  "ds_read_b128 a[168:171], v16 offset:14336\n"
  "v_exp_f32 v124, v124\n"
  "v_mfma_f32_16x16x32_bf16 v[172:175], a[8:11], a[152:155], v[172:175]\n"
  "ds_read_b128 a[172:175], v17 offset:14336\n"
  "v_exp_f32 v126, v126\n"
  "v_mfma_f32_16x16x32_bf16 v[176:179], a[16:19], a[152:155], v[176:179]\n"
  "v_cvt_pk_bf16_f32 v124, v124, v124\n"
  "v_cvt_pk_bf16_f32 v126, v126, v126\n"
  "v_mfma_f32_16x16x32_bf16 v[180:183], a[24:27], a[152:155], v[180:183]\n"
  "v_and_b32 v136, v8, v124\n"
  "v_and_b32 v137, v11, v126\n"
  "v_mfma_f32_16x16x32_bf16 v[184:187], a[152:155], a[32:35], v[184:187]\n"
  "v_and_b32 v138, v20, v124\n"
  "v_and_b32 v139, v21, v126\n"
  "v_mfma_f32_16x16x32_bf16 v[188:191], a[152:155], a[40:43], v[188:191]\n"
  "s_nop 0\n"
  "v_mfma_f32_16x16x32_bf16 v[132:135], v[12:15], v[136:139], 0\n"
  "v_mfma_f32_16x16x32_bf16 v[140:143], v[4:7], v[136:139], 0\n"
  "v_mfma_f32_16x16x32_bf16 v[168:171], a[4:7], a[156:159], v[168:171]\n"
  "v_mfma_f32_16x16x32_bf16 v[124:127], v[36:39], v[136:139], 0\n"
  "v_mov_b64 v[0:1], v[6:7]\n"
  "v_mfma_f32_16x16x32_bf16 v[172:175], a[12:15], a[156:159], v[172:175]\n"
  "v_mov_b64 v[32:33], v[38:39]\n"
  "v_cvt_pk_bf16_f32 v144, v144, v145\n"
  "v_mfma_f32_16x16x32_bf16 v[176:179], a[20:23], a[156:159], v[176:179]\n"
  "v_cvt_pk_bf16_f32 v145, v146, v147\n"
  "v_cvt_pk_bf16_f32 v146, v148, v149\n"
  "v_mfma_f32_16x16x32_bf16 v[180:183], a[28:31], a[156:159], v[180:183]\n"
  "v_rcp_f32 v132, v132\n"
  "v_mfma_f32_16x16x32_bf16 v[184:187], a[156:159], a[36:39], v[184:187]\n"
  "v_mul_f32 v140, v132, v140\n"
  "v_mul_f32 v141, v132, v141\n"
  "v_mfma_f32_16x16x32_bf16 v[188:191], a[156:159], a[44:47], v[188:191]\n"
  "v_mul_f32 v142, v132, v142\n"
  "v_mul_f32 v143, v132, v143\n"
  "s_waitcnt lgkmcnt(2)\n"
  "v_mfma_f32_16x16x32_bf16 v[168:171], a[96:99], a[160:163], v[168:171]\n"
  "ds_read_b128 a[144:147], v9 offset:14336\n"
  "v_mul_f32 v124, v132, v124\n"
  "v_mul_f32 v125, v132, v125\n"
  "v_mfma_f32_16x16x32_bf16 v[168:171], a[100:103], a[164:167], v[168:171]\n"
  "ds_read_b128 a[148:151], v10 offset:14336\n"
  "v_mul_f32 v126, v132, v126\n"
  "v_mul_f32 v127, v132, v127\n"
  "v_mfma_f32_16x16x32_bf16 v[172:175], a[104:107], a[160:163], v[172:175]\n"
  "v_cvt_pk_bf16_f32 v120, v140, v141\n"
  "v_cvt_pk_bf16_f32 v121, v142, v143\n"
  "v_mfma_f32_16x16x32_bf16 v[172:175], a[108:111], a[164:167], v[172:175]\n"
  "v_cvt_pk_bf16_f32 v122, v124, v125\n"
  "v_cvt_pk_bf16_f32 v123, v126, v127\n"
  "v_mfma_f32_16x16x32_bf16 v[176:179], a[112:115], a[160:163], v[176:179]\n"
  "buffer_store_dwordx4 v[120:123], v26, s[28:31], s34 offen offset:2048 sc1\n"
  "v_cvt_pk_bf16_f32 v147, v150, v151\n"
  "v_mfma_f32_16x16x32_bf16 v[176:179], a[116:119], a[164:167], v[176:179]\n"
  "v_cvt_pk_bf16_f32 v152, v152, v153\n"
  "v_cvt_pk_bf16_f32 v153, v154, v155\n"
  "v_mfma_f32_16x16x32_bf16 v[180:183], a[120:123], a[160:163], v[180:183]\n"
  "v_cvt_pk_bf16_f32 v154, v156, v157\n"
  "v_cvt_pk_bf16_f32 v155, v158, v159\n"
  "v_mfma_f32_16x16x32_bf16 v[180:183], a[124:127], a[164:167], v[180:183]\n"
  "v_mfma_f32_16x16x32_bf16 v[148:151], v[128:131], v[144:147], v[240:243]\n"
  "v_mfma_f32_16x16x32_bf16 v[156:159], v[152:155], v[144:147], v[244:247]\n"
  "v_mfma_f32_16x16x32_bf16 v[184:187], a[160:163], a[128:131], v[184:187]\n"
  "v_cvt_pk_bf16_f32 v2, v160, v161\n"
  "v_cvt_pk_bf16_f32 v3, v162, v163\n"
  "v_mfma_f32_16x16x32_bf16 v[184:187], a[164:167], a[132:135], v[184:187]\n"
  "v_cvt_pk_bf16_f32 v34, v164, v165\n"
  "v_cvt_pk_bf16_f32 v35, v166, v167\n"
  "v_mfma_f32_16x16x32_bf16 v[188:191], a[160:163], a[136:139], v[188:191]\n"
  "v_max_f32 v148, v148, v149\n"
  "v_max_f32 v150, v150, v151\n"
  "v_mfma_f32_16x16x32_bf16 v[188:191], a[164:167], a[140:143], v[188:191]\n"
  "v_max3_f32 v148, v148, v156, v157\n"
  "v_max3_f32 v150, v150, v158, v159\n"
  "s_waitcnt lgkmcnt(2)\n"
  "v_mfma_f32_16x16x32_bf16 v[192:195], a[0:3], a[168:171], v[192:195]\n"
  "ds_read_b128 a[152:155], v16 offset:16384\n"
  "v_max_f32 v23, v148, v150\n"
  "v_max_f32 v25, v148, v150\n"
  "v_mfma_f32_16x16x32_bf16 v[196:199], a[8:11], a[168:171], v[196:199]\n"
  "ds_read_b128 a[156:159], v17 offset:16384\n"
  "v_permlane16_swap_b32 v23, v25\n"
  "v_mfma_f32_16x16x32_bf16 v[200:203], a[16:19], a[168:171], v[200:203]\n"
  "v_max_f32 v23, v23, v25\n"
  "v_mov_b32 v25, v23\n"
  "v_mfma_f32_16x16x32_bf16 v[204:207], a[24:27], a[168:171], v[204:207]\n"
  "s_nop 0\n"
  "v_permlane32_swap_b32 v23, v25\n"
  "v_mfma_f32_16x16x32_bf16 v[208:211], a[168:171], a[32:35], v[208:211]\n"
  "v_max_f32 v23, v23, v25\n"
  "v_mul_f32 v23, s71, v23\n"
  "v_mfma_f32_16x16x32_bf16 v[212:215], a[168:171], a[40:43], v[212:215]\n"
  "v_fma_f32 v148, v148, s71, -v23\n"
  "v_fma_f32 v150, v150, s71, -v23\n"
  "v_mfma_f32_16x16x32_bf16 v[192:195], a[4:7], a[172:175], v[192:195]\n"
  "v_exp_f32 v148, v148\n"
  "v_mfma_f32_16x16x32_bf16 v[196:199], a[12:15], a[172:175], v[196:199]\n"
  "v_exp_f32 v150, v150\n"
  "v_mfma_f32_16x16x32_bf16 v[200:203], a[20:23], a[172:175], v[200:203]\n"
  "v_cvt_pk_bf16_f32 v148, v148, v148\n"
  "v_cvt_pk_bf16_f32 v150, v150, v150\n"
  "v_mfma_f32_16x16x32_bf16 v[204:207], a[28:31], a[172:175], v[204:207]\n"
  "v_and_b32 v160, v8, v148\n"
  "v_and_b32 v161, v11, v150\n"
  "v_mfma_f32_16x16x32_bf16 v[208:211], a[172:175], a[36:39], v[208:211]\n"
  "v_and_b32 v162, v20, v148\n"
  "v_and_b32 v163, v21, v150\n"
  "v_mfma_f32_16x16x32_bf16 v[212:215], a[172:175], a[44:47], v[212:215]\n"
  "s_nop 0\n"
  "v_mfma_f32_16x16x32_bf16 v[156:159], v[12:15], v[160:163], 0\n"
  "v_mfma_f32_16x16x32_bf16 v[164:167], v[0:3], v[160:163], 0\n"
  "s_waitcnt lgkmcnt(2)\n"
  "v_mfma_f32_16x16x32_bf16 v[192:195], a[96:99], a[144:147], v[192:195]\n"
  "ds_read_b128 a[160:163], v9 offset:16384\n"
  "v_mfma_f32_16x16x32_bf16 v[148:151], v[32:35], v[160:163], 0\n"
  "v_mov_b64 v[4:5], v[2:3]\n"
  "v_mfma_f32_16x16x32_bf16 v[192:195], a[100:103], a[148:151], v[192:195]\n"
  "ds_read_b128 a[164:167], v10 offset:16384\n"
  "v_mov_b64 v[36:37], v[34:35]\n"
  "v_cvt_pk_bf16_f32 v168, v168, v169\n"
  "v_mfma_f32_16x16x32_bf16 v[196:199], a[104:107], a[144:147], v[196:199]\n"
  "v_cvt_pk_bf16_f32 v169, v170, v171\n"
  "v_cvt_pk_bf16_f32 v170, v172, v173\n"
  "v_mfma_f32_16x16x32_bf16 v[196:199], a[108:111], a[148:151], v[196:199]\n"
  "v_rcp_f32 v156, v156\n"
  "v_mfma_f32_16x16x32_bf16 v[200:203], a[112:115], a[144:147], v[200:203]\n"
  "v_mul_f32 v164, v156, v164\n"
  "v_mul_f32 v165, v156, v165\n"
  "v_mfma_f32_16x16x32_bf16 v[200:203], a[116:119], a[148:151], v[200:203]\n"
  "v_mul_f32 v166, v156, v166\n"
  "v_mul_f32 v167, v156, v167\n"
  "v_mfma_f32_16x16x32_bf16 v[204:207], a[120:123], a[144:147], v[204:207]\n"
  "v_mul_f32 v148, v156, v148\n"
  "v_mul_f32 v149, v156, v149\n"
  "v_mfma_f32_16x16x32_bf16 v[204:207], a[124:127], a[148:151], v[204:207]\n"
  "v_mul_f32 v150, v156, v150\n"
  "v_mul_f32 v151, v156, v151\n"
  "v_mfma_f32_16x16x32_bf16 v[208:211], a[144:147], a[128:131], v[208:211]\n"
  "v_cvt_pk_bf16_f32 v144, v164, v165\n"
  "v_cvt_pk_bf16_f32 v145, v166, v167\n"
  "v_mfma_f32_16x16x32_bf16 v[208:211], a[148:151], a[132:135], v[208:211]\n"
  "v_cvt_pk_bf16_f32 v146, v148, v149\n"
  "v_cvt_pk_bf16_f32 v147, v150, v151\n"
  "v_mfma_f32_16x16x32_bf16 v[212:215], a[144:147], a[136:139], v[212:215]\n"
  "buffer_store_dwordx4 v[144:147], v26, s[28:31], s35 offen offset:0 sc1\n"
  "v_cvt_pk_bf16_f32 v171, v174, v175\n"
  "v_mfma_f32_16x16x32_bf16 v[212:215], a[148:151], a[140:143], v[212:215]\n"
  "v_cvt_pk_bf16_f32 v176, v176, v177\n"
  "v_cvt_pk_bf16_f32 v177, v178, v179\n"
  "s_waitcnt lgkmcnt(2)\n"
  "v_mfma_f32_16x16x32_bf16 v[216:219], a[0:3], a[152:155], v[216:219]\n"
  "v_cvt_pk_bf16_f32 v178, v180, v181\n"
  "v_cvt_pk_bf16_f32 v179, v182, v183\n"
  "v_mfma_f32_16x16x32_bf16 v[220:223], a[8:11], a[152:155], v[220:223]\n"
  "v_mfma_f32_16x16x32_bf16 v[172:175], v[152:155], v[168:171], v[240:243]\n"
  "v_mfma_f32_16x16x32_bf16 v[180:183], v[176:179], v[168:171], v[244:247]\n"
  "v_mfma_f32_16x16x32_bf16 v[224:227], a[16:19], a[152:155], v[224:227]\n"
  "v_cvt_pk_bf16_f32 v6, v184, v185\n"
  "v_cvt_pk_bf16_f32 v7, v186, v187\n"
  "v_mfma_f32_16x16x32_bf16 v[228:231], a[24:27], a[152:155], v[228:231]\n"
  "v_cvt_pk_bf16_f32 v38, v188, v189\n"
  "v_cvt_pk_bf16_f32 v39, v190, v191\n"
  "v_mfma_f32_16x16x32_bf16 v[232:235], a[152:155], a[32:35], v[232:235]\n"
  "v_max_f32 v172, v172, v173\n"
  "v_max_f32 v174, v174, v175\n"
  "v_mfma_f32_16x16x32_bf16 v[236:239], a[152:155], a[40:43], v[236:239]\n"
  "v_max3_f32 v172, v172, v180, v181\n"
  "v_max3_f32 v174, v174, v182, v183\n"
  "v_mfma_f32_16x16x32_bf16 v[216:219], a[4:7], a[156:159], v[216:219]\n"
  "v_max_f32 v22, v172, v174\n"
  "v_max_f32 v24, v172, v174\n"
  "v_mfma_f32_16x16x32_bf16 v[220:223], a[12:15], a[156:159], v[220:223]\n"
  "s_nop 0\n"
  "v_permlane16_swap_b32 v22, v24\n"
  "v_mfma_f32_16x16x32_bf16 v[224:227], a[20:23], a[156:159], v[224:227]\n"
  "v_max_f32 v22, v22, v24\n"
  "v_mov_b32 v24, v22\n"
  "v_mfma_f32_16x16x32_bf16 v[228:231], a[28:31], a[156:159], v[228:231]\n"
  "s_nop 0\n"
  "v_permlane32_swap_b32 v22, v24\n"
  "v_mfma_f32_16x16x32_bf16 v[232:235], a[156:159], a[36:39], v[232:235]\n"
  "v_max_f32 v22, v22, v24\n"
  "v_mul_f32 v22, s71, v22\n"
  "v_mfma_f32_16x16x32_bf16 v[236:239], a[156:159], a[44:47], v[236:239]\n"
  "v_fma_f32 v172, v172, s71, -v22\n"
  "v_fma_f32 v174, v174, s71, -v22\n"
  "s_waitcnt lgkmcnt(0)\n"
  "v_mfma_f32_16x16x32_bf16 v[216:219], a[96:99], a[160:163], v[216:219]\n"
  "v_exp_f32 v172, v172\n"
  "v_mfma_f32_16x16x32_bf16 v[216:219], a[100:103], a[164:167], v[216:219]\n"
  "v_exp_f32 v174, v174\n"
  "v_mfma_f32_16x16x32_bf16 v[220:223], a[104:107], a[160:163], v[220:223]\n"
  "v_cvt_pk_bf16_f32 v172, v172, v172\n"
  "v_cvt_pk_bf16_f32 v174, v174, v174\n"
  "v_mfma_f32_16x16x32_bf16 v[220:223], a[108:111], a[164:167], v[220:223]\n"
  "v_and_b32 v184, v8, v172\n"
  "v_and_b32 v185, v11, v174\n"
  "v_mfma_f32_16x16x32_bf16 v[224:227], a[112:115], a[160:163], v[224:227]\n"
  "v_and_b32 v186, v20, v172\n"
  "v_and_b32 v187, v21, v174\n"
  "v_mfma_f32_16x16x32_bf16 v[224:227], a[116:119], a[164:167], v[224:227]\n"
  "s_nop 0\n"
  "v_mfma_f32_16x16x32_bf16 v[180:183], v[12:15], v[184:187], 0\n"
  "v_mfma_f32_16x16x32_bf16 v[188:191], v[4:7], v[184:187], 0\n"
  "v_mfma_f32_16x16x32_bf16 v[228:231], a[120:123], a[160:163], v[228:231]\n"
  "v_mfma_f32_16x16x32_bf16 v[172:175], v[36:39], v[184:187], 0\n"
  "v_mov_b64 v[0:1], v[6:7]\n"
  "v_mfma_f32_16x16x32_bf16 v[228:231], a[124:127], a[164:167], v[228:231]\n"
  "v_mov_b64 v[32:33], v[38:39]\n"
  "v_cvt_pk_bf16_f32 v192, v192, v193\n"
  "v_mfma_f32_16x16x32_bf16 v[232:235], a[160:163], a[128:131], v[232:235]\n"
  "v_cvt_pk_bf16_f32 v193, v194, v195\n"
  "v_cvt_pk_bf16_f32 v194, v196, v197\n"
  "v_mfma_f32_16x16x32_bf16 v[232:235], a[164:167], a[132:135], v[232:235]\n"
  "v_rcp_f32 v180, v180\n"
  "v_mfma_f32_16x16x32_bf16 v[236:239], a[160:163], a[136:139], v[236:239]\n"
  "v_mul_f32 v188, v180, v188\n"
  "v_mul_f32 v189, v180, v189\n"
  "v_mfma_f32_16x16x32_bf16 v[236:239], a[164:167], a[140:143], v[236:239]\n"
  "v_mul_f32 v190, v180, v190\n"
  "v_mul_f32 v191, v180, v191\n"
  "v_mul_f32 v172, v180, v172\n"
  "v_cvt_pk_bf16_f32 v195, v198, v199\n"
  "v_cvt_pk_bf16_f32 v216, v216, v217\n"
  "v_mul_f32 v173, v180, v173\n"
  "v_cvt_pk_bf16_f32 v200, v200, v201\n"
  "v_cvt_pk_bf16_f32 v217, v218, v219\n"
  "v_mul_f32 v174, v180, v174\n"
  "v_cvt_pk_bf16_f32 v201, v202, v203\n"
  "v_cvt_pk_bf16_f32 v218, v220, v221\n"
  "v_mul_f32 v175, v180, v175\n"
  "v_cvt_pk_bf16_f32 v202, v204, v205\n"
  "v_cvt_pk_bf16_f32 v219, v222, v223\n"
  "v_cvt_pk_bf16_f32 v168, v188, v189\n"
  "v_cvt_pk_bf16_f32 v203, v206, v207\n"
  "v_cvt_pk_bf16_f32 v224, v224, v225\n"
  "v_cvt_pk_bf16_f32 v169, v190, v191\n"
  "v_mfma_f32_16x16x32_bf16 v[196:199], v[176:179], v[192:195], v[240:243]\n"
  "v_cvt_pk_bf16_f32 v225, v226, v227\n"
  "v_cvt_pk_bf16_f32 v170, v172, v173\n"
  "v_mfma_f32_16x16x32_bf16 v[204:207], v[200:203], v[192:195], v[244:247]\n"
  "v_cvt_pk_bf16_f32 v226, v228, v229\n"
  "v_cvt_pk_bf16_f32 v171, v174, v175\n"
  "v_cvt_pk_bf16_f32 v2, v208, v209\n"
  "v_cvt_pk_bf16_f32 v227, v230, v231\n"
  "buffer_store_dwordx4 v[168:171], v26, s[28:31], s35 offen offset:2048 sc1\n"
  "v_cvt_pk_bf16_f32 v3, v210, v211\n"
  "v_mfma_f32_16x16x32_bf16 v[220:223], v[200:203], v[216:219], v[240:243]\n"
  "v_cvt_pk_bf16_f32 v34, v212, v213\n"
  "v_mfma_f32_16x16x32_bf16 v[228:231], v[224:227], v[216:219], v[244:247]\n"
  "v_cvt_pk_bf16_f32 v35, v214, v215\n"
  "v_cvt_pk_bf16_f32 v6, v232, v233\n"
  "v_max_f32 v196, v196, v197\n"
  "v_cvt_pk_bf16_f32 v7, v234, v235\n"
  "v_max_f32 v198, v198, v199\n"
  "v_cvt_pk_bf16_f32 v38, v236, v237\n"
  "v_max3_f32 v196, v196, v204, v205\n"
  "v_cvt_pk_bf16_f32 v39, v238, v239\n"
  "v_max3_f32 v198, v198, v206, v207\n"
  "v_max_f32 v220, v220, v221\n"
  "v_max_f32 v23, v196, v198\n"
  "v_max_f32 v222, v222, v223\n"
  "v_max_f32 v25, v196, v198\n"
  "v_max3_f32 v220, v220, v228, v229\n"
  "s_nop 0\n"
  "v_permlane16_swap_b32 v23, v25\n"
  "v_max3_f32 v222, v222, v230, v231\n"
  "v_max_f32 v23, v23, v25\n"
  "v_max_f32 v22, v220, v222\n"
  "v_mov_b32 v25, v23\n"
  "v_max_f32 v24, v220, v222\n"
  "s_nop 0\n"
  "v_permlane32_swap_b32 v23, v25\n"
  "v_permlane16_swap_b32 v22, v24\n"
  "v_max_f32 v23, v23, v25\n"
  "v_max_f32 v22, v22, v24\n"
  "v_mul_f32 v23, s71, v23\n"
  "v_mov_b32 v24, v22\n"
  "v_fma_f32 v196, v196, s71, -v23\n"
  "s_nop 0\n"
  "v_permlane32_swap_b32 v22, v24\n"
  "v_fma_f32 v198, v198, s71, -v23\n"
  "v_max_f32 v22, v22, v24\n"
  "v_exp_f32 v196, v196\n"
  "v_mul_f32 v22, s71, v22\n"
  "v_exp_f32 v198, v198\n"
  "v_fma_f32 v220, v220, s71, -v22\n"
  "v_cvt_pk_bf16_f32 v196, v196, v196\n"
  "v_fma_f32 v222, v222, s71, -v22\n"
  "v_cvt_pk_bf16_f32 v198, v198, v198\n"
  "v_exp_f32 v220, v220\n"
  "v_and_b32 v208, v8, v196\n"
  "v_exp_f32 v222, v222\n"
  "v_and_b32 v209, v11, v198\n"
  "v_cvt_pk_bf16_f32 v220, v220, v220\n"
  "v_and_b32 v210, v20, v196\n"
  "v_cvt_pk_bf16_f32 v222, v222, v222\n"
  "v_and_b32 v211, v21, v198\n"
  "v_and_b32 v232, v8, v220\n"
  "s_nop 0\n"
  "v_mfma_f32_16x16x32_bf16 v[204:207], v[12:15], v[208:211], 0\n"
  "v_and_b32 v233, v11, v222\n"
  "v_mfma_f32_16x16x32_bf16 v[212:215], v[0:3], v[208:211], 0\n"
  "v_and_b32 v234, v20, v220\n"
  "v_mfma_f32_16x16x32_bf16 v[196:199], v[32:35], v[208:211], 0\n"
  "v_and_b32 v235, v21, v222\n"
  "v_mov_b64 v[4:5], v[2:3]\n"
  "s_nop 0\n"
  "v_mfma_f32_16x16x32_bf16 v[228:231], v[12:15], v[232:235], 0\n"
  "v_mov_b64 v[36:37], v[34:35]\n"
  "v_mfma_f32_16x16x32_bf16 v[236:239], v[4:7], v[232:235], 0\n"
  "v_rcp_f32 v204, v204\n"
  "v_mfma_f32_16x16x32_bf16 v[220:223], v[36:39], v[232:235], 0\n"
  "v_mul_f32 v212, v204, v212\n"
  "s_nop 2\n"
  "v_rcp_f32 v228, v228\n"
  "v_mul_f32 v213, v204, v213\n"
  "v_mul_f32 v236, v228, v236\n"
  "v_mul_f32 v214, v204, v214\n"
  "v_mul_f32 v237, v228, v237\n"
  "v_mul_f32 v215, v204, v215\n"
  "v_mul_f32 v238, v228, v238\n"
  "v_mul_f32 v196, v204, v196\n"
  "v_mul_f32 v239, v228, v239\n"
  "v_mul_f32 v197, v204, v197\n"
  "v_mul_f32 v220, v228, v220\n"
  "v_mul_f32 v198, v204, v198\n"
  "v_mul_f32 v221, v228, v221\n"
  "v_mul_f32 v199, v204, v199\n"
  "v_mul_f32 v222, v228, v222\n"
  "v_cvt_pk_bf16_f32 v192, v212, v213\n"
  "v_mul_f32 v223, v228, v223\n"
  "v_cvt_pk_bf16_f32 v193, v214, v215\n"
  "v_cvt_pk_bf16_f32 v216, v236, v237\n"
  "v_cvt_pk_bf16_f32 v194, v196, v197\n"
  "v_cvt_pk_bf16_f32 v217, v238, v239\n"
  "v_cvt_pk_bf16_f32 v195, v198, v199\n"
  "v_cvt_pk_bf16_f32 v218, v220, v221\n"
  "buffer_store_dwordx4 v[192:195], v26, s[28:31], s27 offen offset:0 sc1\n"
  "v_cvt_pk_bf16_f32 v219, v222, v223\n"
  "buffer_store_dwordx4 v[216:219], v26, s[28:31], s27 offen offset:2048 sc1\n"
  "v_accvgpr_read_b32 v16, a200\n"
  "v_accvgpr_read_b32 v17, a201\n"
  "v_mov_b32 v13, 0\n"
  "v_readfirstlane_b32 s10, v16\n"
  "v_readfirstlane_b32 s11, v17\n"
  "s_add_u32 s10, s10, 1\n"
  "s_addc_u32 s11, s11, 0\n"
  "v_mov_b32 v10, s10\n"
  "v_mov_b32 v11, s11\n"
  "s_waitcnt vmcnt(0)\n"
  "s_barrier\n"
  "s_cmp_lg_u32 s59, 0\n"
  "s_cbranch_scc1 K1A_SIG_%=\n"
  "global_store_dwordx2 v13, v[10:11], s[12:13] sc1\n"
  "K1A_SIG_%=:\n"
  "K1A_CODE_END:\n"
  "s_nop 7\n"
  "K2_CODE_START:\n"
  "s_load_dwordx8 s[92:99], s[36:37], 0x38\n"
  "s_mov_b32 s65, s59\n"
  "s_and_b32 s84, s38, 3\n"
  "s_and_b32 s85, s38, 4\n"
  "s_lshr_b32 s66, s38, 6\n"
  "s_add_u32 s85, s85, s66\n"
  "s_bfe_u32 s86, s38, 0x30003\n"
  "s_mul_i32 s89, s84, 40960\n"
  "s_add_u32 s89, s89, 65536\n"
  "s_lshl_b32 s66, s85, 3\n"
  "s_lshl_b32 s67, s65, 1\n"
  "s_add_u32 s66, s66, s67\n"
  "s_mul_i32 s66, s66, 72\n"
  "s_lshl_b32 s68, s84, 2\n"
  "s_add_u32 s66, s66, s68\n"
  "s_lshl_b32 s91, s66, 12\n"
  "s_add_u32 s42, s89, 352256\n"
  "s_mov_b32 s43, 0x20000\n"
  "s_and_b32 s68, s67, 3\n"
  "s_and_b32 s69, s67, 4\n"
  "s_or_b32 s58, s68, 0\n"
  "s_xor_b32 s58, s58, s84\n"
  "s_add_u32 s58, s58, 3\n"
  "s_and_b32 s58, s58, 3\n"
  "s_or_b32 s58, s58, s69\n"
  "s_lshl_b32 s58, s58, 12\n"
  "s_add_u32 s58, s58, s39\n"
  "s_or_b32 s59, s68, 1\n"
  "s_xor_b32 s59, s59, s84\n"
  "s_add_u32 s59, s59, 3\n"
  "s_and_b32 s59, s59, 3\n"
  "s_or_b32 s59, s59, s69\n"
  "s_lshl_b32 s59, s59, 12\n"
  "s_add_u32 s59, s59, s39\n"
  "s_lshl_b32 s67, s65, 11\n"
  "s_add_u32 s48, s39, s67\n"
  "s_add_u32 s48, s48, 98304\n"
  "s_mov_b32 s56, 0\n"
  "s_mov_b32 s57, 0\n"
  "s_lshl_b32 s66, s65, 15\n"
  "s_lshl_b32 s67, s86, 8\n"
  "s_add_u32 s66, s66, s67\n"
  "s_sub_u32 s46, 528384, s66\n"
  "s_sub_u32 s88, 1839104, s66\n"
  "s_mov_b32 s47, 0x20000\n"
  "s_lshl_b32 s67, s84, 19\n"
  "s_add_u32 s67, s67, s66\n"
  "s_mul_i32 s68, s84, 1835008\n"
  "s_add_u32 s68, s68, s66\n"
  "s_sub_u32 s67, s67, 4096\n"
  "s_subb_u32 s69, 0, 0\n"
  "s_waitcnt lgkmcnt(0)\n"
  "s_add_u32 s44, s92, s67\n"
  "s_addc_u32 s45, s93, s69\n"
  "s_and_b32 s45, s45, 0xffff\n"
  "s_add_u32 s40, s96, s91\n"
  "s_addc_u32 s41, s97, 0\n"
  "s_and_b32 s41, s41, 0xffff\n"
  "s_sub_u32 s68, s68, 4096\n"
  "s_subb_u32 s69, 0, 0\n"
  "s_add_u32 s90, s94, s68\n"
  "s_addc_u32 s91, s95, s69\n"
  "s_and_b32 s91, s91, 0xffff\n"
  "s_lshl_b32 s66, s85, 2\n"
  "s_add_u32 s66, s66, s84\n"
  "s_lshl_b32 s66, s66, 6\n"
  "s_add_u32 s66, s66, 21299200\n"
  "s_add_u32 s8, s96, s66\n"
  "s_addc_u32 s9, s97, 0\n"
  "s_load_dwordx8 s[24:31], s[8:9], 0x0\n"
  "v_mbcnt_lo_u32_b32 v28, -1, 0\n"
  "v_mbcnt_hi_u32_b32 v28, -1, v28\n"
  "v_lshrrev_b32 v29, 3, v28\n"
  "v_lshrrev_b32 v30, 4, v28\n"
  "v_lshlrev_b32 v0, 4, v28\n"
  "v_add_u32 v1, 294912, v0\n"
  "s_mov_b32 s34, 4294901760\n"
  "s_mov_b32 s35, 4294934528\n"
  "v_bfe_u32 v31, v28, 1, 2\n"
  "v_xor_b32 v31, v31, v30\n"
  "v_lshlrev_b32 v31, 5, v31\n"
  "v_and_b32 v24, 1, v28\n"
  "v_lshl_or_b32 v31, v24, 4, v31\n"
  "v_lshl_or_b32 v23, v29, 11, v31\n"
  "v_add_u32 v4, 4096, v23\n"
  "v_add_u32 v5, 19456, v23\n"
  "v_add_u32 v6, 4224, v23\n"
  "v_add_u32 v7, 19584, v23\n"
  "s_add_u32 m0, s48, 0\n"
  "s_nop 0\n"
  "buffer_load_dwordx4 v4, s[44:47], s57 offen offset:0 lds\n"
  "buffer_load_dwordx4 v5, s[44:47], s57 offen offset:1024 lds\n"
  "s_add_u32 m0, s48, 8192\n"
  "s_nop 0\n"
  "buffer_load_dwordx4 v6, s[44:47], s57 offen offset:0 lds\n"
  "buffer_load_dwordx4 v7, s[44:47], s57 offen offset:1024 lds\n"
  "s_add_u32 s57, s57, 131072\n"
  "s_add_u32 s48, s48, 16384\n"
  "v_mov_b32 v212, v10\n"
  "v_mov_b32 v213, v11\n"
  "v_and_b32 v215, 3, v28\n"
  "v_lshlrev_b32 v215, 3, v215\n"
  "s_lshr_b32 s66, s65, 1\n"
  "s_lshl_b32 s66, s66, 14\n"
  "s_add_u32 s66, s66, s39\n"
  "v_add_u32 v8, s66, v0\n"
  "v_bfe_u32 v27, v28, 2, 2\n"
  "v_lshl_add_u32 v27, v30, 2, v27\n"
  "v_and_b32 v26, 3, v28\n"
  "v_lshlrev_b32 v26, 3, v26\n"
  "v_bfe_u32 v25, v28, 3, 2\n"
  "v_lshl_or_b32 v25, v25, 5, v26\n"
  "v_lshl_or_b32 v25, v27, 7, v25\n"
  "s_and_b32 s66, s65, 1\n"
  "s_lshl_b32 s66, s66, 13\n"
  "s_add_u32 s66, s66, s39\n"
  "s_add_u32 s66, s66, 98304\n"
  "v_add_u32 v9, s66, v25\n"
  "v_xor_b32 v10, 32, v9\n"
  "v_xor_b32 v11, 64, v9\n"
  "v_xor_b32 v12, 96, v9\n"
  "s_waitcnt lgkmcnt(0)\n"
  "s_xor_b64 s[24:25], s[24:25], s[10:11]\n"
  "s_xor_b64 s[26:27], s[26:27], s[10:11]\n"
  "s_or_b64 s[24:25], s[24:25], s[26:27]\n"
  "s_xor_b64 s[28:29], s[28:29], s[10:11]\n"
  "s_or_b64 s[24:25], s[24:25], s[28:29]\n"
  "s_xor_b64 s[30:31], s[30:31], s[10:11]\n"
  "s_or_b64 s[24:25], s[24:25], s[30:31]\n"
  "s_cbranch_scc0 K2_OKO_%=\n"
  "K2_FBO_%=:\n"
  "s_mov_b32 s60, 0x40000\n"
  "K2_SPO_%=:\n"
  "global_load_dwordx2 v[16:17], v215, s[8:9] sc1\n"
  "s_waitcnt vmcnt(0)\n"
  "v_cmp_ne_u64 vcc, s[10:11], v[16:17]\n"
  "s_sub_u32 s60, s60, 1\n"
  "s_cbranch_scc1 K2_OKO_%=\n"
  "s_and_b32 s66, vcc_lo, 15\n"
  "s_cbranch_scc0 K2_OKO_%=\n"
  "s_sleep 1\n"
  "s_branch K2_SPO_%=\n"
  "K2_OKO_%=:\n"
  "s_mov_b32 m0, s58\n"
  "s_nop 0\n"
  "buffer_load_dwordx4 v0, s[40:43], s56 offen offset:0 sc1 lds\n"
  "buffer_load_dwordx4 v0, s[40:43], s56 offen offset:1024 sc1 lds\n"
  "buffer_load_dwordx4 v0, s[40:43], s56 offen offset:2048 sc1 lds\n"
  "buffer_load_dwordx4 v0, s[40:43], s56 offen offset:3072 sc1 lds\n"
  "s_mov_b32 m0, s59\n"
  "s_nop 0\n"
  "buffer_load_dwordx4 v1, s[40:43], s56 offen offset:0 sc1 lds\n"
  "buffer_load_dwordx4 v1, s[40:43], s56 offen offset:1024 sc1 lds\n"
  "buffer_load_dwordx4 v1, s[40:43], s56 offen offset:2048 sc1 lds\n"
  "buffer_load_dwordx4 v1, s[40:43], s56 offen offset:3072 sc1 lds\n"
  "s_add_u32 s56, s56, 4096\n"
  "s_add_u32 s58, s58, 32768\n"
  "s_add_u32 s59, s59, 32768\n"
  "s_add_u32 m0, s48, 0\n"
  "s_nop 0\n"
  "buffer_load_dwordx4 v4, s[44:47], s57 offen offset:0 lds\n"
  "buffer_load_dwordx4 v5, s[44:47], s57 offen offset:1024 lds\n"
  "s_add_u32 m0, s48, 8192\n"
  "s_nop 0\n"
  "buffer_load_dwordx4 v6, s[44:47], s57 offen offset:0 lds\n"
  "buffer_load_dwordx4 v7, s[44:47], s57 offen offset:1024 lds\n"
  "s_add_u32 s57, s57, 131072\n"
  "s_add_u32 s48, s48, 16384\n"
  "s_add_u32 s52, s96, 20971520\n"
  "s_addc_u32 s53, s97, 0\n"
  "s_and_b32 s53, s53, 0xffff\n"
  "s_mov_b32 s54, 8192\n"
  "s_mov_b32 s55, 0x20000\n"
  "s_lshr_b32 s66, s65, 1\n"
  "s_lshl_b32 s67, s85, 10\n"
  "s_lshl_b32 s69, s66, 9\n"
  "s_add_u32 s67, s67, s69\n"
  "v_and_b32 v19, 15, v28\n"
  "v_lshlrev_b32 v18, 2, v19\n"
  "s_xor_b32 s69, s84, 1\n"
  "s_lshl_b32 s69, s69, 7\n"
  "s_add_u32 s69, s69, s67\n"
  "s_mov_b32 s87, s69\n"
  "s_xor_b32 s69, s84, 2\n"
  "s_lshl_b32 s69, s69, 7\n"
  "s_add_u32 s69, s69, s67\n"
  "s_xor_b32 s69, s84, 3\n"
  "s_lshl_b32 s69, s69, 7\n"
  "s_add_u32 s69, s69, s67\n"
  "s_xor_b32 s69, s84, 0\n"
  "s_lshl_b32 s69, s69, 7\n"
  "s_add_u32 s69, s69, s67\n"
  "s_lshl_b32 s68, s69, 9\n"
  "s_lshl_b32 s69, s86, 8\n"
  "s_add_u32 s68, s68, s69\n"
  "s_and_b32 s69, s65, 1\n"
  "s_lshl_b32 s69, s69, 7\n"
  "s_add_u32 s68, s68, s69\n"
  "s_mov_b32 s76, s98\n"
  "s_and_b32 s77, s99, 0xffff\n"
  "s_mov_b32 s78, 4194304\n"
  "s_mov_b32 s79, 0x20000\n"
  "s_add_u32 s72, s96, 22020096\n"
  "s_addc_u32 s73, s97, 0\n"
  "s_and_b32 s73, s73, 0xffff\n"
  "s_mov_b32 s74, 16777216\n"
  "s_mov_b32 s75, 0x20000\n"
  "s_lshl_b32 s66, s38, 16\n"
  "s_lshl_b32 s69, s65, 12\n"
  "s_add_u32 s66, s66, s69\n"
  "s_add_u32 s62, s66, 0\n"
  "s_xor_b32 s70, s62, 65536\n"
  "s_add_u32 s63, s66, 16384\n"
  "s_xor_b32 s71, s63, 131072\n"
  "s_add_u32 s64, s66, 32768\n"
  "s_xor_b32 s33, s64, 196608\n"
  "s_add_u32 s82, s96, 21037056\n"
  "s_addc_u32 s83, s97, 0\n"
  "s_lshl_b32 s69, s38, 2\n"
  "s_add_u32 s69, s69, s65\n"
  "s_lshl_b32 s69, s69, 8\n"
  "s_add_u32 s80, s82, s69\n"
  "s_addc_u32 s81, s83, 0\n"
  "s_xor_b32 s16, s69, 1024\n"
  "s_xor_b32 s17, s69, 2112\n"
  "s_xor_b32 s98, s69, 3200\n"
  "v_min_u32 v17, 2, v28\n"
  "v_add_u32 v16, 1, v17\n"
  "v_lshlrev_b32 v16, 10, v16\n"
  "v_lshl_or_b32 v16, v17, 6, v16\n"
  "v_xor_b32 v16, s69, v16\n"
  "v_mul_u32_u24 v2, 144, v19\n"
  "v_lshl_add_u32 v2, v30, 3, v2\n"
  "v_and_b32 v19, 7, v28\n"
  "v_lshlrev_b32 v19, 4, v19\n"
  "v_mul_u32_u24 v3, 144, v29\n"
  "v_add_u32 v3, v3, v19\n"
  "v_lshl_or_b32 v19, v29, 11, v19\n"
  "s_mul_i32 s69, s65, 4608\n"
  "s_add_u32 s69, s69, s39\n"
  "v_add_u32 v3, s69, v3\n"
  "v_add_u32 v2, s69, v2\n"
  "v_mov_b32 v29, v16\n"
  "s_or_b32 s99, s38, s65\n"
  "s_xor_b32 s84, s87, 384\n"
  "s_xor_b32 s85, s87, 256\n"
  "s_xor_b32 s86, s87, 128\n"
  "v_mov_b32 v30, s10\n"
  "v_mov_b32 v31, s11\n"
  "s_waitcnt vmcnt(12)\n"
  "s_barrier\n"
  "s_bitcmp1_b32 s38, 5\n"
  "s_cselect_b32 s66, s65, 1\n"
  "s_cmp_lg_u32 s66, 0\n"
  "s_cbranch_scc1 K2_NODONE_%=\n"
  "global_store_dwordx2 v13, v[212:213], s[12:13] sc1\n"
  "K2_NODONE_%=:\n"
  "ds_read_b64_tr_b16 a[32:33], v9 offset:0\n"
  "ds_read_b64_tr_b16 a[34:35], v9 offset:2048\n"
  "ds_read_b64_tr_b16 a[40:41], v10 offset:0\n"
  "ds_read_b64_tr_b16 a[42:43], v10 offset:2048\n"
  "ds_read_b64_tr_b16 a[48:49], v11 offset:0\n"
  "ds_read_b64_tr_b16 a[50:51], v11 offset:2048\n"
  "ds_read_b64_tr_b16 a[56:57], v12 offset:0\n"
  "ds_read_b64_tr_b16 a[58:59], v12 offset:2048\n"
  "ds_read_b64_tr_b16 a[36:37], v9 offset:4096\n"
  "ds_read_b64_tr_b16 a[38:39], v9 offset:6144\n"
  "ds_read_b64_tr_b16 a[44:45], v10 offset:4096\n"
  "ds_read_b64_tr_b16 a[46:47], v10 offset:6144\n"
  "ds_read_b64_tr_b16 a[52:53], v11 offset:4096\n"
  "ds_read_b64_tr_b16 a[54:55], v11 offset:6144\n"
  "ds_read_b64_tr_b16 a[60:61], v12 offset:4096\n"
  "ds_read_b64_tr_b16 a[62:63], v12 offset:6144\n"
  "s_mov_b32 m0, s58\n"
  "s_nop 0\n"
  "buffer_load_dwordx4 v0, s[40:43], s56 offen offset:0 sc1 lds\n"
  "buffer_load_dwordx4 v0, s[40:43], s56 offen offset:1024 sc1 lds\n"
  "buffer_load_dwordx4 v0, s[40:43], s56 offen offset:2048 sc1 lds\n"
  "buffer_load_dwordx4 v0, s[40:43], s56 offen offset:3072 sc1 lds\n"
  "s_mov_b32 m0, s59\n"
  "s_nop 0\n"
  "buffer_load_dwordx4 v1, s[40:43], s56 offen offset:0 sc1 lds\n"
  "buffer_load_dwordx4 v1, s[40:43], s56 offen offset:1024 sc1 lds\n"
  "buffer_load_dwordx4 v1, s[40:43], s56 offen offset:2048 sc1 lds\n"
  "buffer_load_dwordx4 v1, s[40:43], s56 offen offset:3072 sc1 lds\n"
  "s_add_u32 s56, s56, 4096\n"
  "s_add_u32 s58, s58, 32768\n"
  "s_add_u32 s59, s59, 32768\n"
  "s_add_u32 m0, s48, 0\n"
  "s_nop 0\n"
  "buffer_load_dwordx4 v4, s[44:47], s57 offen offset:0 lds\n"
  "buffer_load_dwordx4 v5, s[44:47], s57 offen offset:1024 lds\n"
  "s_add_u32 m0, s48, 8192\n"
  "s_nop 0\n"
  "buffer_load_dwordx4 v6, s[44:47], s57 offen offset:0 lds\n"
  "buffer_load_dwordx4 v7, s[44:47], s57 offen offset:1024 lds\n"
  "s_add_u32 s57, s57, 131072\n"
  "s_add_u32 s48, s48, 4294934528\n"
  "s_and_b32 s12, s8, 0xffffff00\n"
  "s_mov_b32 s13, s9\n"
  "s_add_u32 s12, s12, 4096\n"
  "s_addc_u32 s13, s13, 0\n"
  "v_bfe_u32 v13, v28, 2, 2\n"
  "v_lshl_or_b32 v13, v13, 6, v215\n"
  "s_waitcnt vmcnt(12) lgkmcnt(0)\n"
  "s_barrier\n"
  "s_mov_b32 s49, 0\n"
  "s_mov_b32 s50, 2\n"
  "v_add_u32 v9, 16384, v9\n"
  "v_add_u32 v10, 16384, v10\n"
  "v_add_u32 v11, 16384, v11\n"
  "v_add_u32 v12, 16384, v12\n"
  "ds_read_b128 a[96:99], v8 offset:0\n"
  "ds_read_b128 a[100:103], v8 offset:1024\n"
  "s_mov_b32 m0, s58\n"
  "s_nop 0\n"
  "buffer_load_dwordx4 v0, s[40:43], s56 offen offset:0 sc1 lds\n"
  "buffer_load_dwordx4 v0, s[40:43], s56 offen offset:1024 sc1 lds\n"
  "ds_read_b128 a[104:107], v8 offset:2048\n"
  "ds_read_b128 a[108:111], v8 offset:3072\n"
  "buffer_load_dwordx4 v0, s[40:43], s56 offen offset:2048 sc1 lds\n"
  "buffer_load_dwordx4 v0, s[40:43], s56 offen offset:3072 sc1 lds\n"
  "s_waitcnt lgkmcnt(2)\n"
  "v_mfma_f32_16x16x32_bf16 v[32:35], a[32:35], a[96:99], 0\n"
  "ds_read_b128 a[112:115], v8 offset:4096\n"
  "v_mfma_f32_16x16x32_bf16 v[36:39], a[40:43], a[96:99], 0\n"
  "ds_read_b128 a[116:119], v8 offset:5120\n"
  "s_mov_b32 m0, s59\n"
  "v_mfma_f32_16x16x32_bf16 v[40:43], a[48:51], a[96:99], 0\n"
  "buffer_load_dwordx4 v1, s[40:43], s56 offen offset:0 sc1 lds\n"
  "v_mfma_f32_16x16x32_bf16 v[44:47], a[56:59], a[96:99], 0\n"
  "ds_read_b64_tr_b16 a[0:1], v9 offset:0\n"
  "v_mfma_f32_16x16x32_bf16 v[32:35], a[36:39], a[100:103], v[32:35]\n"
  "ds_read_b64_tr_b16 a[2:3], v9 offset:2048\n"
  "v_mfma_f32_16x16x32_bf16 v[36:39], a[44:47], a[100:103], v[36:39]\n"
  "v_mfma_f32_16x16x32_bf16 v[40:43], a[52:55], a[100:103], v[40:43]\n"
  "buffer_load_dwordx4 v1, s[40:43], s56 offen offset:1024 sc1 lds\n"
  "v_mfma_f32_16x16x32_bf16 v[44:47], a[60:63], a[100:103], v[44:47]\n"
  "ds_read_b64_tr_b16 a[8:9], v10 offset:0\n"
  "s_waitcnt lgkmcnt(5)\n"
  "v_mfma_f32_16x16x32_bf16 v[48:51], a[32:35], a[104:107], 0\n"
  "ds_read_b128 a[120:123], v8 offset:6144\n"
  "v_mfma_f32_16x16x32_bf16 v[52:55], a[40:43], a[104:107], 0\n"
  "ds_read_b128 a[124:127], v8 offset:7168\n"
  "v_mfma_f32_16x16x32_bf16 v[56:59], a[48:51], a[104:107], 0\n"
  "buffer_load_dwordx4 v1, s[40:43], s56 offen offset:2048 sc1 lds\n"
  "v_mfma_f32_16x16x32_bf16 v[60:63], a[56:59], a[104:107], 0\n"
  "ds_read_b64_tr_b16 a[10:11], v10 offset:2048\n"
  "v_mfma_f32_16x16x32_bf16 v[48:51], a[36:39], a[108:111], v[48:51]\n"
  "ds_read_b64_tr_b16 a[16:17], v11 offset:0\n"
  "v_mfma_f32_16x16x32_bf16 v[52:55], a[44:47], a[108:111], v[52:55]\n"
  "ds_read_b64_tr_b16 a[18:19], v11 offset:2048\n"
  "v_mfma_f32_16x16x32_bf16 v[56:59], a[52:55], a[108:111], v[56:59]\n"
  "buffer_load_dwordx4 v1, s[40:43], s56 offen offset:3072 sc1 lds\n"
  "v_mfma_f32_16x16x32_bf16 v[60:63], a[60:63], a[108:111], v[60:63]\n"
  "s_add_u32 s56, s56, 4096\n"
  "ds_read_b64_tr_b16 a[24:25], v12 offset:0\n"
  "s_waitcnt lgkmcnt(9)\n"
  "v_mfma_f32_16x16x32_bf16 v[64:67], a[32:35], a[112:115], 0\n"
  "ds_read_b128 a[96:99], v8 offset:8192\n"
  "s_cmp_eq_u32 s49, 0\n"
  "s_cselect_b32 s66, s34, 32768\n"
  "v_mfma_f32_16x16x32_bf16 v[68:71], a[40:43], a[112:115], 0\n"
  "ds_read_b128 a[100:103], v8 offset:9216\n"
  "s_add_u32 s58, s58, s66\n"
  "v_mfma_f32_16x16x32_bf16 v[72:75], a[48:51], a[112:115], 0\n"
  "s_add_u32 s59, s59, s66\n"
  "ds_read_b64_tr_b16 a[26:27], v12 offset:2048\n"
  "v_mfma_f32_16x16x32_bf16 v[76:79], a[56:59], a[112:115], 0\n"
  "v_mfma_f32_16x16x32_bf16 v[64:67], a[36:39], a[116:119], v[64:67]\n"
  "s_add_u32 m0, s48, 0\n"
  "ds_read_b64_tr_b16 a[4:5], v9 offset:4096\n"
  "v_mfma_f32_16x16x32_bf16 v[68:71], a[44:47], a[116:119], v[68:71]\n"
  "buffer_load_dwordx4 v4, s[44:47], s57 offen offset:0 lds\n"
  "v_mfma_f32_16x16x32_bf16 v[72:75], a[52:55], a[116:119], v[72:75]\n"
  "ds_read_b64_tr_b16 a[6:7], v9 offset:6144\n"
  "v_mfma_f32_16x16x32_bf16 v[76:79], a[60:63], a[116:119], v[76:79]\n"
  "s_waitcnt lgkmcnt(9)\n"
  "v_mfma_f32_16x16x32_bf16 v[80:83], a[32:35], a[120:123], 0\n"
  "ds_read_b128 a[104:107], v8 offset:10240\n"
  "v_mfma_f32_16x16x32_bf16 v[84:87], a[40:43], a[120:123], 0\n"
  "ds_read_b128 a[108:111], v8 offset:11264\n"
  "v_mfma_f32_16x16x32_bf16 v[88:91], a[48:51], a[120:123], 0\n"
  "buffer_load_dwordx4 v5, s[44:47], s57 offen offset:1024 lds\n"
  "v_mfma_f32_16x16x32_bf16 v[92:95], a[56:59], a[120:123], 0\n"
  "ds_read_b64_tr_b16 a[12:13], v10 offset:4096\n"
  "v_mfma_f32_16x16x32_bf16 v[80:83], a[36:39], a[124:127], v[80:83]\n"
  "s_add_u32 m0, s48, 8192\n"
  "ds_read_b64_tr_b16 a[14:15], v10 offset:6144\n"
  "v_mfma_f32_16x16x32_bf16 v[84:87], a[44:47], a[124:127], v[84:87]\n"
  "buffer_load_dwordx4 v6, s[44:47], s57 offen offset:0 lds\n"
  "v_mfma_f32_16x16x32_bf16 v[88:91], a[52:55], a[124:127], v[88:91]\n"
  "ds_read_b64_tr_b16 a[20:21], v11 offset:4096\n"
  "s_cmp_eq_u32 s50, 0\n"
  "s_cselect_b32 s51, s34, 32768\n"
  "v_mfma_f32_16x16x32_bf16 v[92:95], a[60:63], a[124:127], v[92:95]\n"
  "s_cmp_eq_u32 s50, 1\n"
  "s_cselect_b32 s61, s35, 16384\n"
  "s_waitcnt lgkmcnt(8)\n"
  "v_mfma_f32_16x16x32_bf16 v[96:99], a[32:35], a[96:99], 0\n"
  "ds_read_b128 a[112:115], v8 offset:12288\n"
  "v_mfma_f32_16x16x32_bf16 v[100:103], a[40:43], a[96:99], 0\n"
  "ds_read_b128 a[116:119], v8 offset:13312\n"
  "v_mfma_f32_16x16x32_bf16 v[104:107], a[48:51], a[96:99], 0\n"
  "buffer_load_dwordx4 v7, s[44:47], s57 offen offset:1024 lds\n"
  "v_mfma_f32_16x16x32_bf16 v[108:111], a[56:59], a[96:99], 0\n"
  "s_mov_b64 s[44:45], s[90:91]\n"
  "s_mov_b32 s46, s88\n"
  "s_mov_b32 s57, 0\n"
  "s_cmp_eq_u32 s50, 0\n"
  "s_cselect_b32 s50, 3, s50\n"
  "v_mfma_f32_16x16x32_bf16 v[96:99], a[36:39], a[100:103], v[96:99]\n"
  "ds_read_b64_tr_b16 a[22:23], v11 offset:6144\n"
  "v_mfma_f32_16x16x32_bf16 v[100:103], a[44:47], a[100:103], v[100:103]\n"
  "s_cmp_eq_u32 s49, 1\n"
  "s_cselect_b32 s67, s35, 16384\n"
  "ds_read_b64_tr_b16 a[28:29], v12 offset:4096\n"
  "v_mfma_f32_16x16x32_bf16 v[104:107], a[52:55], a[100:103], v[104:107]\n"
  "ds_read_b64_tr_b16 a[30:31], v12 offset:6144\n"
  "s_sub_u32 s50, s50, 1\n"
  "v_mfma_f32_16x16x32_bf16 v[108:111], a[60:63], a[100:103], v[108:111]\n"
  "s_waitcnt lgkmcnt(8)\n"
  "v_mfma_f32_16x16x32_bf16 v[112:115], a[32:35], a[104:107], 0\n"
  "ds_read_b128 a[120:123], v8 offset:14336\n"
  "s_add_u32 s48, s48, s67\n"
  "v_mfma_f32_16x16x32_bf16 v[116:119], a[40:43], a[104:107], 0\n"
  "ds_read_b128 a[124:127], v8 offset:15360\n"
  "v_mfma_f32_16x16x32_bf16 v[120:123], a[48:51], a[104:107], 0\n"
  "s_cmp_eq_u32 s49, 0\n"
  "s_cselect_b32 s49, 3, s49\n"
  "v_mfma_f32_16x16x32_bf16 v[124:127], a[56:59], a[104:107], 0\n"
  "v_add_u32 v8, s51, v8\n"
  "v_mfma_f32_16x16x32_bf16 v[112:115], a[36:39], a[108:111], v[112:115]\n"
  "v_mfma_f32_16x16x32_bf16 v[116:119], a[44:47], a[108:111], v[116:119]\n"
  "s_sub_u32 s49, s49, 1\n"
  "v_mfma_f32_16x16x32_bf16 v[120:123], a[52:55], a[108:111], v[120:123]\n"
  "v_mfma_f32_16x16x32_bf16 v[124:127], a[60:63], a[108:111], v[124:127]\n"
  "s_waitcnt vmcnt(12) lgkmcnt(0)\n"
  "s_barrier\n"
  "v_mfma_f32_16x16x32_bf16 v[128:131], a[32:35], a[112:115], 0\n"
  "ds_read_b128 a[96:99], v8 offset:0\n"
  "v_mfma_f32_16x16x32_bf16 v[132:135], a[40:43], a[112:115], 0\n"
  "ds_read_b128 a[100:103], v8 offset:1024\n"
  "v_mfma_f32_16x16x32_bf16 v[136:139], a[48:51], a[112:115], 0\n"
  "s_mov_b32 m0, s58\n"
  "v_mfma_f32_16x16x32_bf16 v[140:143], a[56:59], a[112:115], 0\n"
  "buffer_load_dwordx4 v0, s[40:43], s56 offen offset:0 sc1 lds\n"
  "v_mfma_f32_16x16x32_bf16 v[128:131], a[36:39], a[116:119], v[128:131]\n"
  "v_add_u32 v9, s61, v9\n"
  "v_mfma_f32_16x16x32_bf16 v[132:135], a[44:47], a[116:119], v[132:135]\n"
  "v_mfma_f32_16x16x32_bf16 v[136:139], a[52:55], a[116:119], v[136:139]\n"
  "v_add_u32 v10, s61, v10\n"
  "v_mfma_f32_16x16x32_bf16 v[140:143], a[60:63], a[116:119], v[140:143]\n"
  "buffer_load_dwordx4 v0, s[40:43], s56 offen offset:1024 sc1 lds\n"
  "v_mfma_f32_16x16x32_bf16 v[144:147], a[32:35], a[120:123], 0\n"
  "ds_read_b128 a[104:107], v8 offset:2048\n"
  "v_mfma_f32_16x16x32_bf16 v[148:151], a[40:43], a[120:123], 0\n"
  "ds_read_b128 a[108:111], v8 offset:3072\n"
  "v_mfma_f32_16x16x32_bf16 v[152:155], a[48:51], a[120:123], 0\n"
  "v_add_u32 v11, s61, v11\n"
  "v_mfma_f32_16x16x32_bf16 v[156:159], a[56:59], a[120:123], 0\n"
  "buffer_load_dwordx4 v0, s[40:43], s56 offen offset:2048 sc1 lds\n"
  "v_mfma_f32_16x16x32_bf16 v[144:147], a[36:39], a[124:127], v[144:147]\n"
  "v_add_u32 v12, s61, v12\n"
  "v_mfma_f32_16x16x32_bf16 v[148:151], a[44:47], a[124:127], v[148:151]\n"
  "v_mfma_f32_16x16x32_bf16 v[152:155], a[52:55], a[124:127], v[152:155]\n"
  "v_mfma_f32_16x16x32_bf16 v[156:159], a[60:63], a[124:127], v[156:159]\n"
  "buffer_load_dwordx4 v0, s[40:43], s56 offen offset:3072 sc1 lds\n"
  "s_waitcnt lgkmcnt(2)\n"
  "v_mfma_f32_16x16x32_bf16 v[32:35], a[0:3], a[96:99], v[32:35]\n"
  "ds_read_b128 a[112:115], v8 offset:4096\n"
  "v_mfma_f32_16x16x32_bf16 v[36:39], a[8:11], a[96:99], v[36:39]\n"
  "ds_read_b128 a[116:119], v8 offset:5120\n"
  "s_mov_b32 m0, s59\n"
  "global_load_dwordx2 v[14:15], v13, s[12:13] sc1\n"
  "v_mfma_f32_16x16x32_bf16 v[40:43], a[16:19], a[96:99], v[40:43]\n"
  "buffer_load_dwordx4 v1, s[40:43], s56 offen offset:0 sc1 lds\n"
  "v_mfma_f32_16x16x32_bf16 v[44:47], a[24:27], a[96:99], v[44:47]\n"
  "ds_read_b64_tr_b16 a[32:33], v9 offset:0\n"
  "v_mfma_f32_16x16x32_bf16 v[32:35], a[4:7], a[100:103], v[32:35]\n"
  "ds_read_b64_tr_b16 a[34:35], v9 offset:2048\n"
  "v_mfma_f32_16x16x32_bf16 v[36:39], a[12:15], a[100:103], v[36:39]\n"
  "v_mfma_f32_16x16x32_bf16 v[40:43], a[20:23], a[100:103], v[40:43]\n"
  "buffer_load_dwordx4 v1, s[40:43], s56 offen offset:1024 sc1 lds\n"
  "s_load_dwordx8 s[24:31], s[8:9], 4096\n"
  "v_mfma_f32_16x16x32_bf16 v[44:47], a[28:31], a[100:103], v[44:47]\n"
  "ds_read_b64_tr_b16 a[40:41], v10 offset:0\n"
  "s_waitcnt lgkmcnt(5)\n"
  "v_mfma_f32_16x16x32_bf16 v[48:51], a[0:3], a[104:107], v[48:51]\n"
  "ds_read_b128 a[120:123], v8 offset:6144\n"
  "v_mfma_f32_16x16x32_bf16 v[52:55], a[8:11], a[104:107], v[52:55]\n"
  "ds_read_b128 a[124:127], v8 offset:7168\n"
  "v_mfma_f32_16x16x32_bf16 v[56:59], a[16:19], a[104:107], v[56:59]\n"
  "buffer_load_dwordx4 v1, s[40:43], s56 offen offset:2048 sc1 lds\n"
  "v_mfma_f32_16x16x32_bf16 v[60:63], a[24:27], a[104:107], v[60:63]\n"
  "ds_read_b64_tr_b16 a[42:43], v10 offset:2048\n"
  "v_mfma_f32_16x16x32_bf16 v[48:51], a[4:7], a[108:111], v[48:51]\n"
  "ds_read_b64_tr_b16 a[48:49], v11 offset:0\n"
  "v_mfma_f32_16x16x32_bf16 v[52:55], a[12:15], a[108:111], v[52:55]\n"
  "ds_read_b64_tr_b16 a[50:51], v11 offset:2048\n"
  "v_mfma_f32_16x16x32_bf16 v[56:59], a[20:23], a[108:111], v[56:59]\n"
  "buffer_load_dwordx4 v1, s[40:43], s56 offen offset:3072 sc1 lds\n"
  "v_mfma_f32_16x16x32_bf16 v[60:63], a[28:31], a[108:111], v[60:63]\n"
  "s_mov_b32 s56, s89\n"
  "ds_read_b64_tr_b16 a[56:57], v12 offset:0\n"
  "s_waitcnt lgkmcnt(9)\n"
  "v_mfma_f32_16x16x32_bf16 v[64:67], a[0:3], a[112:115], v[64:67]\n"
  "ds_read_b128 a[96:99], v8 offset:8192\n"
  "s_cmp_eq_u32 s49, 0\n"
  "s_cselect_b32 s66, s34, 32768\n"
  "v_mfma_f32_16x16x32_bf16 v[68:71], a[8:11], a[112:115], v[68:71]\n"
  "ds_read_b128 a[100:103], v8 offset:9216\n"
  "s_add_u32 s58, s58, s66\n"
  "v_mfma_f32_16x16x32_bf16 v[72:75], a[16:19], a[112:115], v[72:75]\n"
  "s_add_u32 s59, s59, s66\n"
  "ds_read_b64_tr_b16 a[58:59], v12 offset:2048\n"
  "v_mfma_f32_16x16x32_bf16 v[76:79], a[24:27], a[112:115], v[76:79]\n"
  "v_mfma_f32_16x16x32_bf16 v[64:67], a[4:7], a[116:119], v[64:67]\n"
  "s_add_u32 m0, s48, 0\n"
  "ds_read_b64_tr_b16 a[36:37], v9 offset:4096\n"
  "v_mfma_f32_16x16x32_bf16 v[68:71], a[12:15], a[116:119], v[68:71]\n"
  "buffer_load_dwordx4 v4, s[44:47], s57 offen offset:0 lds\n"
  "v_mfma_f32_16x16x32_bf16 v[72:75], a[20:23], a[116:119], v[72:75]\n"
  "ds_read_b64_tr_b16 a[38:39], v9 offset:6144\n"
  "v_mfma_f32_16x16x32_bf16 v[76:79], a[28:31], a[116:119], v[76:79]\n"
  "s_waitcnt lgkmcnt(9)\n"
  "v_mfma_f32_16x16x32_bf16 v[80:83], a[0:3], a[120:123], v[80:83]\n"
  "ds_read_b128 a[104:107], v8 offset:10240\n"
  "v_mfma_f32_16x16x32_bf16 v[84:87], a[8:11], a[120:123], v[84:87]\n"
  "ds_read_b128 a[108:111], v8 offset:11264\n"
  "v_mfma_f32_16x16x32_bf16 v[88:91], a[16:19], a[120:123], v[88:91]\n"
  "buffer_load_dwordx4 v5, s[44:47], s57 offen offset:1024 lds\n"
  "v_mfma_f32_16x16x32_bf16 v[92:95], a[24:27], a[120:123], v[92:95]\n"
  "ds_read_b64_tr_b16 a[44:45], v10 offset:4096\n"
  "v_mfma_f32_16x16x32_bf16 v[80:83], a[4:7], a[124:127], v[80:83]\n"
  "s_add_u32 m0, s48, 8192\n"
  "ds_read_b64_tr_b16 a[46:47], v10 offset:6144\n"
  "v_mfma_f32_16x16x32_bf16 v[84:87], a[12:15], a[124:127], v[84:87]\n"
  "buffer_load_dwordx4 v6, s[44:47], s57 offen offset:0 lds\n"
  "v_mfma_f32_16x16x32_bf16 v[88:91], a[20:23], a[124:127], v[88:91]\n"
  "ds_read_b64_tr_b16 a[52:53], v11 offset:4096\n"
  "s_cmp_eq_u32 s50, 0\n"
  "s_cselect_b32 s51, s34, 32768\n"
  "v_mfma_f32_16x16x32_bf16 v[92:95], a[28:31], a[124:127], v[92:95]\n"
  "s_cmp_eq_u32 s50, 1\n"
  "s_cselect_b32 s61, s35, 16384\n"
  "s_waitcnt lgkmcnt(8)\n"
  "v_mfma_f32_16x16x32_bf16 v[96:99], a[0:3], a[96:99], v[96:99]\n"
  "ds_read_b128 a[112:115], v8 offset:12288\n"
  "v_mfma_f32_16x16x32_bf16 v[100:103], a[8:11], a[96:99], v[100:103]\n"
  "ds_read_b128 a[116:119], v8 offset:13312\n"
  "v_mfma_f32_16x16x32_bf16 v[104:107], a[16:19], a[96:99], v[104:107]\n"
  "buffer_load_dwordx4 v7, s[44:47], s57 offen offset:1024 lds\n"
  "v_mfma_f32_16x16x32_bf16 v[108:111], a[24:27], a[96:99], v[108:111]\n"
  "s_add_u32 s57, s57, 131072\n"
  "ds_read_b64_tr_b16 a[54:55], v11 offset:6144\n"
  "v_mfma_f32_16x16x32_bf16 v[96:99], a[4:7], a[100:103], v[96:99]\n"
  "ds_read_b64_tr_b16 a[60:61], v12 offset:4096\n"
  "s_cmp_eq_u32 s50, 0\n"
  "s_cselect_b32 s50, 3, s50\n"
  "v_mfma_f32_16x16x32_bf16 v[100:103], a[12:15], a[100:103], v[100:103]\n"
  "s_cmp_eq_u32 s49, 1\n"
  "s_cselect_b32 s67, s35, 16384\n"
  "ds_read_b64_tr_b16 a[62:63], v12 offset:6144\n"
  "v_mfma_f32_16x16x32_bf16 v[104:107], a[20:23], a[100:103], v[104:107]\n"
  "s_sub_u32 s50, s50, 1\n"
  "v_mfma_f32_16x16x32_bf16 v[108:111], a[28:31], a[100:103], v[108:111]\n"
  "s_waitcnt lgkmcnt(8)\n"
  "v_mfma_f32_16x16x32_bf16 v[112:115], a[0:3], a[104:107], v[112:115]\n"
  "ds_read_b128 a[120:123], v8 offset:14336\n"
  "s_add_u32 s48, s48, s67\n"
  "v_mfma_f32_16x16x32_bf16 v[116:119], a[8:11], a[104:107], v[116:119]\n"
  "ds_read_b128 a[124:127], v8 offset:15360\n"
  "v_mfma_f32_16x16x32_bf16 v[120:123], a[16:19], a[104:107], v[120:123]\n"
  "s_cmp_eq_u32 s49, 0\n"
  "s_cselect_b32 s49, 3, s49\n"
  "v_mfma_f32_16x16x32_bf16 v[124:127], a[24:27], a[104:107], v[124:127]\n"
  "v_add_u32 v8, s51, v8\n"
  "v_mfma_f32_16x16x32_bf16 v[112:115], a[4:7], a[108:111], v[112:115]\n"
  "v_mfma_f32_16x16x32_bf16 v[116:119], a[12:15], a[108:111], v[116:119]\n"
  "s_sub_u32 s49, s49, 1\n"
  "v_mfma_f32_16x16x32_bf16 v[120:123], a[20:23], a[108:111], v[120:123]\n"
  "v_mfma_f32_16x16x32_bf16 v[124:127], a[28:31], a[108:111], v[124:127]\n"
  "s_waitcnt vmcnt(8) lgkmcnt(0)\n"
  "s_barrier\n"
  "v_mfma_f32_16x16x32_bf16 v[128:131], a[0:3], a[112:115], v[128:131]\n"
  "ds_read_b128 a[96:99], v8 offset:0\n"
  "s_xor_b64 s[24:25], s[24:25], s[10:11]\n"
  "s_xor_b64 s[26:27], s[26:27], s[10:11]\n"
  "v_cmp_ne_u64 vcc, s[10:11], v[14:15]\n"
  "v_mfma_f32_16x16x32_bf16 v[132:135], a[8:11], a[112:115], v[132:135]\n"
  "ds_read_b128 a[100:103], v8 offset:1024\n"
  "s_xor_b64 s[28:29], s[28:29], s[10:11]\n"
  "s_xor_b64 s[30:31], s[30:31], s[10:11]\n"
  "s_or_b64 s[24:25], s[24:25], s[26:27]\n"
  "v_mfma_f32_16x16x32_bf16 v[136:139], a[16:19], a[112:115], v[136:139]\n"
  "s_or_b64 s[28:29], s[28:29], s[30:31]\n"
  "s_or_b64 s[24:25], s[24:25], s[28:29]\n"
  "s_cbranch_scc1 K2_FBH_%=\n"
  "K2_OKH_%=:\n"
  "s_mov_b32 m0, s58\n"
  "v_mfma_f32_16x16x32_bf16 v[140:143], a[24:27], a[112:115], v[140:143]\n"
  "buffer_load_dwordx4 v0, s[40:43], s56 offen offset:0 sc1 lds\n"
  "v_mfma_f32_16x16x32_bf16 v[128:131], a[4:7], a[116:119], v[128:131]\n"
  "v_add_u32 v9, s61, v9\n"
  "s_and_b32 s66, vcc_lo, 0xffff\n"
  "s_cbranch_scc1 K2_FBG_%=\n"
  "K2_OKG_%=:\n"
  "v_mfma_f32_16x16x32_bf16 v[132:135], a[12:15], a[116:119], v[132:135]\n"
  "buffer_load_dword v20, v18, s[52:55], s87 offen offset:0 sc1\n"
  "v_mfma_f32_16x16x32_bf16 v[136:139], a[20:23], a[116:119], v[136:139]\n"
  "v_add_u32 v10, s61, v10\n"
  "buffer_load_dword v21, v18, s[52:55], s87 offen offset:64 sc1\n"
  "v_mfma_f32_16x16x32_bf16 v[140:143], a[28:31], a[116:119], v[140:143]\n"
  "buffer_load_dwordx4 v0, s[40:43], s56 offen offset:1024 sc1 lds\n"
  "buffer_load_dword v22, v18, s[52:55], s84 offen offset:0 sc1\n"
  "v_mfma_f32_16x16x32_bf16 v[144:147], a[0:3], a[120:123], v[144:147]\n"
  "ds_read_b128 a[104:107], v8 offset:2048\n"
  "buffer_load_dword v23, v18, s[52:55], s84 offen offset:64 sc1\n"
  "v_mfma_f32_16x16x32_bf16 v[148:151], a[8:11], a[120:123], v[148:151]\n"
  "ds_read_b128 a[108:111], v8 offset:3072\n"
  "buffer_load_dword v24, v18, s[52:55], s85 offen offset:0 sc1\n"
  "v_mfma_f32_16x16x32_bf16 v[152:155], a[16:19], a[120:123], v[152:155]\n"
  "v_add_u32 v11, s61, v11\n"
  "buffer_load_dword v25, v18, s[52:55], s85 offen offset:64 sc1\n"
  "v_mfma_f32_16x16x32_bf16 v[156:159], a[24:27], a[120:123], v[156:159]\n"
  "buffer_load_dwordx4 v0, s[40:43], s56 offen offset:2048 sc1 lds\n"
  "buffer_load_dword v26, v18, s[52:55], s86 offen offset:0 sc1\n"
  "v_mfma_f32_16x16x32_bf16 v[144:147], a[4:7], a[124:127], v[144:147]\n"
  "v_add_u32 v12, s61, v12\n"
  "buffer_load_dword v27, v18, s[52:55], s86 offen offset:64 sc1\n"
  "v_mfma_f32_16x16x32_bf16 v[148:151], a[12:15], a[124:127], v[148:151]\n"
  "v_mfma_f32_16x16x32_bf16 v[152:155], a[20:23], a[124:127], v[152:155]\n"
  "v_mov_b32 v18, 0\n"
  "v_mfma_f32_16x16x32_bf16 v[156:159], a[28:31], a[124:127], v[156:159]\n"
  "buffer_load_dwordx4 v0, s[40:43], s56 offen offset:3072 sc1 lds\n"
  "s_waitcnt lgkmcnt(2)\n"
  "v_mfma_f32_16x16x32_bf16 v[32:35], a[32:35], a[96:99], v[32:35]\n"
  "ds_read_b128 a[112:115], v8 offset:4096\n"
  "v_mfma_f32_16x16x32_bf16 v[36:39], a[40:43], a[96:99], v[36:39]\n"
  "ds_read_b128 a[116:119], v8 offset:5120\n"
  "s_mov_b32 m0, s59\n"
  "v_mfma_f32_16x16x32_bf16 v[40:43], a[48:51], a[96:99], v[40:43]\n"
  "buffer_load_dwordx4 v1, s[40:43], s56 offen offset:0 sc1 lds\n"
  "v_mfma_f32_16x16x32_bf16 v[44:47], a[56:59], a[96:99], v[44:47]\n"
  "ds_read_b64_tr_b16 a[0:1], v9 offset:0\n"
  "v_mfma_f32_16x16x32_bf16 v[32:35], a[36:39], a[100:103], v[32:35]\n"
  "ds_read_b64_tr_b16 a[2:3], v9 offset:2048\n"
  "v_mfma_f32_16x16x32_bf16 v[36:39], a[44:47], a[100:103], v[36:39]\n"
  "v_mfma_f32_16x16x32_bf16 v[40:43], a[52:55], a[100:103], v[40:43]\n"
  "buffer_load_dwordx4 v1, s[40:43], s56 offen offset:1024 sc1 lds\n"
  "v_mfma_f32_16x16x32_bf16 v[44:47], a[60:63], a[100:103], v[44:47]\n"
  "ds_read_b64_tr_b16 a[8:9], v10 offset:0\n"
  "s_waitcnt lgkmcnt(5)\n"
  "v_mfma_f32_16x16x32_bf16 v[48:51], a[32:35], a[104:107], v[48:51]\n"
  "ds_read_b128 a[120:123], v8 offset:6144\n"
  "v_mfma_f32_16x16x32_bf16 v[52:55], a[40:43], a[104:107], v[52:55]\n"
  "ds_read_b128 a[124:127], v8 offset:7168\n"
  "v_mfma_f32_16x16x32_bf16 v[56:59], a[48:51], a[104:107], v[56:59]\n"
  "buffer_load_dwordx4 v1, s[40:43], s56 offen offset:2048 sc1 lds\n"
  "v_mfma_f32_16x16x32_bf16 v[60:63], a[56:59], a[104:107], v[60:63]\n"
  "ds_read_b64_tr_b16 a[10:11], v10 offset:2048\n"
  "v_mfma_f32_16x16x32_bf16 v[48:51], a[36:39], a[108:111], v[48:51]\n"
  "ds_read_b64_tr_b16 a[16:17], v11 offset:0\n"
  "v_mfma_f32_16x16x32_bf16 v[52:55], a[44:47], a[108:111], v[52:55]\n"
  "ds_read_b64_tr_b16 a[18:19], v11 offset:2048\n"
  "v_mfma_f32_16x16x32_bf16 v[56:59], a[52:55], a[108:111], v[56:59]\n"
  "buffer_load_dwordx4 v1, s[40:43], s56 offen offset:3072 sc1 lds\n"
  "v_mfma_f32_16x16x32_bf16 v[60:63], a[60:63], a[108:111], v[60:63]\n"
  "s_add_u32 s56, s56, 4096\n"
  "ds_read_b64_tr_b16 a[24:25], v12 offset:0\n"
  "s_waitcnt lgkmcnt(9)\n"
  "v_mfma_f32_16x16x32_bf16 v[64:67], a[32:35], a[112:115], v[64:67]\n"
  "ds_read_b128 a[96:99], v8 offset:8192\n"
  "s_cmp_eq_u32 s49, 0\n"
  "s_cselect_b32 s66, s34, 32768\n"
  "v_mfma_f32_16x16x32_bf16 v[68:71], a[40:43], a[112:115], v[68:71]\n"
  "ds_read_b128 a[100:103], v8 offset:9216\n"
  "s_add_u32 s58, s58, s66\n"
  "v_mfma_f32_16x16x32_bf16 v[72:75], a[48:51], a[112:115], v[72:75]\n"
  "s_add_u32 s59, s59, s66\n"
  "ds_read_b64_tr_b16 a[26:27], v12 offset:2048\n"
  "v_mfma_f32_16x16x32_bf16 v[76:79], a[56:59], a[112:115], v[76:79]\n"
  "v_mfma_f32_16x16x32_bf16 v[64:67], a[36:39], a[116:119], v[64:67]\n"
  "s_add_u32 m0, s48, 0\n"
  "ds_read_b64_tr_b16 a[4:5], v9 offset:4096\n"
  "v_mfma_f32_16x16x32_bf16 v[68:71], a[44:47], a[116:119], v[68:71]\n"
  "buffer_load_dwordx4 v4, s[44:47], s57 offen offset:0 lds\n"
  "v_mfma_f32_16x16x32_bf16 v[72:75], a[52:55], a[116:119], v[72:75]\n"
  "ds_read_b64_tr_b16 a[6:7], v9 offset:6144\n"
  "v_mfma_f32_16x16x32_bf16 v[76:79], a[60:63], a[116:119], v[76:79]\n"
  "s_waitcnt lgkmcnt(9)\n"
  "v_mfma_f32_16x16x32_bf16 v[80:83], a[32:35], a[120:123], v[80:83]\n"
  "ds_read_b128 a[104:107], v8 offset:10240\n"
  "v_mfma_f32_16x16x32_bf16 v[84:87], a[40:43], a[120:123], v[84:87]\n"
  "ds_read_b128 a[108:111], v8 offset:11264\n"
  "v_mfma_f32_16x16x32_bf16 v[88:91], a[48:51], a[120:123], v[88:91]\n"
  "buffer_load_dwordx4 v5, s[44:47], s57 offen offset:1024 lds\n"
  "v_mfma_f32_16x16x32_bf16 v[92:95], a[56:59], a[120:123], v[92:95]\n"
  "ds_read_b64_tr_b16 a[12:13], v10 offset:4096\n"
  "v_mfma_f32_16x16x32_bf16 v[80:83], a[36:39], a[124:127], v[80:83]\n"
  "s_add_u32 m0, s48, 8192\n"
  "ds_read_b64_tr_b16 a[14:15], v10 offset:6144\n"
  "v_mfma_f32_16x16x32_bf16 v[84:87], a[44:47], a[124:127], v[84:87]\n"
  "buffer_load_dwordx4 v6, s[44:47], s57 offen offset:0 lds\n"
  "v_mfma_f32_16x16x32_bf16 v[88:91], a[52:55], a[124:127], v[88:91]\n"
  "ds_read_b64_tr_b16 a[20:21], v11 offset:4096\n"
  "s_cmp_eq_u32 s50, 0\n"
  "s_cselect_b32 s51, s34, 32768\n"
  "v_mfma_f32_16x16x32_bf16 v[92:95], a[60:63], a[124:127], v[92:95]\n"
  "s_cmp_eq_u32 s50, 1\n"
  "s_cselect_b32 s61, s35, 16384\n"
  "s_waitcnt vmcnt(8)\n"
  "s_waitcnt lgkmcnt(8)\n"
  "v_mfma_f32_16x16x32_bf16 v[96:99], a[32:35], a[96:99], v[96:99]\n"
  "ds_read_b128 a[112:115], v8 offset:12288\n"
  "v_sub_f32 v208, 1.0, v20\n"
  "v_mfma_f32_16x16x32_bf16 v[100:103], a[40:43], a[96:99], v[100:103]\n"
  "ds_read_b128 a[116:119], v8 offset:13312\n"
  "v_sub_f32 v209, 1.0, v21\n"
  "v_mfma_f32_16x16x32_bf16 v[104:107], a[48:51], a[96:99], v[104:107]\n"
  "buffer_load_dwordx4 v7, s[44:47], s57 offen offset:1024 lds\n"
  "v_sub_f32 v210, 1.0, v22\n"
  "v_mfma_f32_16x16x32_bf16 v[108:111], a[56:59], a[96:99], v[108:111]\n"
  "s_add_u32 s57, s57, 131072\n"
  "ds_read_b64_tr_b16 a[22:23], v11 offset:6144\n"
  "v_sub_f32 v211, 1.0, v23\n"
  "v_mfma_f32_16x16x32_bf16 v[96:99], a[36:39], a[100:103], v[96:99]\n"
  "ds_read_b64_tr_b16 a[28:29], v12 offset:4096\n"
  "s_cmp_eq_u32 s50, 0\n"
  "s_cselect_b32 s50, 3, s50\n"
  "v_sub_f32 v212, 1.0, v24\n"
  "v_mfma_f32_16x16x32_bf16 v[100:103], a[44:47], a[100:103], v[100:103]\n"
  "s_cmp_eq_u32 s49, 1\n"
  "s_cselect_b32 s67, s35, 16384\n"
  "ds_read_b64_tr_b16 a[30:31], v12 offset:6144\n"
  "v_sub_f32 v213, 1.0, v25\n"
  "v_mfma_f32_16x16x32_bf16 v[104:107], a[52:55], a[100:103], v[104:107]\n"
  "s_sub_u32 s50, s50, 1\n"
  "v_sub_f32 v214, 1.0, v26\n"
  "v_mfma_f32_16x16x32_bf16 v[108:111], a[60:63], a[100:103], v[108:111]\n"
  "v_sub_f32 v215, 1.0, v27\n"
  "s_waitcnt lgkmcnt(8)\n"
  "v_mfma_f32_16x16x32_bf16 v[112:115], a[32:35], a[104:107], v[112:115]\n"
  "ds_read_b128 a[120:123], v8 offset:14336\n"
  "s_add_u32 s48, s48, s67\n"
  "v_max_f32 v208, 0x21800000, v208\n"
  "v_mfma_f32_16x16x32_bf16 v[116:119], a[40:43], a[104:107], v[116:119]\n"
  "ds_read_b128 a[124:127], v8 offset:15360\n"
  "v_max_f32 v209, 0x21800000, v209\n"
  "v_mfma_f32_16x16x32_bf16 v[120:123], a[48:51], a[104:107], v[120:123]\n"
  "s_cmp_eq_u32 s49, 0\n"
  "s_cselect_b32 s49, 3, s49\n"
  "v_max_f32 v210, 0x21800000, v210\n"
  "v_mfma_f32_16x16x32_bf16 v[124:127], a[56:59], a[104:107], v[124:127]\n"
  "v_add_u32 v8, s51, v8\n"
  "v_max_f32 v211, 0x21800000, v211\n"
  "v_mfma_f32_16x16x32_bf16 v[112:115], a[36:39], a[108:111], v[112:115]\n"
  "v_max_f32 v212, 0x21800000, v212\n"
  "v_mfma_f32_16x16x32_bf16 v[116:119], a[44:47], a[108:111], v[116:119]\n"
  "s_sub_u32 s49, s49, 1\n"
  "v_max_f32 v213, 0x21800000, v213\n"
  "v_mfma_f32_16x16x32_bf16 v[120:123], a[52:55], a[108:111], v[120:123]\n"
  "v_max_f32 v214, 0x21800000, v214\n"
  "v_mfma_f32_16x16x32_bf16 v[124:127], a[60:63], a[108:111], v[124:127]\n"
  "v_max_f32 v215, 0x21800000, v215\n"
  "s_waitcnt lgkmcnt(0)\n"
  "s_barrier\n"
  "v_mfma_f32_16x16x32_bf16 v[128:131], a[32:35], a[112:115], v[128:131]\n"
  "ds_read_b128 a[96:99], v8 offset:0\n"
  "v_rcp_f32 v216, v208\n"
  "v_mfma_f32_16x16x32_bf16 v[132:135], a[40:43], a[112:115], v[132:135]\n"
  "ds_read_b128 a[100:103], v8 offset:1024\n"
  "v_rcp_f32 v217, v209\n"
  "v_mfma_f32_16x16x32_bf16 v[136:139], a[48:51], a[112:115], v[136:139]\n"
  "s_mov_b32 m0, s58\n"
  "v_rcp_f32 v218, v210\n"
  "v_mfma_f32_16x16x32_bf16 v[140:143], a[56:59], a[112:115], v[140:143]\n"
  "buffer_load_dwordx4 v0, s[40:43], s56 offen offset:0 sc1 lds\n"
  "v_rcp_f32 v219, v211\n"
  "v_mfma_f32_16x16x32_bf16 v[128:131], a[36:39], a[116:119], v[128:131]\n"
  "v_add_u32 v9, s61, v9\n"
  "v_rcp_f32 v220, v212\n"
  "v_mfma_f32_16x16x32_bf16 v[132:135], a[44:47], a[116:119], v[132:135]\n"
  "v_rcp_f32 v221, v213\n"
  "v_mfma_f32_16x16x32_bf16 v[136:139], a[52:55], a[116:119], v[136:139]\n"
  "v_add_u32 v10, s61, v10\n"
  "v_rcp_f32 v222, v214\n"
  "v_mfma_f32_16x16x32_bf16 v[140:143], a[60:63], a[116:119], v[140:143]\n"
  "buffer_load_dwordx4 v0, s[40:43], s56 offen offset:1024 sc1 lds\n"
  "v_rcp_f32 v223, v215\n"
  "v_mfma_f32_16x16x32_bf16 v[144:147], a[32:35], a[120:123], v[144:147]\n"
  "ds_read_b128 a[104:107], v8 offset:2048\n"
  "v_mul_f32 v20, v216, v20\n"
  "v_mfma_f32_16x16x32_bf16 v[148:151], a[40:43], a[120:123], v[148:151]\n"
  "ds_read_b128 a[108:111], v8 offset:3072\n"
  "v_mul_f32 v21, v217, v21\n"
  "v_mfma_f32_16x16x32_bf16 v[152:155], a[48:51], a[120:123], v[152:155]\n"
  "v_add_u32 v11, s61, v11\n"
  "v_mul_f32 v22, v218, v22\n"
  "v_mfma_f32_16x16x32_bf16 v[156:159], a[56:59], a[120:123], v[156:159]\n"
  "buffer_load_dwordx4 v0, s[40:43], s56 offen offset:2048 sc1 lds\n"
  "v_mul_f32 v23, v219, v23\n"
  "v_mfma_f32_16x16x32_bf16 v[144:147], a[36:39], a[124:127], v[144:147]\n"
  "v_add_u32 v12, s61, v12\n"
  "v_mul_f32 v24, v220, v24\n"
  "v_mfma_f32_16x16x32_bf16 v[148:151], a[44:47], a[124:127], v[148:151]\n"
  "v_mul_f32 v25, v221, v25\n"
  "v_mfma_f32_16x16x32_bf16 v[152:155], a[52:55], a[124:127], v[152:155]\n"
  "v_mul_f32 v26, v222, v26\n"
  "v_mfma_f32_16x16x32_bf16 v[156:159], a[60:63], a[124:127], v[156:159]\n"
  "buffer_load_dwordx4 v0, s[40:43], s56 offen offset:3072 sc1 lds\n"
  "v_mul_f32 v27, v223, v27\n"
  "s_waitcnt lgkmcnt(2)\n"
  "v_mfma_f32_16x16x32_bf16 v[32:35], a[0:3], a[96:99], v[32:35]\n"
  "ds_read_b128 a[112:115], v8 offset:4096\n"
  "v_mfma_f32_16x16x32_bf16 v[36:39], a[8:11], a[96:99], v[36:39]\n"
  "ds_read_b128 a[116:119], v8 offset:5120\n"
  "s_mov_b32 m0, s59\n"
  "v_mfma_f32_16x16x32_bf16 v[40:43], a[16:19], a[96:99], v[40:43]\n"
  "buffer_load_dwordx4 v1, s[40:43], s56 offen offset:0 sc1 lds\n"
  "v_mfma_f32_16x16x32_bf16 v[44:47], a[24:27], a[96:99], v[44:47]\n"
  "ds_read_b64_tr_b16 a[32:33], v9 offset:0\n"
  "v_mfma_f32_16x16x32_bf16 v[32:35], a[4:7], a[100:103], v[32:35]\n"
  "ds_read_b64_tr_b16 a[34:35], v9 offset:2048\n"
  "v_mfma_f32_16x16x32_bf16 v[36:39], a[12:15], a[100:103], v[36:39]\n"
  "v_mfma_f32_16x16x32_bf16 v[40:43], a[20:23], a[100:103], v[40:43]\n"
  "buffer_load_dwordx4 v1, s[40:43], s56 offen offset:1024 sc1 lds\n"
  "v_mfma_f32_16x16x32_bf16 v[44:47], a[28:31], a[100:103], v[44:47]\n"
  "ds_read_b64_tr_b16 a[40:41], v10 offset:0\n"
  "s_waitcnt lgkmcnt(5)\n"
  "v_mfma_f32_16x16x32_bf16 v[48:51], a[0:3], a[104:107], v[48:51]\n"
  "ds_read_b128 a[120:123], v8 offset:6144\n"
  "v_mfma_f32_16x16x32_bf16 v[52:55], a[8:11], a[104:107], v[52:55]\n"
  "ds_read_b128 a[124:127], v8 offset:7168\n"
  "v_mfma_f32_16x16x32_bf16 v[56:59], a[16:19], a[104:107], v[56:59]\n"
  "buffer_load_dwordx4 v1, s[40:43], s56 offen offset:2048 sc1 lds\n"
  "v_mul_f32 v32, v20, v32\n"
  "v_mul_f32 v33, v20, v33\n"
  "v_mfma_f32_16x16x32_bf16 v[60:63], a[24:27], a[104:107], v[60:63]\n"
  "ds_read_b64_tr_b16 a[42:43], v10 offset:2048\n"
  "v_mfma_f32_16x16x32_bf16 v[48:51], a[4:7], a[108:111], v[48:51]\n"
  "ds_read_b64_tr_b16 a[48:49], v11 offset:0\n"
  "v_mfma_f32_16x16x32_bf16 v[52:55], a[12:15], a[108:111], v[52:55]\n"
  "ds_read_b64_tr_b16 a[50:51], v11 offset:2048\n"
  "v_mfma_f32_16x16x32_bf16 v[56:59], a[20:23], a[108:111], v[56:59]\n"
  "buffer_load_dwordx4 v1, s[40:43], s56 offen offset:3072 sc1 lds\n"
  "v_mul_f32 v34, v20, v34\n"
  "v_mul_f32 v35, v20, v35\n"
  "v_mfma_f32_16x16x32_bf16 v[60:63], a[28:31], a[108:111], v[60:63]\n"
  "s_add_u32 s56, s56, 4096\n"
  "ds_read_b64_tr_b16 a[56:57], v12 offset:0\n"
  "s_waitcnt lgkmcnt(9)\n"
  "v_mfma_f32_16x16x32_bf16 v[64:67], a[0:3], a[112:115], v[64:67]\n"
  "ds_read_b128 a[96:99], v8 offset:8192\n"
  "s_cmp_eq_u32 s49, 0\n"
  "s_cselect_b32 s66, s34, 32768\n"
  "v_mul_f32 v36, v20, v36\n"
  "v_mfma_f32_16x16x32_bf16 v[68:71], a[8:11], a[112:115], v[68:71]\n"
  "ds_read_b128 a[100:103], v8 offset:9216\n"
  "s_add_u32 s58, s58, s66\n"
  "v_mfma_f32_16x16x32_bf16 v[72:75], a[16:19], a[112:115], v[72:75]\n"
  "s_add_u32 s59, s59, s66\n"
  "ds_read_b64_tr_b16 a[58:59], v12 offset:2048\n"
  "v_mfma_f32_16x16x32_bf16 v[76:79], a[24:27], a[112:115], v[76:79]\n"
  "v_mul_f32 v37, v20, v37\n"
  "v_mul_f32 v38, v20, v38\n"
  "v_mul_f32 v39, v20, v39\n"
  "v_mul_f32 v40, v20, v40\n"
  "v_mfma_f32_16x16x32_bf16 v[64:67], a[4:7], a[116:119], v[64:67]\n"
  "s_add_u32 m0, s48, 0\n"
  "ds_read_b64_tr_b16 a[36:37], v9 offset:4096\n"
  "v_mfma_f32_16x16x32_bf16 v[68:71], a[12:15], a[116:119], v[68:71]\n"
  "buffer_load_dwordx4 v4, s[44:47], s57 offen offset:0 lds\n"
  "v_mul_f32 v41, v20, v41\n"
  "v_mfma_f32_16x16x32_bf16 v[72:75], a[20:23], a[116:119], v[72:75]\n"
  "ds_read_b64_tr_b16 a[38:39], v9 offset:6144\n"
  "v_mfma_f32_16x16x32_bf16 v[76:79], a[28:31], a[116:119], v[76:79]\n"
  "v_mul_f32 v42, v20, v42\n"
  "v_mul_f32 v43, v20, v43\n"
  "v_mul_f32 v44, v20, v44\n"
  "v_mul_f32 v45, v20, v45\n"
  "s_waitcnt lgkmcnt(9)\n"
  "v_mfma_f32_16x16x32_bf16 v[80:83], a[0:3], a[120:123], v[80:83]\n"
  "ds_read_b128 a[104:107], v8 offset:10240\n"
  "v_mfma_f32_16x16x32_bf16 v[84:87], a[8:11], a[120:123], v[84:87]\n"
  "ds_read_b128 a[108:111], v8 offset:11264\n"
  "v_mfma_f32_16x16x32_bf16 v[88:91], a[16:19], a[120:123], v[88:91]\n"
  "buffer_load_dwordx4 v5, s[44:47], s57 offen offset:1024 lds\n"
  "v_mul_f32 v46, v20, v46\n"
  "v_mfma_f32_16x16x32_bf16 v[92:95], a[24:27], a[120:123], v[92:95]\n"
  "ds_read_b64_tr_b16 a[44:45], v10 offset:4096\n"
  "v_mfma_f32_16x16x32_bf16 v[80:83], a[4:7], a[124:127], v[80:83]\n"
  "s_add_u32 m0, s48, 8192\n"
  "ds_read_b64_tr_b16 a[46:47], v10 offset:6144\n"
  "v_mfma_f32_16x16x32_bf16 v[84:87], a[12:15], a[124:127], v[84:87]\n"
  "buffer_load_dwordx4 v6, s[44:47], s57 offen offset:0 lds\n"
  "v_mul_f32 v47, v20, v47\n"
  "v_mul_f32 v48, v21, v48\n"
  "v_mfma_f32_16x16x32_bf16 v[88:91], a[20:23], a[124:127], v[88:91]\n"
  "ds_read_b64_tr_b16 a[52:53], v11 offset:4096\n"
  "s_cmp_eq_u32 s50, 0\n"
  "s_cselect_b32 s51, s34, 32768\n"
  "v_mul_f32 v49, v21, v49\n"
  "v_mul_f32 v50, v21, v50\n"
  "v_mfma_f32_16x16x32_bf16 v[92:95], a[28:31], a[124:127], v[92:95]\n"
  "s_cmp_eq_u32 s50, 1\n"
  "s_cselect_b32 s61, s35, 16384\n"
  "v_mul_f32 v51, v21, v51\n"
  "s_waitcnt lgkmcnt(8)\n"
  "v_mfma_f32_16x16x32_bf16 v[96:99], a[0:3], a[96:99], v[96:99]\n"
  "ds_read_b128 a[112:115], v8 offset:12288\n"
  "v_mfma_f32_16x16x32_bf16 v[100:103], a[8:11], a[96:99], v[100:103]\n"
  "ds_read_b128 a[116:119], v8 offset:13312\n"
  "v_mfma_f32_16x16x32_bf16 v[104:107], a[16:19], a[96:99], v[104:107]\n"
  "buffer_load_dwordx4 v7, s[44:47], s57 offen offset:1024 lds\n"
  "v_mul_f32 v52, v21, v52\n"
  "v_mul_f32 v53, v21, v53\n"
  "v_mfma_f32_16x16x32_bf16 v[108:111], a[24:27], a[96:99], v[108:111]\n"
  "s_add_u32 s57, s57, 131072\n"
  "ds_read_b64_tr_b16 a[54:55], v11 offset:6144\n"
  "v_mfma_f32_16x16x32_bf16 v[96:99], a[4:7], a[100:103], v[96:99]\n"
  "ds_read_b64_tr_b16 a[60:61], v12 offset:4096\n"
  "s_cmp_eq_u32 s50, 0\n"
  "s_cselect_b32 s50, 3, s50\n"
  "v_mul_f32 v54, v21, v54\n"
  "v_mfma_f32_16x16x32_bf16 v[100:103], a[12:15], a[100:103], v[100:103]\n"
  "s_cmp_eq_u32 s49, 1\n"
  "s_cselect_b32 s67, s35, 16384\n"
  "ds_read_b64_tr_b16 a[62:63], v12 offset:6144\n"
  "v_mul_f32 v55, v21, v55\n"
  "v_mul_f32 v56, v21, v56\n"
  "v_mfma_f32_16x16x32_bf16 v[104:107], a[20:23], a[100:103], v[104:107]\n"
  "s_sub_u32 s50, s50, 1\n"
  "v_mul_f32 v57, v21, v57\n"
  "v_mul_f32 v58, v21, v58\n"
  "v_mfma_f32_16x16x32_bf16 v[108:111], a[28:31], a[100:103], v[108:111]\n"
  "v_mul_f32 v59, v21, v59\n"
  "v_mul_f32 v60, v21, v60\n"
  "v_mul_f32 v61, v21, v61\n"
  "s_waitcnt lgkmcnt(8)\n"
  "v_mfma_f32_16x16x32_bf16 v[112:115], a[0:3], a[104:107], v[112:115]\n"
  "ds_read_b128 a[120:123], v8 offset:14336\n"
  "s_add_u32 s48, s48, s67\n"
  "v_mfma_f32_16x16x32_bf16 v[116:119], a[8:11], a[104:107], v[116:119]\n"
  "ds_read_b128 a[124:127], v8 offset:15360\n"
  "v_mfma_f32_16x16x32_bf16 v[120:123], a[16:19], a[104:107], v[120:123]\n"
  "s_cmp_eq_u32 s49, 0\n"
  "s_cselect_b32 s49, 3, s49\n"
  "v_mul_f32 v62, v21, v62\n"
  "v_mul_f32 v63, v21, v63\n"
  "v_mfma_f32_16x16x32_bf16 v[124:127], a[24:27], a[104:107], v[124:127]\n"
  "v_add_u32 v8, s51, v8\n"
  "v_mul_f32 v64, v22, v64\n"
  "v_mfma_f32_16x16x32_bf16 v[112:115], a[4:7], a[108:111], v[112:115]\n"
  "v_mul_f32 v65, v22, v65\n"
  "v_mul_f32 v66, v22, v66\n"
  "v_mul_f32 v67, v22, v67\n"
  "v_mul_f32 v68, v22, v68\n"
  "v_mfma_f32_16x16x32_bf16 v[116:119], a[12:15], a[108:111], v[116:119]\n"
  "s_sub_u32 s49, s49, 1\n"
  "v_mul_f32 v69, v22, v69\n"
  "v_mfma_f32_16x16x32_bf16 v[120:123], a[20:23], a[108:111], v[120:123]\n"
  "v_mul_f32 v70, v22, v70\n"
  "v_mul_f32 v71, v22, v71\n"
  "v_mul_f32 v72, v22, v72\n"
  "v_mul_f32 v73, v22, v73\n"
  "v_mfma_f32_16x16x32_bf16 v[124:127], a[28:31], a[108:111], v[124:127]\n"
  "v_mul_f32 v74, v22, v74\n"
  "v_mul_f32 v75, v22, v75\n"
  "v_mul_f32 v76, v22, v76\n"
  "s_waitcnt vmcnt(12) lgkmcnt(0)\n"
  "s_barrier\n"
  "v_mfma_f32_16x16x32_bf16 v[128:131], a[0:3], a[112:115], v[128:131]\n"
  "ds_read_b128 a[96:99], v8 offset:0\n"
  "v_mfma_f32_16x16x32_bf16 v[132:135], a[8:11], a[112:115], v[132:135]\n"
  "ds_read_b128 a[100:103], v8 offset:1024\n"
  "v_mfma_f32_16x16x32_bf16 v[136:139], a[16:19], a[112:115], v[136:139]\n"
  "s_mov_b32 m0, s58\n"
  "v_mul_f32 v77, v22, v77\n"
  "v_mfma_f32_16x16x32_bf16 v[140:143], a[24:27], a[112:115], v[140:143]\n"
  "buffer_load_dwordx4 v0, s[40:43], s56 offen offset:0 sc1 lds\n"
  "v_mul_f32 v78, v22, v78\n"
  "v_mul_f32 v79, v22, v79\n"
  "v_mfma_f32_16x16x32_bf16 v[128:131], a[4:7], a[116:119], v[128:131]\n"
  "v_add_u32 v9, s61, v9\n"
  "v_mul_f32 v80, v23, v80\n"
  "v_mul_f32 v81, v23, v81\n"
  "v_mfma_f32_16x16x32_bf16 v[132:135], a[12:15], a[116:119], v[132:135]\n"
  "v_mul_f32 v82, v23, v82\n"
  "v_mul_f32 v83, v23, v83\n"
  "v_mul_f32 v84, v23, v84\n"
  "v_mfma_f32_16x16x32_bf16 v[136:139], a[20:23], a[116:119], v[136:139]\n"
  "v_add_u32 v10, s61, v10\n"
  "v_mul_f32 v85, v23, v85\n"
  "v_mul_f32 v86, v23, v86\n"
  "v_mfma_f32_16x16x32_bf16 v[140:143], a[28:31], a[116:119], v[140:143]\n"
  "buffer_load_dwordx4 v0, s[40:43], s56 offen offset:1024 sc1 lds\n"
  "v_mul_f32 v87, v23, v87\n"
  "v_mfma_f32_16x16x32_bf16 v[144:147], a[0:3], a[120:123], v[144:147]\n"
  "ds_read_b128 a[104:107], v8 offset:2048\n"
  "v_mfma_f32_16x16x32_bf16 v[148:151], a[8:11], a[120:123], v[148:151]\n"
  "ds_read_b128 a[108:111], v8 offset:3072\n"
  "v_mfma_f32_16x16x32_bf16 v[152:155], a[16:19], a[120:123], v[152:155]\n"
  "v_add_u32 v11, s61, v11\n"
  "v_mul_f32 v88, v23, v88\n"
  "v_mul_f32 v89, v23, v89\n"
  "v_mfma_f32_16x16x32_bf16 v[156:159], a[24:27], a[120:123], v[156:159]\n"
  "buffer_load_dwordx4 v0, s[40:43], s56 offen offset:2048 sc1 lds\n"
  "v_mul_f32 v90, v23, v90\n"
  "v_mul_f32 v91, v23, v91\n"
  "v_mfma_f32_16x16x32_bf16 v[144:147], a[4:7], a[124:127], v[144:147]\n"
  "v_add_u32 v12, s61, v12\n"
  "v_mul_f32 v92, v23, v92\n"
  "v_mfma_f32_16x16x32_bf16 v[148:151], a[12:15], a[124:127], v[148:151]\n"
  "v_mul_f32 v93, v23, v93\n"
  "v_mul_f32 v94, v23, v94\n"
  "v_mul_f32 v95, v23, v95\n"
  "v_mfma_f32_16x16x32_bf16 v[152:155], a[20:23], a[124:127], v[152:155]\n"
  "v_mul_f32 v96, v24, v96\n"
  "v_mul_f32 v97, v24, v97\n"
  "v_mul_f32 v98, v24, v98\n"
  "v_mul_f32 v99, v24, v99\n"
  "v_mfma_f32_16x16x32_bf16 v[156:159], a[28:31], a[124:127], v[156:159]\n"
  "buffer_load_dwordx4 v0, s[40:43], s56 offen offset:3072 sc1 lds\n"
  "v_mul_f32 v100, v24, v100\n"
  "s_waitcnt lgkmcnt(2)\n"
  "v_mfma_f32_16x16x32_bf16 v[32:35], a[32:35], a[96:99], v[32:35]\n"
  "ds_read_b128 a[112:115], v8 offset:4096\n"
  "v_mfma_f32_16x16x32_bf16 v[36:39], a[40:43], a[96:99], v[36:39]\n"
  "ds_read_b128 a[116:119], v8 offset:5120\n"
  "s_mov_b32 m0, s59\n"
  "v_mfma_f32_16x16x32_bf16 v[40:43], a[48:51], a[96:99], v[40:43]\n"
  "buffer_load_dwordx4 v1, s[40:43], s56 offen offset:0 sc1 lds\n"
  "v_mul_f32 v101, v24, v101\n"
  "v_mul_f32 v102, v24, v102\n"
  "v_mfma_f32_16x16x32_bf16 v[44:47], a[56:59], a[96:99], v[44:47]\n"
  "ds_read_b64_tr_b16 a[0:1], v9 offset:0\n"
  "v_mfma_f32_16x16x32_bf16 v[32:35], a[36:39], a[100:103], v[32:35]\n"
  "ds_read_b64_tr_b16 a[2:3], v9 offset:2048\n"
  "v_mfma_f32_16x16x32_bf16 v[36:39], a[44:47], a[100:103], v[36:39]\n"
  "v_mul_f32 v103, v24, v103\n"
  "v_mul_f32 v104, v24, v104\n"
  "v_mul_f32 v105, v24, v105\n"
  "v_mfma_f32_16x16x32_bf16 v[40:43], a[52:55], a[100:103], v[40:43]\n"
  "buffer_load_dwordx4 v1, s[40:43], s56 offen offset:1024 sc1 lds\n"
  "v_mul_f32 v106, v24, v106\n"
  "v_mul_f32 v107, v24, v107\n"
  "v_mfma_f32_16x16x32_bf16 v[44:47], a[60:63], a[100:103], v[44:47]\n"
  "ds_read_b64_tr_b16 a[8:9], v10 offset:0\n"
  "s_waitcnt lgkmcnt(5)\n"
  "v_mfma_f32_16x16x32_bf16 v[48:51], a[32:35], a[104:107], v[48:51]\n"
  "ds_read_b128 a[120:123], v8 offset:6144\n"
  "v_mfma_f32_16x16x32_bf16 v[52:55], a[40:43], a[104:107], v[52:55]\n"
  "ds_read_b128 a[124:127], v8 offset:7168\n"
  "v_mfma_f32_16x16x32_bf16 v[56:59], a[48:51], a[104:107], v[56:59]\n"
  "buffer_load_dwordx4 v1, s[40:43], s56 offen offset:2048 sc1 lds\n"
  "v_mul_f32 v108, v24, v108\n"
  "v_mul_f32 v109, v24, v109\n"
  "v_mfma_f32_16x16x32_bf16 v[60:63], a[56:59], a[104:107], v[60:63]\n"
  "ds_read_b64_tr_b16 a[10:11], v10 offset:2048\n"
  "v_mfma_f32_16x16x32_bf16 v[48:51], a[36:39], a[108:111], v[48:51]\n"
  "ds_read_b64_tr_b16 a[16:17], v11 offset:0\n"
  "v_mfma_f32_16x16x32_bf16 v[52:55], a[44:47], a[108:111], v[52:55]\n"
  "ds_read_b64_tr_b16 a[18:19], v11 offset:2048\n"
  "v_mfma_f32_16x16x32_bf16 v[56:59], a[52:55], a[108:111], v[56:59]\n"
  "buffer_load_dwordx4 v1, s[40:43], s56 offen offset:3072 sc1 lds\n"
  "v_mul_f32 v110, v24, v110\n"
  "v_mfma_f32_16x16x32_bf16 v[60:63], a[60:63], a[108:111], v[60:63]\n"
  "s_add_u32 s56, s56, 4096\n"
  "ds_read_b64_tr_b16 a[24:25], v12 offset:0\n"
  "s_waitcnt lgkmcnt(9)\n"
  "v_mfma_f32_16x16x32_bf16 v[64:67], a[32:35], a[112:115], v[64:67]\n"
  "ds_read_b128 a[96:99], v8 offset:8192\n"
  "s_cmp_eq_u32 s49, 0\n"
  "s_cselect_b32 s66, s34, 32768\n"
  "v_mul_f32 v111, v24, v111\n"
  "v_mul_f32 v112, v25, v112\n"
  "v_mfma_f32_16x16x32_bf16 v[68:71], a[40:43], a[112:115], v[68:71]\n"
  "ds_read_b128 a[100:103], v8 offset:9216\n"
  "s_add_u32 s58, s58, s66\n"
  "v_mfma_f32_16x16x32_bf16 v[72:75], a[48:51], a[112:115], v[72:75]\n"
  "s_add_u32 s59, s59, s66\n"
  "ds_read_b64_tr_b16 a[26:27], v12 offset:2048\n"
  "v_mfma_f32_16x16x32_bf16 v[76:79], a[56:59], a[112:115], v[76:79]\n"
  "v_mul_f32 v113, v25, v113\n"
  "v_mul_f32 v114, v25, v114\n"
  "v_mul_f32 v115, v25, v115\n"
  "v_mfma_f32_16x16x32_bf16 v[64:67], a[36:39], a[116:119], v[64:67]\n"
  "s_add_u32 m0, s48, 0\n"
  "ds_read_b64_tr_b16 a[4:5], v9 offset:4096\n"
  "v_mfma_f32_16x16x32_bf16 v[68:71], a[44:47], a[116:119], v[68:71]\n"
  "buffer_load_dwordx4 v4, s[44:47], s57 offen offset:0 lds\n"
  "v_mul_f32 v116, v25, v116\n"
  "v_mul_f32 v117, v25, v117\n"
  "v_mfma_f32_16x16x32_bf16 v[72:75], a[52:55], a[116:119], v[72:75]\n"
  "ds_read_b64_tr_b16 a[6:7], v9 offset:6144\n"
  "v_mfma_f32_16x16x32_bf16 v[76:79], a[60:63], a[116:119], v[76:79]\n"
  "v_mul_f32 v118, v25, v118\n"
  "v_mul_f32 v119, v25, v119\n"
  "v_mul_f32 v120, v25, v120\n"
  "s_waitcnt lgkmcnt(9)\n"
  "v_mfma_f32_16x16x32_bf16 v[80:83], a[32:35], a[120:123], v[80:83]\n"
  "ds_read_b128 a[104:107], v8 offset:10240\n"
  "v_mfma_f32_16x16x32_bf16 v[84:87], a[40:43], a[120:123], v[84:87]\n"
  "ds_read_b128 a[108:111], v8 offset:11264\n"
  "v_mfma_f32_16x16x32_bf16 v[88:91], a[48:51], a[120:123], v[88:91]\n"
  "buffer_load_dwordx4 v5, s[44:47], s57 offen offset:1024 lds\n"
  "v_mul_f32 v121, v25, v121\n"
  "v_mul_f32 v122, v25, v122\n"
  "v_mfma_f32_16x16x32_bf16 v[92:95], a[56:59], a[120:123], v[92:95]\n"
  "ds_read_b64_tr_b16 a[12:13], v10 offset:4096\n"
  "v_mfma_f32_16x16x32_bf16 v[80:83], a[36:39], a[124:127], v[80:83]\n"
  "s_add_u32 m0, s48, 8192\n"
  "ds_read_b64_tr_b16 a[14:15], v10 offset:6144\n"
  "v_mfma_f32_16x16x32_bf16 v[84:87], a[44:47], a[124:127], v[84:87]\n"
  "buffer_load_dwordx4 v6, s[44:47], s57 offen offset:0 lds\n"
  "v_mul_f32 v123, v25, v123\n"
  "v_mfma_f32_16x16x32_bf16 v[88:91], a[52:55], a[124:127], v[88:91]\n"
  "ds_read_b64_tr_b16 a[20:21], v11 offset:4096\n"
  "s_cmp_eq_u32 s50, 0\n"
  "s_cselect_b32 s51, s34, 32768\n"
  "v_mul_f32 v124, v25, v124\n"
  "v_mul_f32 v125, v25, v125\n"
  "v_mfma_f32_16x16x32_bf16 v[92:95], a[60:63], a[124:127], v[92:95]\n"
  "s_cmp_eq_u32 s50, 1\n"
  "s_cselect_b32 s61, s35, 16384\n"
  "v_mul_f32 v126, v25, v126\n"
  "v_mul_f32 v127, v25, v127\n"
  "s_waitcnt lgkmcnt(8)\n"
  "v_mfma_f32_16x16x32_bf16 v[96:99], a[32:35], a[96:99], v[96:99]\n"
  "ds_read_b128 a[112:115], v8 offset:12288\n"
  "v_mfma_f32_16x16x32_bf16 v[100:103], a[40:43], a[96:99], v[100:103]\n"
  "ds_read_b128 a[116:119], v8 offset:13312\n"
  "v_mfma_f32_16x16x32_bf16 v[104:107], a[48:51], a[96:99], v[104:107]\n"
  "buffer_load_dwordx4 v7, s[44:47], s57 offen offset:1024 lds\n"
  "v_mul_f32 v128, v26, v128\n"
  "v_mfma_f32_16x16x32_bf16 v[108:111], a[56:59], a[96:99], v[108:111]\n"
  "s_add_u32 s57, s57, 131072\n"
  "ds_read_b64_tr_b16 a[22:23], v11 offset:6144\n"
  "v_mfma_f32_16x16x32_bf16 v[96:99], a[36:39], a[100:103], v[96:99]\n"
  "ds_read_b64_tr_b16 a[28:29], v12 offset:4096\n"
  "s_cmp_eq_u32 s50, 0\n"
  "s_cselect_b32 s50, 3, s50\n"
  "v_mul_f32 v129, v26, v129\n"
  "v_mul_f32 v130, v26, v130\n"
  "v_mfma_f32_16x16x32_bf16 v[100:103], a[44:47], a[100:103], v[100:103]\n"
  "s_cmp_eq_u32 s49, 1\n"
  "s_cselect_b32 s67, s35, 16384\n"
  "ds_read_b64_tr_b16 a[30:31], v12 offset:6144\n"
  "v_mul_f32 v131, v26, v131\n"
  "v_mul_f32 v132, v26, v132\n"
  "v_mfma_f32_16x16x32_bf16 v[104:107], a[52:55], a[100:103], v[104:107]\n"
  "s_sub_u32 s50, s50, 1\n"
  "v_mul_f32 v133, v26, v133\n"
  "v_mfma_f32_16x16x32_bf16 v[108:111], a[60:63], a[100:103], v[108:111]\n"
  "v_mul_f32 v134, v26, v134\n"
  "v_mul_f32 v135, v26, v135\n"
  "v_mul_f32 v136, v26, v136\n"
  "v_mul_f32 v137, v26, v137\n"
  "s_waitcnt lgkmcnt(8)\n"
  "v_mfma_f32_16x16x32_bf16 v[112:115], a[32:35], a[104:107], v[112:115]\n"
  "ds_read_b128 a[120:123], v8 offset:14336\n"
  "s_add_u32 s48, s48, s67\n"
  "v_mfma_f32_16x16x32_bf16 v[116:119], a[40:43], a[104:107], v[116:119]\n"
  "ds_read_b128 a[124:127], v8 offset:15360\n"
  "v_mfma_f32_16x16x32_bf16 v[120:123], a[48:51], a[104:107], v[120:123]\n"
  "s_cmp_eq_u32 s49, 0\n"
  "s_cselect_b32 s49, 3, s49\n"
  "v_mul_f32 v138, v26, v138\n"
  "v_mfma_f32_16x16x32_bf16 v[124:127], a[56:59], a[104:107], v[124:127]\n"
  "v_add_u32 v8, s51, v8\n"
  "v_mul_f32 v139, v26, v139\n"
  "v_mul_f32 v140, v26, v140\n"
  "v_mfma_f32_16x16x32_bf16 v[112:115], a[36:39], a[108:111], v[112:115]\n"
  "v_mul_f32 v141, v26, v141\n"
  "v_mul_f32 v142, v26, v142\n"
  "v_mul_f32 v143, v26, v143\n"
  "v_mfma_f32_16x16x32_bf16 v[116:119], a[44:47], a[108:111], v[116:119]\n"
  "s_sub_u32 s49, s49, 1\n"
  "v_mul_f32 v144, v27, v144\n"
  "v_mul_f32 v145, v27, v145\n"
  "v_mfma_f32_16x16x32_bf16 v[120:123], a[52:55], a[108:111], v[120:123]\n"
  "v_mul_f32 v146, v27, v146\n"
  "v_mul_f32 v147, v27, v147\n"
  "v_mul_f32 v148, v27, v148\n"
  "v_mfma_f32_16x16x32_bf16 v[124:127], a[60:63], a[108:111], v[124:127]\n"
  "v_mul_f32 v149, v27, v149\n"
  "v_mul_f32 v150, v27, v150\n"
  "v_mul_f32 v151, v27, v151\n"
  "s_waitcnt vmcnt(12) lgkmcnt(0)\n"
  "s_barrier\n"
  "v_mfma_f32_16x16x32_bf16 v[128:131], a[32:35], a[112:115], v[128:131]\n"
  "ds_read_b128 a[96:99], v8 offset:0\n"
  "v_mfma_f32_16x16x32_bf16 v[132:135], a[40:43], a[112:115], v[132:135]\n"
  "ds_read_b128 a[100:103], v8 offset:1024\n"
  "v_mfma_f32_16x16x32_bf16 v[136:139], a[48:51], a[112:115], v[136:139]\n"
  "s_mov_b32 m0, s58\n"
  "v_mul_f32 v152, v27, v152\n"
  "v_mul_f32 v153, v27, v153\n"
  "v_mfma_f32_16x16x32_bf16 v[140:143], a[56:59], a[112:115], v[140:143]\n"
  "buffer_load_dwordx4 v0, s[40:43], s56 offen offset:0 sc1 lds\n"
  "v_mul_f32 v154, v27, v154\n"
  "v_mul_f32 v155, v27, v155\n"
  "v_mfma_f32_16x16x32_bf16 v[128:131], a[36:39], a[116:119], v[128:131]\n"
  "v_add_u32 v9, s61, v9\n"
  "v_mul_f32 v156, v27, v156\n"
  "v_mfma_f32_16x16x32_bf16 v[132:135], a[44:47], a[116:119], v[132:135]\n"
  "v_mul_f32 v157, v27, v157\n"
  "v_mul_f32 v158, v27, v158\n"
  "v_mul_f32 v159, v27, v159\n"
  "v_mfma_f32_16x16x32_bf16 v[136:139], a[52:55], a[116:119], v[136:139]\n"
  "v_add_u32 v10, s61, v10\n"
  "v_mfma_f32_16x16x32_bf16 v[140:143], a[60:63], a[116:119], v[140:143]\n"
  "buffer_load_dwordx4 v0, s[40:43], s56 offen offset:1024 sc1 lds\n"
  "v_mfma_f32_16x16x32_bf16 v[144:147], a[32:35], a[120:123], v[144:147]\n"
  "ds_read_b128 a[104:107], v8 offset:2048\n"
  "v_mfma_f32_16x16x32_bf16 v[148:151], a[40:43], a[120:123], v[148:151]\n"
  "ds_read_b128 a[108:111], v8 offset:3072\n"
  "v_mfma_f32_16x16x32_bf16 v[152:155], a[48:51], a[120:123], v[152:155]\n"
  "v_add_u32 v11, s61, v11\n"
  "v_mfma_f32_16x16x32_bf16 v[156:159], a[56:59], a[120:123], v[156:159]\n"
  "buffer_load_dwordx4 v0, s[40:43], s56 offen offset:2048 sc1 lds\n"
  "v_mfma_f32_16x16x32_bf16 v[144:147], a[36:39], a[124:127], v[144:147]\n"
  "v_add_u32 v12, s61, v12\n"
  "v_mfma_f32_16x16x32_bf16 v[148:151], a[44:47], a[124:127], v[148:151]\n"
  "v_mfma_f32_16x16x32_bf16 v[152:155], a[52:55], a[124:127], v[152:155]\n"
  "v_mfma_f32_16x16x32_bf16 v[156:159], a[60:63], a[124:127], v[156:159]\n"
  "buffer_load_dwordx4 v0, s[40:43], s56 offen offset:3072 sc1 lds\n"
  "s_cmp_eq_u32 s99, 0\n"
  "s_cbranch_scc1 K2_BQ_%=\n"
  "K2_BQR_%=:\n"
  "s_mov_b32 s60, 5\n"
  ".p2align 6\n"
  "K2_LOOP_%=:\n"
  "s_waitcnt lgkmcnt(2)\n"
  "v_mfma_f32_16x16x32_bf16 v[32:35], a[0:3], a[96:99], v[32:35]\n"
  "ds_read_b128 a[112:115], v8 offset:4096\n"
  "v_mfma_f32_16x16x32_bf16 v[36:39], a[8:11], a[96:99], v[36:39]\n"
  "ds_read_b128 a[116:119], v8 offset:5120\n"
  "s_mov_b32 m0, s59\n"
  "v_mfma_f32_16x16x32_bf16 v[40:43], a[16:19], a[96:99], v[40:43]\n"
  "buffer_load_dwordx4 v1, s[40:43], s56 offen offset:0 sc1 lds\n"
  "v_mfma_f32_16x16x32_bf16 v[44:47], a[24:27], a[96:99], v[44:47]\n"
  "ds_read_b64_tr_b16 a[32:33], v9 offset:0\n"
  "v_mfma_f32_16x16x32_bf16 v[32:35], a[4:7], a[100:103], v[32:35]\n"
  "ds_read_b64_tr_b16 a[34:35], v9 offset:2048\n"
  "v_mfma_f32_16x16x32_bf16 v[36:39], a[12:15], a[100:103], v[36:39]\n"
  "v_mfma_f32_16x16x32_bf16 v[40:43], a[20:23], a[100:103], v[40:43]\n"
  "buffer_load_dwordx4 v1, s[40:43], s56 offen offset:1024 sc1 lds\n"
  "v_mfma_f32_16x16x32_bf16 v[44:47], a[28:31], a[100:103], v[44:47]\n"
  "ds_read_b64_tr_b16 a[40:41], v10 offset:0\n"
  "s_waitcnt lgkmcnt(5)\n"
  "v_mfma_f32_16x16x32_bf16 v[48:51], a[0:3], a[104:107], v[48:51]\n"
  "ds_read_b128 a[120:123], v8 offset:6144\n"
  "v_mfma_f32_16x16x32_bf16 v[52:55], a[8:11], a[104:107], v[52:55]\n"
  "ds_read_b128 a[124:127], v8 offset:7168\n"
  "v_mfma_f32_16x16x32_bf16 v[56:59], a[16:19], a[104:107], v[56:59]\n"
  "buffer_load_dwordx4 v1, s[40:43], s56 offen offset:2048 sc1 lds\n"
  "v_mfma_f32_16x16x32_bf16 v[60:63], a[24:27], a[104:107], v[60:63]\n"
  "ds_read_b64_tr_b16 a[42:43], v10 offset:2048\n"
  "v_mfma_f32_16x16x32_bf16 v[48:51], a[4:7], a[108:111], v[48:51]\n"
  "ds_read_b64_tr_b16 a[48:49], v11 offset:0\n"
  "v_mfma_f32_16x16x32_bf16 v[52:55], a[12:15], a[108:111], v[52:55]\n"
  "ds_read_b64_tr_b16 a[50:51], v11 offset:2048\n"
  "v_mfma_f32_16x16x32_bf16 v[56:59], a[20:23], a[108:111], v[56:59]\n"
  "buffer_load_dwordx4 v1, s[40:43], s56 offen offset:3072 sc1 lds\n"
  "v_mfma_f32_16x16x32_bf16 v[60:63], a[28:31], a[108:111], v[60:63]\n"
  "s_add_u32 s56, s56, 4096\n"
  "ds_read_b64_tr_b16 a[56:57], v12 offset:0\n"
  "s_waitcnt lgkmcnt(9)\n"
  "v_mfma_f32_16x16x32_bf16 v[64:67], a[0:3], a[112:115], v[64:67]\n"
  "ds_read_b128 a[96:99], v8 offset:8192\n"
  "s_cmp_eq_u32 s49, 0\n"
  "s_cselect_b32 s66, s34, 32768\n"
  "v_mfma_f32_16x16x32_bf16 v[68:71], a[8:11], a[112:115], v[68:71]\n"
  "ds_read_b128 a[100:103], v8 offset:9216\n"
  "s_add_u32 s58, s58, s66\n"
  "v_mfma_f32_16x16x32_bf16 v[72:75], a[16:19], a[112:115], v[72:75]\n"
  "s_add_u32 s59, s59, s66\n"
  "ds_read_b64_tr_b16 a[58:59], v12 offset:2048\n"
  "v_mfma_f32_16x16x32_bf16 v[76:79], a[24:27], a[112:115], v[76:79]\n"
  "v_mfma_f32_16x16x32_bf16 v[64:67], a[4:7], a[116:119], v[64:67]\n"
  "s_add_u32 m0, s48, 0\n"
  "ds_read_b64_tr_b16 a[36:37], v9 offset:4096\n"
  "v_mfma_f32_16x16x32_bf16 v[68:71], a[12:15], a[116:119], v[68:71]\n"
  "buffer_load_dwordx4 v4, s[44:47], s57 offen offset:0 lds\n"
  "v_mfma_f32_16x16x32_bf16 v[72:75], a[20:23], a[116:119], v[72:75]\n"
  "ds_read_b64_tr_b16 a[38:39], v9 offset:6144\n"
  "v_mfma_f32_16x16x32_bf16 v[76:79], a[28:31], a[116:119], v[76:79]\n"
  "s_waitcnt lgkmcnt(9)\n"
  "v_mfma_f32_16x16x32_bf16 v[80:83], a[0:3], a[120:123], v[80:83]\n"
  "ds_read_b128 a[104:107], v8 offset:10240\n"
  "v_mfma_f32_16x16x32_bf16 v[84:87], a[8:11], a[120:123], v[84:87]\n"
  "ds_read_b128 a[108:111], v8 offset:11264\n"
  "v_mfma_f32_16x16x32_bf16 v[88:91], a[16:19], a[120:123], v[88:91]\n"
  "buffer_load_dwordx4 v5, s[44:47], s57 offen offset:1024 lds\n"
  "v_mfma_f32_16x16x32_bf16 v[92:95], a[24:27], a[120:123], v[92:95]\n"
  "ds_read_b64_tr_b16 a[44:45], v10 offset:4096\n"
  "v_mfma_f32_16x16x32_bf16 v[80:83], a[4:7], a[124:127], v[80:83]\n"
  "s_add_u32 m0, s48, 8192\n"
  "ds_read_b64_tr_b16 a[46:47], v10 offset:6144\n"
  "v_mfma_f32_16x16x32_bf16 v[84:87], a[12:15], a[124:127], v[84:87]\n"
  "buffer_load_dwordx4 v6, s[44:47], s57 offen offset:0 lds\n"
  "v_mfma_f32_16x16x32_bf16 v[88:91], a[20:23], a[124:127], v[88:91]\n"
  "ds_read_b64_tr_b16 a[52:53], v11 offset:4096\n"
  "s_cmp_eq_u32 s50, 0\n"
  "s_cselect_b32 s51, s34, 32768\n"
  "v_mfma_f32_16x16x32_bf16 v[92:95], a[28:31], a[124:127], v[92:95]\n"
  "s_cmp_eq_u32 s50, 1\n"
  "s_cselect_b32 s61, s35, 16384\n"
  "s_waitcnt lgkmcnt(8)\n"
  "v_mfma_f32_16x16x32_bf16 v[96:99], a[0:3], a[96:99], v[96:99]\n"
  "ds_read_b128 a[112:115], v8 offset:12288\n"
  "v_mfma_f32_16x16x32_bf16 v[100:103], a[8:11], a[96:99], v[100:103]\n"
  "ds_read_b128 a[116:119], v8 offset:13312\n"
  "v_mfma_f32_16x16x32_bf16 v[104:107], a[16:19], a[96:99], v[104:107]\n"
  "buffer_load_dwordx4 v7, s[44:47], s57 offen offset:1024 lds\n"
  "v_mfma_f32_16x16x32_bf16 v[108:111], a[24:27], a[96:99], v[108:111]\n"
  "s_add_u32 s57, s57, 131072\n"
  "ds_read_b64_tr_b16 a[54:55], v11 offset:6144\n"
  "v_mfma_f32_16x16x32_bf16 v[96:99], a[4:7], a[100:103], v[96:99]\n"
  "ds_read_b64_tr_b16 a[60:61], v12 offset:4096\n"
  "s_cmp_eq_u32 s50, 0\n"
  "s_cselect_b32 s50, 3, s50\n"
  "v_mfma_f32_16x16x32_bf16 v[100:103], a[12:15], a[100:103], v[100:103]\n"
  "s_cmp_eq_u32 s49, 1\n"
  "s_cselect_b32 s67, s35, 16384\n"
  "ds_read_b64_tr_b16 a[62:63], v12 offset:6144\n"
  "v_mfma_f32_16x16x32_bf16 v[104:107], a[20:23], a[100:103], v[104:107]\n"
  "s_sub_u32 s50, s50, 1\n"
  "v_mfma_f32_16x16x32_bf16 v[108:111], a[28:31], a[100:103], v[108:111]\n"
  "s_waitcnt lgkmcnt(8)\n"
  "v_mfma_f32_16x16x32_bf16 v[112:115], a[0:3], a[104:107], v[112:115]\n"
  "ds_read_b128 a[120:123], v8 offset:14336\n"
  "s_add_u32 s48, s48, s67\n"
  "v_mfma_f32_16x16x32_bf16 v[116:119], a[8:11], a[104:107], v[116:119]\n"
  "ds_read_b128 a[124:127], v8 offset:15360\n"
  "v_mfma_f32_16x16x32_bf16 v[120:123], a[16:19], a[104:107], v[120:123]\n"
  "s_cmp_eq_u32 s49, 0\n"
  "s_cselect_b32 s49, 3, s49\n"
  "v_mfma_f32_16x16x32_bf16 v[124:127], a[24:27], a[104:107], v[124:127]\n"
  "v_add_u32 v8, s51, v8\n"
  "v_mfma_f32_16x16x32_bf16 v[112:115], a[4:7], a[108:111], v[112:115]\n"
  "v_mfma_f32_16x16x32_bf16 v[116:119], a[12:15], a[108:111], v[116:119]\n"
  "s_sub_u32 s49, s49, 1\n"
  "v_mfma_f32_16x16x32_bf16 v[120:123], a[20:23], a[108:111], v[120:123]\n"
  "v_mfma_f32_16x16x32_bf16 v[124:127], a[28:31], a[108:111], v[124:127]\n"
  "s_waitcnt vmcnt(12) lgkmcnt(0)\n"
  "s_barrier\n"
  "v_mfma_f32_16x16x32_bf16 v[128:131], a[0:3], a[112:115], v[128:131]\n"
  "ds_read_b128 a[96:99], v8 offset:0\n"
  "v_mfma_f32_16x16x32_bf16 v[132:135], a[8:11], a[112:115], v[132:135]\n"
  "ds_read_b128 a[100:103], v8 offset:1024\n"
  "v_mfma_f32_16x16x32_bf16 v[136:139], a[16:19], a[112:115], v[136:139]\n"
  "s_mov_b32 m0, s58\n"
  "v_mfma_f32_16x16x32_bf16 v[140:143], a[24:27], a[112:115], v[140:143]\n"
  "buffer_load_dwordx4 v0, s[40:43], s56 offen offset:0 sc1 lds\n"
  "v_mfma_f32_16x16x32_bf16 v[128:131], a[4:7], a[116:119], v[128:131]\n"
  "v_add_u32 v9, s61, v9\n"
  "v_mfma_f32_16x16x32_bf16 v[132:135], a[12:15], a[116:119], v[132:135]\n"
  "v_mfma_f32_16x16x32_bf16 v[136:139], a[20:23], a[116:119], v[136:139]\n"
  "v_add_u32 v10, s61, v10\n"
  "v_mfma_f32_16x16x32_bf16 v[140:143], a[28:31], a[116:119], v[140:143]\n"
  "buffer_load_dwordx4 v0, s[40:43], s56 offen offset:1024 sc1 lds\n"
  "v_mfma_f32_16x16x32_bf16 v[144:147], a[0:3], a[120:123], v[144:147]\n"
  "ds_read_b128 a[104:107], v8 offset:2048\n"
  "v_mfma_f32_16x16x32_bf16 v[148:151], a[8:11], a[120:123], v[148:151]\n"
  "ds_read_b128 a[108:111], v8 offset:3072\n"
  "v_mfma_f32_16x16x32_bf16 v[152:155], a[16:19], a[120:123], v[152:155]\n"
  "v_add_u32 v11, s61, v11\n"
  "v_mfma_f32_16x16x32_bf16 v[156:159], a[24:27], a[120:123], v[156:159]\n"
  "buffer_load_dwordx4 v0, s[40:43], s56 offen offset:2048 sc1 lds\n"
  "v_mfma_f32_16x16x32_bf16 v[144:147], a[4:7], a[124:127], v[144:147]\n"
  "v_add_u32 v12, s61, v12\n"
  "v_mfma_f32_16x16x32_bf16 v[148:151], a[12:15], a[124:127], v[148:151]\n"
  "v_mfma_f32_16x16x32_bf16 v[152:155], a[20:23], a[124:127], v[152:155]\n"
  "v_mfma_f32_16x16x32_bf16 v[156:159], a[28:31], a[124:127], v[156:159]\n"
  "buffer_load_dwordx4 v0, s[40:43], s56 offen offset:3072 sc1 lds\n"
  "s_waitcnt lgkmcnt(2)\n"
  "v_mfma_f32_16x16x32_bf16 v[32:35], a[32:35], a[96:99], v[32:35]\n"
  "ds_read_b128 a[112:115], v8 offset:4096\n"
  "v_mfma_f32_16x16x32_bf16 v[36:39], a[40:43], a[96:99], v[36:39]\n"
  "ds_read_b128 a[116:119], v8 offset:5120\n"
  "s_mov_b32 m0, s59\n"
  "v_mfma_f32_16x16x32_bf16 v[40:43], a[48:51], a[96:99], v[40:43]\n"
  "buffer_load_dwordx4 v1, s[40:43], s56 offen offset:0 sc1 lds\n"
  "v_mfma_f32_16x16x32_bf16 v[44:47], a[56:59], a[96:99], v[44:47]\n"
  "ds_read_b64_tr_b16 a[0:1], v9 offset:0\n"
  "v_mfma_f32_16x16x32_bf16 v[32:35], a[36:39], a[100:103], v[32:35]\n"
  "ds_read_b64_tr_b16 a[2:3], v9 offset:2048\n"
  "v_mfma_f32_16x16x32_bf16 v[36:39], a[44:47], a[100:103], v[36:39]\n"
  "v_mfma_f32_16x16x32_bf16 v[40:43], a[52:55], a[100:103], v[40:43]\n"
  "buffer_load_dwordx4 v1, s[40:43], s56 offen offset:1024 sc1 lds\n"
  "v_mfma_f32_16x16x32_bf16 v[44:47], a[60:63], a[100:103], v[44:47]\n"
  "ds_read_b64_tr_b16 a[8:9], v10 offset:0\n"
  "s_waitcnt lgkmcnt(5)\n"
  "v_mfma_f32_16x16x32_bf16 v[48:51], a[32:35], a[104:107], v[48:51]\n"
  "ds_read_b128 a[120:123], v8 offset:6144\n"
  "v_mfma_f32_16x16x32_bf16 v[52:55], a[40:43], a[104:107], v[52:55]\n"
  "ds_read_b128 a[124:127], v8 offset:7168\n"
  "v_mfma_f32_16x16x32_bf16 v[56:59], a[48:51], a[104:107], v[56:59]\n"
  "buffer_load_dwordx4 v1, s[40:43], s56 offen offset:2048 sc1 lds\n"
  "v_mfma_f32_16x16x32_bf16 v[60:63], a[56:59], a[104:107], v[60:63]\n"
  "ds_read_b64_tr_b16 a[10:11], v10 offset:2048\n"
  "v_mfma_f32_16x16x32_bf16 v[48:51], a[36:39], a[108:111], v[48:51]\n"
  "ds_read_b64_tr_b16 a[16:17], v11 offset:0\n"
  "v_mfma_f32_16x16x32_bf16 v[52:55], a[44:47], a[108:111], v[52:55]\n"
  "ds_read_b64_tr_b16 a[18:19], v11 offset:2048\n"
  "v_mfma_f32_16x16x32_bf16 v[56:59], a[52:55], a[108:111], v[56:59]\n"
  "buffer_load_dwordx4 v1, s[40:43], s56 offen offset:3072 sc1 lds\n"
  "v_mfma_f32_16x16x32_bf16 v[60:63], a[60:63], a[108:111], v[60:63]\n"
  "s_add_u32 s56, s56, 4096\n"
  "ds_read_b64_tr_b16 a[24:25], v12 offset:0\n"
  "s_waitcnt lgkmcnt(9)\n"
  "v_mfma_f32_16x16x32_bf16 v[64:67], a[32:35], a[112:115], v[64:67]\n"
  "ds_read_b128 a[96:99], v8 offset:8192\n"
  "s_cmp_eq_u32 s49, 0\n"
  "s_cselect_b32 s66, s34, 32768\n"
  "v_mfma_f32_16x16x32_bf16 v[68:71], a[40:43], a[112:115], v[68:71]\n"
  "ds_read_b128 a[100:103], v8 offset:9216\n"
  "s_add_u32 s58, s58, s66\n"
  "v_mfma_f32_16x16x32_bf16 v[72:75], a[48:51], a[112:115], v[72:75]\n"
  "s_add_u32 s59, s59, s66\n"
  "ds_read_b64_tr_b16 a[26:27], v12 offset:2048\n"
  "v_mfma_f32_16x16x32_bf16 v[76:79], a[56:59], a[112:115], v[76:79]\n"
  "v_mfma_f32_16x16x32_bf16 v[64:67], a[36:39], a[116:119], v[64:67]\n"
  "s_add_u32 m0, s48, 0\n"
  "ds_read_b64_tr_b16 a[4:5], v9 offset:4096\n"
  "v_mfma_f32_16x16x32_bf16 v[68:71], a[44:47], a[116:119], v[68:71]\n"
  "buffer_load_dwordx4 v4, s[44:47], s57 offen offset:0 lds\n"
  "v_mfma_f32_16x16x32_bf16 v[72:75], a[52:55], a[116:119], v[72:75]\n"
  "ds_read_b64_tr_b16 a[6:7], v9 offset:6144\n"
  "v_mfma_f32_16x16x32_bf16 v[76:79], a[60:63], a[116:119], v[76:79]\n"
  "s_waitcnt lgkmcnt(9)\n"
  "v_mfma_f32_16x16x32_bf16 v[80:83], a[32:35], a[120:123], v[80:83]\n"
  "ds_read_b128 a[104:107], v8 offset:10240\n"
  "v_mfma_f32_16x16x32_bf16 v[84:87], a[40:43], a[120:123], v[84:87]\n"
  "ds_read_b128 a[108:111], v8 offset:11264\n"
  "v_mfma_f32_16x16x32_bf16 v[88:91], a[48:51], a[120:123], v[88:91]\n"
  "buffer_load_dwordx4 v5, s[44:47], s57 offen offset:1024 lds\n"
  "v_mfma_f32_16x16x32_bf16 v[92:95], a[56:59], a[120:123], v[92:95]\n"
  "ds_read_b64_tr_b16 a[12:13], v10 offset:4096\n"
  "v_mfma_f32_16x16x32_bf16 v[80:83], a[36:39], a[124:127], v[80:83]\n"
  "s_add_u32 m0, s48, 8192\n"
  "ds_read_b64_tr_b16 a[14:15], v10 offset:6144\n"
  "v_mfma_f32_16x16x32_bf16 v[84:87], a[44:47], a[124:127], v[84:87]\n"
  "buffer_load_dwordx4 v6, s[44:47], s57 offen offset:0 lds\n"
  "v_mfma_f32_16x16x32_bf16 v[88:91], a[52:55], a[124:127], v[88:91]\n"
  "ds_read_b64_tr_b16 a[20:21], v11 offset:4096\n"
  "s_cmp_eq_u32 s50, 0\n"
  "s_cselect_b32 s51, s34, 32768\n"
  "v_mfma_f32_16x16x32_bf16 v[92:95], a[60:63], a[124:127], v[92:95]\n"
  "s_cmp_eq_u32 s50, 1\n"
  "s_cselect_b32 s61, s35, 16384\n"
  "s_waitcnt lgkmcnt(8)\n"
  "v_mfma_f32_16x16x32_bf16 v[96:99], a[32:35], a[96:99], v[96:99]\n"
  "ds_read_b128 a[112:115], v8 offset:12288\n"
  "v_mfma_f32_16x16x32_bf16 v[100:103], a[40:43], a[96:99], v[100:103]\n"
  "ds_read_b128 a[116:119], v8 offset:13312\n"
  "v_mfma_f32_16x16x32_bf16 v[104:107], a[48:51], a[96:99], v[104:107]\n"
  "buffer_load_dwordx4 v7, s[44:47], s57 offen offset:1024 lds\n"
  "v_mfma_f32_16x16x32_bf16 v[108:111], a[56:59], a[96:99], v[108:111]\n"
  "s_add_u32 s57, s57, 131072\n"
  "ds_read_b64_tr_b16 a[22:23], v11 offset:6144\n"
  "v_mfma_f32_16x16x32_bf16 v[96:99], a[36:39], a[100:103], v[96:99]\n"
  "ds_read_b64_tr_b16 a[28:29], v12 offset:4096\n"
  "s_cmp_eq_u32 s50, 0\n"
  "s_cselect_b32 s50, 3, s50\n"
  "v_mfma_f32_16x16x32_bf16 v[100:103], a[44:47], a[100:103], v[100:103]\n"
  "s_cmp_eq_u32 s49, 1\n"
  "s_cselect_b32 s67, s35, 16384\n"
  "ds_read_b64_tr_b16 a[30:31], v12 offset:6144\n"
  "v_mfma_f32_16x16x32_bf16 v[104:107], a[52:55], a[100:103], v[104:107]\n"
  "s_sub_u32 s50, s50, 1\n"
  "v_mfma_f32_16x16x32_bf16 v[108:111], a[60:63], a[100:103], v[108:111]\n"
  "s_waitcnt lgkmcnt(8)\n"
  "v_mfma_f32_16x16x32_bf16 v[112:115], a[32:35], a[104:107], v[112:115]\n"
  "ds_read_b128 a[120:123], v8 offset:14336\n"
  "s_add_u32 s48, s48, s67\n"
  "v_mfma_f32_16x16x32_bf16 v[116:119], a[40:43], a[104:107], v[116:119]\n"
  "ds_read_b128 a[124:127], v8 offset:15360\n"
  "v_mfma_f32_16x16x32_bf16 v[120:123], a[48:51], a[104:107], v[120:123]\n"
  "s_cmp_eq_u32 s49, 0\n"
  "s_cselect_b32 s49, 3, s49\n"
  "v_mfma_f32_16x16x32_bf16 v[124:127], a[56:59], a[104:107], v[124:127]\n"
  "v_add_u32 v8, s51, v8\n"
  "v_mfma_f32_16x16x32_bf16 v[112:115], a[36:39], a[108:111], v[112:115]\n"
  "v_mfma_f32_16x16x32_bf16 v[116:119], a[44:47], a[108:111], v[116:119]\n"
  "s_sub_u32 s49, s49, 1\n"
  "v_mfma_f32_16x16x32_bf16 v[120:123], a[52:55], a[108:111], v[120:123]\n"
  "v_mfma_f32_16x16x32_bf16 v[124:127], a[60:63], a[108:111], v[124:127]\n"
  "s_waitcnt vmcnt(12) lgkmcnt(0)\n"
  "s_barrier\n"
  "v_mfma_f32_16x16x32_bf16 v[128:131], a[32:35], a[112:115], v[128:131]\n"
  "ds_read_b128 a[96:99], v8 offset:0\n"
  "v_mfma_f32_16x16x32_bf16 v[132:135], a[40:43], a[112:115], v[132:135]\n"
  "ds_read_b128 a[100:103], v8 offset:1024\n"
  "v_mfma_f32_16x16x32_bf16 v[136:139], a[48:51], a[112:115], v[136:139]\n"
  "s_mov_b32 m0, s58\n"
  "v_mfma_f32_16x16x32_bf16 v[140:143], a[56:59], a[112:115], v[140:143]\n"
  "buffer_load_dwordx4 v0, s[40:43], s56 offen offset:0 sc1 lds\n"
  "v_mfma_f32_16x16x32_bf16 v[128:131], a[36:39], a[116:119], v[128:131]\n"
  "v_add_u32 v9, s61, v9\n"
  "v_mfma_f32_16x16x32_bf16 v[132:135], a[44:47], a[116:119], v[132:135]\n"
  "v_mfma_f32_16x16x32_bf16 v[136:139], a[52:55], a[116:119], v[136:139]\n"
  "v_add_u32 v10, s61, v10\n"
  "v_mfma_f32_16x16x32_bf16 v[140:143], a[60:63], a[116:119], v[140:143]\n"
  "buffer_load_dwordx4 v0, s[40:43], s56 offen offset:1024 sc1 lds\n"
  "v_mfma_f32_16x16x32_bf16 v[144:147], a[32:35], a[120:123], v[144:147]\n"
  "ds_read_b128 a[104:107], v8 offset:2048\n"
  "v_mfma_f32_16x16x32_bf16 v[148:151], a[40:43], a[120:123], v[148:151]\n"
  "ds_read_b128 a[108:111], v8 offset:3072\n"
  "v_mfma_f32_16x16x32_bf16 v[152:155], a[48:51], a[120:123], v[152:155]\n"
  "v_add_u32 v11, s61, v11\n"
  "v_mfma_f32_16x16x32_bf16 v[156:159], a[56:59], a[120:123], v[156:159]\n"
  "buffer_load_dwordx4 v0, s[40:43], s56 offen offset:2048 sc1 lds\n"
  "v_mfma_f32_16x16x32_bf16 v[144:147], a[36:39], a[124:127], v[144:147]\n"
  "v_add_u32 v12, s61, v12\n"
  "v_mfma_f32_16x16x32_bf16 v[148:151], a[44:47], a[124:127], v[148:151]\n"
  "v_mfma_f32_16x16x32_bf16 v[152:155], a[52:55], a[124:127], v[152:155]\n"
  "v_mfma_f32_16x16x32_bf16 v[156:159], a[60:63], a[124:127], v[156:159]\n"
  "buffer_load_dwordx4 v0, s[40:43], s56 offen offset:3072 sc1 lds\n"
  "s_sub_u32 s60, s60, 1\n"
  "s_cmp_lg_u32 s60, 0\n"
  "s_cbranch_scc1 K2_LOOP_%=\n"
  "s_cmp_eq_u32 s99, 0\n"
  "s_cbranch_scc1 K2_BT_%=\n"
  "K2_BTR_%=:\n"
  "s_waitcnt lgkmcnt(2)\n"
  "v_mfma_f32_16x16x32_bf16 v[32:35], a[0:3], a[96:99], v[32:35]\n"
  "ds_read_b128 a[112:115], v8 offset:4096\n"
  "v_mfma_f32_16x16x32_bf16 v[36:39], a[8:11], a[96:99], v[36:39]\n"
  "ds_read_b128 a[116:119], v8 offset:5120\n"
  "s_mov_b32 m0, s59\n"
  "v_mfma_f32_16x16x32_bf16 v[40:43], a[16:19], a[96:99], v[40:43]\n"
  "buffer_load_dwordx4 v1, s[40:43], s56 offen offset:0 sc1 lds\n"
  "v_mfma_f32_16x16x32_bf16 v[44:47], a[24:27], a[96:99], v[44:47]\n"
  "s_cmp_eq_u32 s50, 0\n"
  "s_cselect_b32 s51, s34, 32768\n"
  "ds_read_b64_tr_b16 a[32:33], v9 offset:0\n"
  "v_mfma_f32_16x16x32_bf16 v[32:35], a[4:7], a[100:103], v[32:35]\n"
  "s_cmp_eq_u32 s50, 1\n"
  "s_cselect_b32 s61, s35, 16384\n"
  "ds_read_b64_tr_b16 a[34:35], v9 offset:2048\n"
  "v_mfma_f32_16x16x32_bf16 v[36:39], a[12:15], a[100:103], v[36:39]\n"
  "buffer_load_dwordx4 v1, s[40:43], s56 offen offset:1024 sc1 lds\n"
  "v_mfma_f32_16x16x32_bf16 v[40:43], a[20:23], a[100:103], v[40:43]\n"
  "v_add_u32 v13, s51, v8\n"
  "v_mfma_f32_16x16x32_bf16 v[44:47], a[28:31], a[100:103], v[44:47]\n"
  "ds_read_b64_tr_b16 a[40:41], v10 offset:0\n"
  "s_waitcnt lgkmcnt(5)\n"
  "v_mfma_f32_16x16x32_bf16 v[48:51], a[0:3], a[104:107], v[48:51]\n"
  "ds_read_b128 a[120:123], v8 offset:6144\n"
  "v_mfma_f32_16x16x32_bf16 v[52:55], a[8:11], a[104:107], v[52:55]\n"
  "ds_read_b128 a[124:127], v8 offset:7168\n"
  "v_mfma_f32_16x16x32_bf16 v[56:59], a[16:19], a[104:107], v[56:59]\n"
  "buffer_load_dwordx4 v1, s[40:43], s56 offen offset:2048 sc1 lds\n"
  "v_mfma_f32_16x16x32_bf16 v[60:63], a[24:27], a[104:107], v[60:63]\n"
  "buffer_load_dwordx4 v1, s[40:43], s56 offen offset:3072 sc1 lds\n"
  "v_mfma_f32_16x16x32_bf16 v[48:51], a[4:7], a[108:111], v[48:51]\n"
  "v_add_u32 v4, s61, v9\n"
  "v_add_u32 v5, s61, v10\n"
  "v_mfma_f32_16x16x32_bf16 v[52:55], a[12:15], a[108:111], v[52:55]\n"
  "v_add_u32 v6, s61, v11\n"
  "v_mfma_f32_16x16x32_bf16 v[56:59], a[20:23], a[108:111], v[56:59]\n"
  "v_add_u32 v7, s61, v12\n"
  "v_mfma_f32_16x16x32_bf16 v[60:63], a[28:31], a[108:111], v[60:63]\n"
  "ds_read_b64_tr_b16 a[42:43], v10 offset:2048\n"
  "s_waitcnt lgkmcnt(6)\n"
  "v_mfma_f32_16x16x32_bf16 v[64:67], a[0:3], a[112:115], v[64:67]\n"
  "s_cmp_eq_u32 s50, 0\n"
  "s_cselect_b32 s50, 3, s50\n"
  "ds_read_b64_tr_b16 a[48:49], v11 offset:0\n"
  "v_mfma_f32_16x16x32_bf16 v[68:71], a[8:11], a[112:115], v[68:71]\n"
  "ds_read_b64_tr_b16 a[50:51], v11 offset:2048\n"
  "v_mfma_f32_16x16x32_bf16 v[72:75], a[16:19], a[112:115], v[72:75]\n"
  "s_sub_u32 s50, s50, 1\n"
  "ds_read_b64_tr_b16 a[56:57], v12 offset:0\n"
  "v_mfma_f32_16x16x32_bf16 v[76:79], a[24:27], a[112:115], v[76:79]\n"
  "ds_read_b64_tr_b16 a[58:59], v12 offset:2048\n"
  "v_mfma_f32_16x16x32_bf16 v[64:67], a[4:7], a[116:119], v[64:67]\n"
  "s_cmp_eq_u32 s50, 0\n"
  "s_cselect_b32 s51, s34, 32768\n"
  "ds_read_b64_tr_b16 a[36:37], v9 offset:4096\n"
  "v_mfma_f32_16x16x32_bf16 v[68:71], a[12:15], a[116:119], v[68:71]\n"
  "ds_read_b64_tr_b16 a[38:39], v9 offset:6144\n"
  "v_mfma_f32_16x16x32_bf16 v[72:75], a[20:23], a[116:119], v[72:75]\n"
  "v_add_u32 v14, s51, v13\n"
  "v_mfma_f32_16x16x32_bf16 v[76:79], a[28:31], a[116:119], v[76:79]\n"
  "ds_read_b64_tr_b16 a[44:45], v10 offset:4096\n"
  "s_waitcnt vmcnt(8)\n"
  "s_barrier\n"
  "s_waitcnt lgkmcnt(8)\n"
  "v_mfma_f32_16x16x32_bf16 v[80:83], a[0:3], a[120:123], v[80:83]\n"
  "ds_read_b128 a[96:99], v13 offset:0\n"
  "ds_read_b128 a[104:107], v13 offset:2048\n"
  "v_mfma_f32_16x16x32_bf16 v[84:87], a[8:11], a[120:123], v[84:87]\n"
  "ds_read_b128 a[100:103], v13 offset:1024\n"
  "ds_read_b128 a[108:111], v13 offset:3072\n"
  "v_mfma_f32_16x16x32_bf16 v[88:91], a[16:19], a[120:123], v[88:91]\n"
  "ds_read_b64_tr_b16 a[46:47], v10 offset:6144\n"
  "v_mfma_f32_16x16x32_bf16 v[92:95], a[24:27], a[120:123], v[92:95]\n"
  "ds_read_b64_tr_b16 a[52:53], v11 offset:4096\n"
  "v_mfma_f32_16x16x32_bf16 v[80:83], a[4:7], a[124:127], v[80:83]\n"
  "ds_read_b64_tr_b16 a[54:55], v11 offset:6144\n"
  "v_mfma_f32_16x16x32_bf16 v[84:87], a[12:15], a[124:127], v[84:87]\n"
  "ds_read_b64_tr_b16 a[60:61], v12 offset:4096\n"
  "v_mfma_f32_16x16x32_bf16 v[88:91], a[20:23], a[124:127], v[88:91]\n"
  "ds_read_b64_tr_b16 a[62:63], v12 offset:6144\n"
  "v_mfma_f32_16x16x32_bf16 v[92:95], a[28:31], a[124:127], v[92:95]\n"
  "ds_read_b64_tr_b16 a[64:65], v4 offset:0\n"
  "s_waitcnt lgkmcnt(7)\n"
  "v_mfma_f32_16x16x32_bf16 v[32:35], a[32:35], a[96:99], v[32:35]\n"
  "ds_read_b128 a[112:115], v13 offset:4096\n"
  "v_mfma_f32_16x16x32_bf16 v[36:39], a[40:43], a[96:99], v[36:39]\n"
  "ds_read_b128 a[116:119], v13 offset:5120\n"
  "v_mfma_f32_16x16x32_bf16 v[40:43], a[48:51], a[96:99], v[40:43]\n"
  "ds_read_b64_tr_b16 a[66:67], v4 offset:2048\n"
  "v_mfma_f32_16x16x32_bf16 v[44:47], a[56:59], a[96:99], v[44:47]\n"
  "ds_read_b64_tr_b16 a[72:73], v5 offset:0\n"
  "v_mfma_f32_16x16x32_bf16 v[32:35], a[36:39], a[100:103], v[32:35]\n"
  "ds_read_b64_tr_b16 a[74:75], v5 offset:2048\n"
  "s_waitcnt lgkmcnt(10)\n"
  "v_mfma_f32_16x16x32_bf16 v[36:39], a[44:47], a[100:103], v[36:39]\n"
  "ds_read_b64_tr_b16 a[80:81], v6 offset:0\n"
  "s_waitcnt lgkmcnt(9)\n"
  "v_mfma_f32_16x16x32_bf16 v[40:43], a[52:55], a[100:103], v[40:43]\n"
  "ds_read_b64_tr_b16 a[82:83], v6 offset:2048\n"
  "s_waitcnt lgkmcnt(8)\n"
  "v_mfma_f32_16x16x32_bf16 v[44:47], a[60:63], a[100:103], v[44:47]\n"
  "ds_read_b64_tr_b16 a[88:89], v7 offset:0\n"
  "v_mfma_f32_16x16x32_bf16 v[48:51], a[32:35], a[104:107], v[48:51]\n"
  "ds_read_b128 a[120:123], v13 offset:6144\n"
  "v_mfma_f32_16x16x32_bf16 v[52:55], a[40:43], a[104:107], v[52:55]\n"
  "ds_read_b128 a[124:127], v13 offset:7168\n"
  "v_mfma_f32_16x16x32_bf16 v[56:59], a[48:51], a[104:107], v[56:59]\n"
  "ds_read_b64_tr_b16 a[90:91], v7 offset:2048\n"
  "v_mfma_f32_16x16x32_bf16 v[60:63], a[56:59], a[104:107], v[60:63]\n"
  "ds_read_b64_tr_b16 a[68:69], v4 offset:4096\n"
  "v_mfma_f32_16x16x32_bf16 v[48:51], a[36:39], a[108:111], v[48:51]\n"
  "v_mfma_f32_16x16x32_bf16 v[52:55], a[44:47], a[108:111], v[52:55]\n"
  "ds_read_b64_tr_b16 a[70:71], v4 offset:6144\n"
  "v_mfma_f32_16x16x32_bf16 v[56:59], a[52:55], a[108:111], v[56:59]\n"
  "v_mfma_f32_16x16x32_bf16 v[60:63], a[60:63], a[108:111], v[60:63]\n"
  "ds_read_b64_tr_b16 a[76:77], v5 offset:4096\n"
  "s_waitcnt lgkmcnt(12)\n"
  "v_mfma_f32_16x16x32_bf16 v[64:67], a[32:35], a[112:115], v[64:67]\n"
  "v_mfma_f32_16x16x32_bf16 v[68:71], a[40:43], a[112:115], v[68:71]\n"
  "ds_read_b64_tr_b16 a[78:79], v5 offset:6144\n"
  "v_mfma_f32_16x16x32_bf16 v[72:75], a[48:51], a[112:115], v[72:75]\n"
  "v_mfma_f32_16x16x32_bf16 v[76:79], a[56:59], a[112:115], v[76:79]\n"
  "ds_read_b64_tr_b16 a[84:85], v6 offset:4096\n"
  "v_mfma_f32_16x16x32_bf16 v[64:67], a[36:39], a[116:119], v[64:67]\n"
  "v_mfma_f32_16x16x32_bf16 v[68:71], a[44:47], a[116:119], v[68:71]\n"
  "ds_read_b64_tr_b16 a[86:87], v6 offset:6144\n"
  "v_mfma_f32_16x16x32_bf16 v[72:75], a[52:55], a[116:119], v[72:75]\n"
  "v_mfma_f32_16x16x32_bf16 v[76:79], a[60:63], a[116:119], v[76:79]\n"
  "ds_read_b64_tr_b16 a[92:93], v7 offset:4096\n"
  "s_waitcnt vmcnt(0)\n"
  "s_barrier\n"
  "s_waitcnt lgkmcnt(8)\n"
  "v_mfma_f32_16x16x32_bf16 v[80:83], a[32:35], a[120:123], v[80:83]\n"
  "ds_read_b128 a[96:99], v14 offset:0\n"
  "ds_read_b128 a[104:107], v14 offset:2048\n"
  "v_mfma_f32_16x16x32_bf16 v[84:87], a[40:43], a[120:123], v[84:87]\n"
  "ds_read_b128 a[100:103], v14 offset:1024\n"
  "ds_read_b128 a[108:111], v14 offset:3072\n"
  "v_mfma_f32_16x16x32_bf16 v[88:91], a[48:51], a[120:123], v[88:91]\n"
  "ds_read_b64_tr_b16 a[94:95], v7 offset:6144\n"
  "v_mfma_f32_16x16x32_bf16 v[92:95], a[56:59], a[120:123], v[92:95]\n"
  "v_mfma_f32_16x16x32_bf16 v[80:83], a[36:39], a[124:127], v[80:83]\n"
  "v_mfma_f32_16x16x32_bf16 v[84:87], a[44:47], a[124:127], v[84:87]\n"
  "v_mfma_f32_16x16x32_bf16 v[88:91], a[52:55], a[124:127], v[88:91]\n"
  "v_mfma_f32_16x16x32_bf16 v[92:95], a[60:63], a[124:127], v[92:95]\n"
  "s_waitcnt lgkmcnt(2)\n"
  "v_mfma_f32_16x16x32_bf16 v[32:35], a[64:67], a[96:99], v[32:35]\n"
  "ds_read_b128 a[112:115], v14 offset:4096\n"
  "v_mfma_f32_16x16x32_bf16 v[32:35], a[68:71], a[100:103], v[32:35]\n"
  "ds_read_b128 a[116:119], v14 offset:5120\n"
  "v_mfma_f32_16x16x32_bf16 v[36:39], a[72:75], a[96:99], v[36:39]\n"
  "v_mfma_f32_16x16x32_bf16 v[36:39], a[76:79], a[100:103], v[36:39]\n"
  "v_mfma_f32_16x16x32_bf16 v[40:43], a[80:83], a[96:99], v[40:43]\n"
  "v_mfma_f32_16x16x32_bf16 v[40:43], a[84:87], a[100:103], v[40:43]\n"
  "v_mfma_f32_16x16x32_bf16 v[44:47], a[88:91], a[96:99], v[44:47]\n"
  "s_waitcnt lgkmcnt(2)\n"
  "v_mfma_f32_16x16x32_bf16 v[44:47], a[92:95], a[100:103], v[44:47]\n"
  "v_cvt_pk_bf16_f32 v32, v32, v33\n"
  "v_mfma_f32_16x16x32_bf16 v[48:51], a[64:67], a[104:107], v[48:51]\n"
  "ds_read_b128 a[120:123], v14 offset:6144\n"
  "v_cvt_pk_bf16_f32 v33, v34, v35\n"
  "v_mfma_f32_16x16x32_bf16 v[48:51], a[68:71], a[108:111], v[48:51]\n"
  "ds_read_b128 a[124:127], v14 offset:7168\n"
  "v_cvt_pk_bf16_f32 v34, v36, v37\n"
  "v_mfma_f32_16x16x32_bf16 v[52:55], a[72:75], a[104:107], v[52:55]\n"
  "v_cvt_pk_bf16_f32 v35, v38, v39\n"
  "v_mfma_f32_16x16x32_bf16 v[52:55], a[76:79], a[108:111], v[52:55]\n"
  "buffer_store_dwordx4 v[32:35], v0, s[72:75], s62 offen offset:0 sc1\n"
  "v_mfma_f32_16x16x32_bf16 v[56:59], a[80:83], a[104:107], v[56:59]\n"
  "v_cvt_pk_bf16_f32 v40, v40, v41\n"
  "v_mfma_f32_16x16x32_bf16 v[56:59], a[84:87], a[108:111], v[56:59]\n"
  "v_cvt_pk_bf16_f32 v41, v42, v43\n"
  "v_mfma_f32_16x16x32_bf16 v[60:63], a[88:91], a[104:107], v[60:63]\n"
  "v_cvt_pk_bf16_f32 v42, v44, v45\n"
  "v_mfma_f32_16x16x32_bf16 v[60:63], a[92:95], a[108:111], v[60:63]\n"
  "v_cvt_pk_bf16_f32 v43, v46, v47\n"
  "s_waitcnt lgkmcnt(2)\n"
  "v_mfma_f32_16x16x32_bf16 v[64:67], a[64:67], a[112:115], v[64:67]\n"
  "ds_read_b128 a[96:99], v8 offset:8192\n"
  "buffer_store_dwordx4 v[40:43], v0, s[72:75], s62 offen offset:1024 sc1\n"
  "v_mfma_f32_16x16x32_bf16 v[64:67], a[68:71], a[116:119], v[64:67]\n"
  "ds_read_b128 a[100:103], v8 offset:9216\n"
  "v_cvt_pk_bf16_f32 v48, v48, v49\n"
  "v_mfma_f32_16x16x32_bf16 v[68:71], a[72:75], a[112:115], v[68:71]\n"
  "v_cvt_pk_bf16_f32 v49, v50, v51\n"
  "v_mfma_f32_16x16x32_bf16 v[68:71], a[76:79], a[116:119], v[68:71]\n"
  "v_cvt_pk_bf16_f32 v50, v52, v53\n"
  "v_mfma_f32_16x16x32_bf16 v[72:75], a[80:83], a[112:115], v[72:75]\n"
  "v_cvt_pk_bf16_f32 v51, v54, v55\n"
  "v_mfma_f32_16x16x32_bf16 v[72:75], a[84:87], a[116:119], v[72:75]\n"
  "buffer_store_dwordx4 v[48:51], v0, s[72:75], s62 offen offset:2048 sc1\n"
  "v_mfma_f32_16x16x32_bf16 v[76:79], a[88:91], a[112:115], v[76:79]\n"
  "v_cvt_pk_bf16_f32 v56, v56, v57\n"
  "v_mfma_f32_16x16x32_bf16 v[76:79], a[92:95], a[116:119], v[76:79]\n"
  "v_cvt_pk_bf16_f32 v57, v58, v59\n"
  "s_waitcnt lgkmcnt(2)\n"
  "v_mfma_f32_16x16x32_bf16 v[80:83], a[64:67], a[120:123], v[80:83]\n"
  "ds_read_b128 a[104:107], v8 offset:10240\n"
  "v_cvt_pk_bf16_f32 v58, v60, v61\n"
  "v_mfma_f32_16x16x32_bf16 v[80:83], a[68:71], a[124:127], v[80:83]\n"
  "ds_read_b128 a[108:111], v8 offset:11264\n"
  "v_cvt_pk_bf16_f32 v59, v62, v63\n"
  "v_mfma_f32_16x16x32_bf16 v[84:87], a[72:75], a[120:123], v[84:87]\n"
  "buffer_store_dwordx4 v[56:59], v0, s[72:75], s62 offen offset:3072 sc1\n"
  "v_mfma_f32_16x16x32_bf16 v[84:87], a[76:79], a[124:127], v[84:87]\n"
  "v_cvt_pk_bf16_f32 v64, v64, v65\n"
  "v_mfma_f32_16x16x32_bf16 v[88:91], a[80:83], a[120:123], v[88:91]\n"
  "v_cvt_pk_bf16_f32 v65, v66, v67\n"
  "v_mfma_f32_16x16x32_bf16 v[88:91], a[84:87], a[124:127], v[88:91]\n"
  "v_cvt_pk_bf16_f32 v66, v68, v69\n"
  "v_mfma_f32_16x16x32_bf16 v[92:95], a[88:91], a[120:123], v[92:95]\n"
  "v_cvt_pk_bf16_f32 v67, v70, v71\n"
  "v_mfma_f32_16x16x32_bf16 v[92:95], a[92:95], a[124:127], v[92:95]\n"
  "buffer_store_dwordx4 v[64:67], v0, s[72:75], s63 offen offset:0 sc1\n"
  "s_waitcnt lgkmcnt(2)\n"
  "v_mfma_f32_16x16x32_bf16 v[96:99], a[0:3], a[96:99], v[96:99]\n"
  "ds_read_b128 a[112:115], v13 offset:8192\n"
  "v_cvt_pk_bf16_f32 v72, v72, v73\n"
  "v_mfma_f32_16x16x32_bf16 v[100:103], a[8:11], a[96:99], v[100:103]\n"
  "ds_read_b128 a[116:119], v13 offset:9216\n"
  "v_cvt_pk_bf16_f32 v73, v74, v75\n"
  "v_mfma_f32_16x16x32_bf16 v[104:107], a[16:19], a[96:99], v[104:107]\n"
  "v_cvt_pk_bf16_f32 v74, v76, v77\n"
  "v_mfma_f32_16x16x32_bf16 v[108:111], a[24:27], a[96:99], v[108:111]\n"
  "v_cvt_pk_bf16_f32 v75, v78, v79\n"
  "v_mfma_f32_16x16x32_bf16 v[96:99], a[4:7], a[100:103], v[96:99]\n"
  "buffer_store_dwordx4 v[72:75], v0, s[72:75], s63 offen offset:1024 sc1\n"
  "v_mfma_f32_16x16x32_bf16 v[100:103], a[12:15], a[100:103], v[100:103]\n"
  "v_cvt_pk_bf16_f32 v80, v80, v81\n"
  "v_mfma_f32_16x16x32_bf16 v[104:107], a[20:23], a[100:103], v[104:107]\n"
  "v_cvt_pk_bf16_f32 v81, v82, v83\n"
  "v_mfma_f32_16x16x32_bf16 v[108:111], a[28:31], a[100:103], v[108:111]\n"
  "v_cvt_pk_bf16_f32 v82, v84, v85\n"
  "s_waitcnt lgkmcnt(2)\n"
  "v_mfma_f32_16x16x32_bf16 v[112:115], a[0:3], a[104:107], v[112:115]\n"
  "ds_read_b128 a[120:123], v13 offset:10240\n"
  "v_cvt_pk_bf16_f32 v83, v86, v87\n"
  "v_mfma_f32_16x16x32_bf16 v[116:119], a[8:11], a[104:107], v[116:119]\n"
  "ds_read_b128 a[124:127], v13 offset:11264\n"
  "buffer_store_dwordx4 v[80:83], v0, s[72:75], s63 offen offset:2048 sc1\n"
  "v_mfma_f32_16x16x32_bf16 v[120:123], a[16:19], a[104:107], v[120:123]\n"
  "v_cvt_pk_bf16_f32 v88, v88, v89\n"
  "v_mfma_f32_16x16x32_bf16 v[124:127], a[24:27], a[104:107], v[124:127]\n"
  "v_cvt_pk_bf16_f32 v89, v90, v91\n"
  "v_mfma_f32_16x16x32_bf16 v[112:115], a[4:7], a[108:111], v[112:115]\n"
  "v_cvt_pk_bf16_f32 v90, v92, v93\n"
  "v_mfma_f32_16x16x32_bf16 v[116:119], a[12:15], a[108:111], v[116:119]\n"
  "v_cvt_pk_bf16_f32 v91, v94, v95\n"
  "v_mfma_f32_16x16x32_bf16 v[120:123], a[20:23], a[108:111], v[120:123]\n"
  "buffer_store_dwordx4 v[88:91], v0, s[72:75], s63 offen offset:3072 sc1\n"
  "v_mfma_f32_16x16x32_bf16 v[124:127], a[28:31], a[108:111], v[124:127]\n"
  "s_waitcnt lgkmcnt(2)\n"
  "v_mfma_f32_16x16x32_bf16 v[96:99], a[32:35], a[112:115], v[96:99]\n"
  "ds_read_b128 a[96:99], v14 offset:8192\n"
  "v_mfma_f32_16x16x32_bf16 v[100:103], a[40:43], a[112:115], v[100:103]\n"
  "ds_read_b128 a[100:103], v14 offset:9216\n"
  "v_mfma_f32_16x16x32_bf16 v[104:107], a[48:51], a[112:115], v[104:107]\n"
  "v_mfma_f32_16x16x32_bf16 v[108:111], a[56:59], a[112:115], v[108:111]\n"
  "v_mfma_f32_16x16x32_bf16 v[96:99], a[36:39], a[116:119], v[96:99]\n"
  "v_mfma_f32_16x16x32_bf16 v[100:103], a[44:47], a[116:119], v[100:103]\n"
  "v_mfma_f32_16x16x32_bf16 v[104:107], a[52:55], a[116:119], v[104:107]\n"
  "v_mfma_f32_16x16x32_bf16 v[108:111], a[60:63], a[116:119], v[108:111]\n"
  "s_waitcnt lgkmcnt(2)\n"
  "v_mfma_f32_16x16x32_bf16 v[112:115], a[32:35], a[120:123], v[112:115]\n"
  "ds_read_b128 a[104:107], v14 offset:10240\n"
  "v_mfma_f32_16x16x32_bf16 v[116:119], a[40:43], a[120:123], v[116:119]\n"
  "ds_read_b128 a[108:111], v14 offset:11264\n"
  "v_mfma_f32_16x16x32_bf16 v[120:123], a[48:51], a[120:123], v[120:123]\n"
  "v_mfma_f32_16x16x32_bf16 v[124:127], a[56:59], a[120:123], v[124:127]\n"
  "v_mfma_f32_16x16x32_bf16 v[112:115], a[36:39], a[124:127], v[112:115]\n"
  "v_mfma_f32_16x16x32_bf16 v[116:119], a[44:47], a[124:127], v[116:119]\n"
  "v_mfma_f32_16x16x32_bf16 v[120:123], a[52:55], a[124:127], v[120:123]\n"
  "v_mfma_f32_16x16x32_bf16 v[124:127], a[60:63], a[124:127], v[124:127]\n"
  "s_waitcnt lgkmcnt(2)\n"
  "v_mfma_f32_16x16x32_bf16 v[96:99], a[64:67], a[96:99], v[96:99]\n"
  "ds_read_b128 a[112:115], v8 offset:12288\n"
  "v_mfma_f32_16x16x32_bf16 v[96:99], a[68:71], a[100:103], v[96:99]\n"
  "ds_read_b128 a[116:119], v8 offset:13312\n"
  "v_mfma_f32_16x16x32_bf16 v[100:103], a[72:75], a[96:99], v[100:103]\n"
  "v_mfma_f32_16x16x32_bf16 v[100:103], a[76:79], a[100:103], v[100:103]\n"
  "v_mfma_f32_16x16x32_bf16 v[104:107], a[80:83], a[96:99], v[104:107]\n"
  "v_mfma_f32_16x16x32_bf16 v[104:107], a[84:87], a[100:103], v[104:107]\n"
  "v_mfma_f32_16x16x32_bf16 v[108:111], a[88:91], a[96:99], v[108:111]\n"
  "v_mfma_f32_16x16x32_bf16 v[108:111], a[92:95], a[100:103], v[108:111]\n"
  "s_nop 0\n"
  "v_cvt_pk_bf16_f32 v96, v96, v97\n"
  "s_waitcnt lgkmcnt(2)\n"
  "v_mfma_f32_16x16x32_bf16 v[112:115], a[64:67], a[104:107], v[112:115]\n"
  "ds_read_b128 a[120:123], v8 offset:14336\n"
  "v_cvt_pk_bf16_f32 v97, v98, v99\n"
  "v_mfma_f32_16x16x32_bf16 v[112:115], a[68:71], a[108:111], v[112:115]\n"
  "ds_read_b128 a[124:127], v8 offset:15360\n"
  "v_cvt_pk_bf16_f32 v98, v100, v101\n"
  "v_mfma_f32_16x16x32_bf16 v[116:119], a[72:75], a[104:107], v[116:119]\n"
  "v_cvt_pk_bf16_f32 v99, v102, v103\n"
  "v_mfma_f32_16x16x32_bf16 v[116:119], a[76:79], a[108:111], v[116:119]\n"
  "buffer_store_dwordx4 v[96:99], v0, s[72:75], s64 offen offset:0 sc1\n"
  "v_mfma_f32_16x16x32_bf16 v[120:123], a[80:83], a[104:107], v[120:123]\n"
  "v_cvt_pk_bf16_f32 v104, v104, v105\n"
  "v_mfma_f32_16x16x32_bf16 v[120:123], a[84:87], a[108:111], v[120:123]\n"
  "v_cvt_pk_bf16_f32 v105, v106, v107\n"
  "v_mfma_f32_16x16x32_bf16 v[124:127], a[88:91], a[104:107], v[124:127]\n"
  "v_cvt_pk_bf16_f32 v106, v108, v109\n"
  "v_mfma_f32_16x16x32_bf16 v[124:127], a[92:95], a[108:111], v[124:127]\n"
  "v_cvt_pk_bf16_f32 v107, v110, v111\n"
  "s_waitcnt lgkmcnt(2)\n"
  "v_mfma_f32_16x16x32_bf16 v[128:131], a[0:3], a[112:115], v[128:131]\n"
  "ds_read_b128 a[96:99], v13 offset:12288\n"
  "buffer_store_dwordx4 v[104:107], v0, s[72:75], s64 offen offset:1024 sc1\n"
  "v_mfma_f32_16x16x32_bf16 v[132:135], a[8:11], a[112:115], v[132:135]\n"
  "ds_read_b128 a[100:103], v13 offset:13312\n"
  "v_cvt_pk_bf16_f32 v112, v112, v113\n"
  "v_mfma_f32_16x16x32_bf16 v[136:139], a[16:19], a[112:115], v[136:139]\n"
  "v_cvt_pk_bf16_f32 v113, v114, v115\n"
  "v_mfma_f32_16x16x32_bf16 v[140:143], a[24:27], a[112:115], v[140:143]\n"
  "v_cvt_pk_bf16_f32 v114, v116, v117\n"
  "v_mfma_f32_16x16x32_bf16 v[128:131], a[4:7], a[116:119], v[128:131]\n"
  "v_cvt_pk_bf16_f32 v115, v118, v119\n"
  "v_mfma_f32_16x16x32_bf16 v[132:135], a[12:15], a[116:119], v[132:135]\n"
  "buffer_store_dwordx4 v[112:115], v0, s[72:75], s64 offen offset:2048 sc1\n"
  "v_mfma_f32_16x16x32_bf16 v[136:139], a[20:23], a[116:119], v[136:139]\n"
  "v_cvt_pk_bf16_f32 v120, v120, v121\n"
  "v_mfma_f32_16x16x32_bf16 v[140:143], a[28:31], a[116:119], v[140:143]\n"
  "v_cvt_pk_bf16_f32 v121, v122, v123\n"
  "s_waitcnt lgkmcnt(2)\n"
  "v_mfma_f32_16x16x32_bf16 v[144:147], a[0:3], a[120:123], v[144:147]\n"
  "ds_read_b128 a[104:107], v13 offset:14336\n"
  "v_cvt_pk_bf16_f32 v122, v124, v125\n"
  "v_mfma_f32_16x16x32_bf16 v[148:151], a[8:11], a[120:123], v[148:151]\n"
  "ds_read_b128 a[108:111], v13 offset:15360\n"
  "v_cvt_pk_bf16_f32 v123, v126, v127\n"
  "v_mfma_f32_16x16x32_bf16 v[152:155], a[16:19], a[120:123], v[152:155]\n"
  "buffer_store_dwordx4 v[120:123], v0, s[72:75], s64 offen offset:3072 sc1\n"
  "v_mfma_f32_16x16x32_bf16 v[156:159], a[24:27], a[120:123], v[156:159]\n"
  "v_mfma_f32_16x16x32_bf16 v[144:147], a[4:7], a[124:127], v[144:147]\n"
  "v_mfma_f32_16x16x32_bf16 v[148:151], a[12:15], a[124:127], v[148:151]\n"
  "v_mfma_f32_16x16x32_bf16 v[152:155], a[20:23], a[124:127], v[152:155]\n"
  "v_mfma_f32_16x16x32_bf16 v[156:159], a[28:31], a[124:127], v[156:159]\n"
  "s_waitcnt lgkmcnt(2)\n"
  "v_mfma_f32_16x16x32_bf16 v[128:131], a[32:35], a[96:99], v[128:131]\n"
  "ds_read_b128 a[112:115], v14 offset:12288\n"
  "s_waitcnt vmcnt(8)\n"
  "global_store_dwordx2 v18, v[30:31], s[80:81] offset:0 sc1\n"
  "v_mfma_f32_16x16x32_bf16 v[132:135], a[40:43], a[96:99], v[132:135]\n"
  "ds_read_b128 a[116:119], v14 offset:13312\n"
  "v_mfma_f32_16x16x32_bf16 v[136:139], a[48:51], a[96:99], v[136:139]\n"
  "v_mfma_f32_16x16x32_bf16 v[140:143], a[56:59], a[96:99], v[140:143]\n"
  "v_mfma_f32_16x16x32_bf16 v[128:131], a[36:39], a[100:103], v[128:131]\n"
  "v_mfma_f32_16x16x32_bf16 v[132:135], a[44:47], a[100:103], v[132:135]\n"
  "v_mfma_f32_16x16x32_bf16 v[136:139], a[52:55], a[100:103], v[136:139]\n"
  "v_mfma_f32_16x16x32_bf16 v[140:143], a[60:63], a[100:103], v[140:143]\n"
  "s_waitcnt lgkmcnt(2)\n"
  "v_mfma_f32_16x16x32_bf16 v[144:147], a[32:35], a[104:107], v[144:147]\n"
  "ds_read_b128 a[120:123], v14 offset:14336\n"
  "v_mfma_f32_16x16x32_bf16 v[148:151], a[40:43], a[104:107], v[148:151]\n"
  "ds_read_b128 a[124:127], v14 offset:15360\n"
  "v_mfma_f32_16x16x32_bf16 v[152:155], a[48:51], a[104:107], v[152:155]\n"
  "v_mfma_f32_16x16x32_bf16 v[156:159], a[56:59], a[104:107], v[156:159]\n"
  "v_mfma_f32_16x16x32_bf16 v[144:147], a[36:39], a[108:111], v[144:147]\n"
  "v_mfma_f32_16x16x32_bf16 v[148:151], a[44:47], a[108:111], v[148:151]\n"
  "v_mfma_f32_16x16x32_bf16 v[152:155], a[52:55], a[108:111], v[152:155]\n"
  "v_mfma_f32_16x16x32_bf16 v[156:159], a[60:63], a[108:111], v[156:159]\n"
  "s_waitcnt lgkmcnt(2)\n"
  "v_mfma_f32_16x16x32_bf16 v[128:131], a[64:67], a[112:115], v[128:131]\n"
  "s_waitcnt vmcnt(5)\n"
  "global_store_dwordx2 v18, v[30:31], s[80:81] offset:64 sc1\n"
  "v_mfma_f32_16x16x32_bf16 v[132:135], a[72:75], a[112:115], v[132:135]\n"
  "v_mfma_f32_16x16x32_bf16 v[136:139], a[80:83], a[112:115], v[136:139]\n"
  "v_mfma_f32_16x16x32_bf16 v[140:143], a[88:91], a[112:115], v[140:143]\n"
  "v_mfma_f32_16x16x32_bf16 v[128:131], a[68:71], a[116:119], v[128:131]\n"
  "v_mfma_f32_16x16x32_bf16 v[132:135], a[76:79], a[116:119], v[132:135]\n"
  "v_mfma_f32_16x16x32_bf16 v[136:139], a[84:87], a[116:119], v[136:139]\n"
  "v_mfma_f32_16x16x32_bf16 v[140:143], a[92:95], a[116:119], v[140:143]\n"
  "s_waitcnt lgkmcnt(0)\n"
  "v_mfma_f32_16x16x32_bf16 v[144:147], a[64:67], a[120:123], v[144:147]\n"
  "v_mfma_f32_16x16x32_bf16 v[148:151], a[72:75], a[120:123], v[148:151]\n"
  "v_mfma_f32_16x16x32_bf16 v[152:155], a[80:83], a[120:123], v[152:155]\n"
  "v_mfma_f32_16x16x32_bf16 v[156:159], a[88:91], a[120:123], v[156:159]\n"
  "v_mfma_f32_16x16x32_bf16 v[144:147], a[68:71], a[124:127], v[144:147]\n"
  "s_load_dwordx2 s[24:25], s[82:83], s16\n"
  "v_mfma_f32_16x16x32_bf16 v[148:151], a[76:79], a[124:127], v[148:151]\n"
  "s_load_dwordx2 s[26:27], s[82:83], s17\n"
  "v_mfma_f32_16x16x32_bf16 v[152:155], a[84:87], a[124:127], v[152:155]\n"
  "v_mfma_f32_16x16x32_bf16 v[156:159], a[92:95], a[124:127], v[156:159]\n"
  "s_mov_b32 s60, 0x20000\n"
  "s_mov_b32 s87, 0x3f80\n"
  "s_mov_b32 s88, 0x3f800000\n"
  "s_waitcnt lgkmcnt(0)\n"
  "s_xor_b64 s[24:25], s[24:25], s[10:11]\n"
  "s_xor_b64 s[26:27], s[26:27], s[10:11]\n"
  "s_or_b64 s[24:25], s[24:25], s[26:27]\n"
  "s_cbranch_scc1 K2_SLOW_%=\n"
  "buffer_load_dwordx4 v[160:163], v0, s[72:75], s70 offen offset:0 sc1\n"
  "buffer_load_dwordx4 v[164:167], v0, s[72:75], s70 offen offset:1024 sc1\n"
  "buffer_load_dwordx4 v[168:171], v0, s[72:75], s70 offen offset:2048 sc1\n"
  "buffer_load_dwordx4 v[172:175], v0, s[72:75], s70 offen offset:3072 sc1\n"
  "buffer_load_dwordx4 v[176:179], v0, s[72:75], s71 offen offset:0 sc1\n"
  "buffer_load_dwordx4 v[180:183], v0, s[72:75], s71 offen offset:1024 sc1\n"
  "buffer_load_dwordx4 v[184:187], v0, s[72:75], s71 offen offset:2048 sc1\n"
  "buffer_load_dwordx4 v[188:191], v0, s[72:75], s71 offen offset:3072 sc1\n"
  "s_waitcnt vmcnt(10)\n"
  "global_store_dwordx2 v18, v[30:31], s[80:81] offset:128 sc1\n"
  "K2_JOIN_%=:\n"
  "s_barrier\n"
  "s_waitcnt vmcnt(7)\n"
  "v_dot2_f32_bf16 v128, v160, s87, v128\n"
  "v_dot2_f32_bf16 v129, v160, s88, v129\n"
  "v_dot2_f32_bf16 v130, v161, s87, v130\n"
  "v_dot2_f32_bf16 v131, v161, s88, v131\n"
  "v_dot2_f32_bf16 v132, v162, s87, v132\n"
  "v_dot2_f32_bf16 v133, v162, s88, v133\n"
  "v_dot2_f32_bf16 v134, v163, s87, v134\n"
  "v_dot2_f32_bf16 v135, v163, s88, v135\n"
  "s_waitcnt vmcnt(6)\n"
  "v_dot2_f32_bf16 v136, v164, s87, v136\n"
  "v_dot2_f32_bf16 v137, v164, s88, v137\n"
  "v_dot2_f32_bf16 v138, v165, s87, v138\n"
  "v_dot2_f32_bf16 v139, v165, s88, v139\n"
  "v_dot2_f32_bf16 v140, v166, s87, v140\n"
  "v_dot2_f32_bf16 v141, v166, s88, v141\n"
  "v_dot2_f32_bf16 v142, v167, s87, v142\n"
  "v_dot2_f32_bf16 v143, v167, s88, v143\n"
  "s_waitcnt vmcnt(5)\n"
  "v_dot2_f32_bf16 v144, v168, s87, v144\n"
  "v_dot2_f32_bf16 v145, v168, s88, v145\n"
  "v_dot2_f32_bf16 v146, v169, s87, v146\n"
  "v_dot2_f32_bf16 v147, v169, s88, v147\n"
  "v_dot2_f32_bf16 v148, v170, s87, v148\n"
  "v_dot2_f32_bf16 v149, v170, s88, v149\n"
  "v_dot2_f32_bf16 v150, v171, s87, v150\n"
  "v_dot2_f32_bf16 v151, v171, s88, v151\n"
  "s_waitcnt vmcnt(4)\n"
  "v_dot2_f32_bf16 v152, v172, s87, v152\n"
  "v_dot2_f32_bf16 v153, v172, s88, v153\n"
  "v_dot2_f32_bf16 v154, v173, s87, v154\n"
  "v_dot2_f32_bf16 v155, v173, s88, v155\n"
  "v_dot2_f32_bf16 v156, v174, s87, v156\n"
  "v_dot2_f32_bf16 v157, v174, s88, v157\n"
  "v_dot2_f32_bf16 v158, v175, s87, v158\n"
  "v_dot2_f32_bf16 v159, v175, s88, v159\n"
  "s_load_dwordx2 s[24:25], s[82:83], s98\n"
  "s_waitcnt vmcnt(3)\n"
  "v_dot2_f32_bf16 v128, v176, s87, v128\n"
  "v_dot2_f32_bf16 v129, v176, s88, v129\n"
  "v_dot2_f32_bf16 v130, v177, s87, v130\n"
  "v_dot2_f32_bf16 v131, v177, s88, v131\n"
  "v_dot2_f32_bf16 v132, v178, s87, v132\n"
  "v_dot2_f32_bf16 v133, v178, s88, v133\n"
  "v_dot2_f32_bf16 v134, v179, s87, v134\n"
  "v_dot2_f32_bf16 v135, v179, s88, v135\n"
  "s_waitcnt vmcnt(2)\n"
  "v_dot2_f32_bf16 v136, v180, s87, v136\n"
  "v_dot2_f32_bf16 v137, v180, s88, v137\n"
  "v_dot2_f32_bf16 v138, v181, s87, v138\n"
  "v_dot2_f32_bf16 v139, v181, s88, v139\n"
  "v_dot2_f32_bf16 v140, v182, s87, v140\n"
  "v_dot2_f32_bf16 v141, v182, s88, v141\n"
  "v_dot2_f32_bf16 v142, v183, s87, v142\n"
  "v_dot2_f32_bf16 v143, v183, s88, v143\n"
  "s_waitcnt vmcnt(1)\n"
  "v_dot2_f32_bf16 v144, v184, s87, v144\n"
  "v_dot2_f32_bf16 v145, v184, s88, v145\n"
  "v_dot2_f32_bf16 v146, v185, s87, v146\n"
  "v_dot2_f32_bf16 v147, v185, s88, v147\n"
  "v_dot2_f32_bf16 v148, v186, s87, v148\n"
  "v_dot2_f32_bf16 v149, v186, s88, v149\n"
  "v_dot2_f32_bf16 v150, v187, s87, v150\n"
  "v_dot2_f32_bf16 v151, v187, s88, v151\n"
  "s_waitcnt vmcnt(0)\n"
  "v_dot2_f32_bf16 v152, v188, s87, v152\n"
  "v_dot2_f32_bf16 v153, v188, s88, v153\n"
  "v_dot2_f32_bf16 v154, v189, s87, v154\n"
  "v_dot2_f32_bf16 v155, v189, s88, v155\n"
  "v_dot2_f32_bf16 v156, v190, s87, v156\n"
  "v_dot2_f32_bf16 v157, v190, s88, v157\n"
  "v_dot2_f32_bf16 v158, v191, s87, v158\n"
  "v_dot2_f32_bf16 v159, v191, s88, v159\n"
  "s_waitcnt lgkmcnt(0)\n"
  "s_cmp_eq_u64 s[24:25], s[10:11]\n"
  "s_cbranch_scc1 K2_GOTB_%=\n"
  "K2_POLLB_%=:\n"
  "global_load_dwordx2 v[16:17], v29, s[82:83] sc1\n"
  "s_waitcnt vmcnt(0)\n"
  "v_cmp_ne_u64 vcc, s[10:11], v[16:17]\n"
  "s_sub_u32 s60, s60, 1\n"
  "s_cbranch_scc1 K2_GOTB_%=\n"
  "s_and_b32 s66, vcc_lo, 4\n"
  "s_cbranch_scc0 K2_GOTB_%=\n"
  "s_sleep 1\n"
  "s_branch K2_POLLB_%=\n"
  "K2_GOTB_%=:\n"
  "buffer_load_dwordx4 v[192:195], v0, s[72:75], s33 offen offset:0 sc1\n"
  "buffer_load_dwordx4 v[196:199], v0, s[72:75], s33 offen offset:1024 sc1\n"
  "buffer_load_dwordx4 v[200:203], v0, s[72:75], s33 offen offset:2048 sc1\n"
  "buffer_load_dwordx4 v[204:207], v0, s[72:75], s33 offen offset:3072 sc1\n"
  "K2_LAST_%=:\n"
  "s_waitcnt vmcnt(3)\n"
  "v_dot2_f32_bf16 v128, v192, s87, v128\n"
  "v_dot2_f32_bf16 v129, v192, s88, v129\n"
  "v_dot2_f32_bf16 v130, v193, s87, v130\n"
  "v_dot2_f32_bf16 v131, v193, s88, v131\n"
  "v_dot2_f32_bf16 v132, v194, s87, v132\n"
  "v_dot2_f32_bf16 v133, v194, s88, v133\n"
  "v_dot2_f32_bf16 v134, v195, s87, v134\n"
  "v_dot2_f32_bf16 v135, v195, s88, v135\n"
  "s_waitcnt vmcnt(2)\n"
  "v_dot2_f32_bf16 v136, v196, s87, v136\n"
  "v_dot2_f32_bf16 v137, v196, s88, v137\n"
  "v_dot2_f32_bf16 v138, v197, s87, v138\n"
  "v_dot2_f32_bf16 v139, v197, s88, v139\n"
  "v_dot2_f32_bf16 v140, v198, s87, v140\n"
  "v_dot2_f32_bf16 v141, v198, s88, v141\n"
  "v_dot2_f32_bf16 v142, v199, s87, v142\n"
  "v_dot2_f32_bf16 v143, v199, s88, v143\n"
  "v_mul_f32 v128, v214, v128\n"
  "v_mul_f32 v129, v214, v129\n"
  "v_mul_f32 v130, v214, v130\n"
  "v_mul_f32 v131, v214, v131\n"
  "v_mul_f32 v132, v214, v132\n"
  "v_mul_f32 v133, v214, v133\n"
  "v_mul_f32 v134, v214, v134\n"
  "v_mul_f32 v135, v214, v135\n"
  "v_cvt_pk_bf16_f32 v128, v128, v129\n"
  "v_cvt_pk_bf16_f32 v129, v130, v131\n"
  "v_cvt_pk_bf16_f32 v130, v132, v133\n"
  "v_cvt_pk_bf16_f32 v131, v134, v135\n"
  "ds_write_b64 v2, v[128:129] offset:0\n"
  "ds_write_b64 v2, v[130:131] offset:32\n"
  "s_waitcnt vmcnt(1)\n"
  "v_dot2_f32_bf16 v144, v200, s87, v144\n"
  "v_dot2_f32_bf16 v145, v200, s88, v145\n"
  "v_dot2_f32_bf16 v146, v201, s87, v146\n"
  "v_dot2_f32_bf16 v147, v201, s88, v147\n"
  "v_dot2_f32_bf16 v148, v202, s87, v148\n"
  "v_dot2_f32_bf16 v149, v202, s88, v149\n"
  "v_dot2_f32_bf16 v150, v203, s87, v150\n"
  "v_dot2_f32_bf16 v151, v203, s88, v151\n"
  "v_mul_f32 v136, v214, v136\n"
  "v_mul_f32 v137, v214, v137\n"
  "v_mul_f32 v138, v214, v138\n"
  "v_mul_f32 v139, v214, v139\n"
  "v_mul_f32 v140, v214, v140\n"
  "v_mul_f32 v141, v214, v141\n"
  "v_mul_f32 v142, v214, v142\n"
  "v_mul_f32 v143, v214, v143\n"
  "v_cvt_pk_bf16_f32 v136, v136, v137\n"
  "v_cvt_pk_bf16_f32 v137, v138, v139\n"
  "v_cvt_pk_bf16_f32 v138, v140, v141\n"
  "v_cvt_pk_bf16_f32 v139, v142, v143\n"
  "ds_write_b64 v2, v[136:137] offset:64\n"
  "ds_write_b64 v2, v[138:139] offset:96\n"
  "s_waitcnt vmcnt(0)\n"
  "v_dot2_f32_bf16 v152, v204, s87, v152\n"
  "v_dot2_f32_bf16 v153, v204, s88, v153\n"
  "v_dot2_f32_bf16 v154, v205, s87, v154\n"
  "v_dot2_f32_bf16 v155, v205, s88, v155\n"
  "v_dot2_f32_bf16 v156, v206, s87, v156\n"
  "v_dot2_f32_bf16 v157, v206, s88, v157\n"
  "v_dot2_f32_bf16 v158, v207, s87, v158\n"
  "v_dot2_f32_bf16 v159, v207, s88, v159\n"
  "v_mul_f32 v144, v215, v144\n"
  "v_mul_f32 v145, v215, v145\n"
  "v_mul_f32 v146, v215, v146\n"
  "v_mul_f32 v147, v215, v147\n"
  "v_mul_f32 v148, v215, v148\n"
  "v_mul_f32 v149, v215, v149\n"
  "v_mul_f32 v150, v215, v150\n"
  "v_mul_f32 v151, v215, v151\n"
  "v_cvt_pk_bf16_f32 v144, v144, v145\n"
  "v_cvt_pk_bf16_f32 v145, v146, v147\n"
  "v_cvt_pk_bf16_f32 v146, v148, v149\n"
  "v_cvt_pk_bf16_f32 v147, v150, v151\n"
  "ds_write_b64 v2, v[144:145] offset:2304\n"
  "ds_write_b64 v2, v[146:147] offset:2336\n"
  "v_mul_f32 v152, v215, v152\n"
  "v_mul_f32 v153, v215, v153\n"
  "v_mul_f32 v154, v215, v154\n"
  "v_mul_f32 v155, v215, v155\n"
  "v_mul_f32 v156, v215, v156\n"
  "v_mul_f32 v157, v215, v157\n"
  "v_mul_f32 v158, v215, v158\n"
  "v_mul_f32 v159, v215, v159\n"
  "v_cvt_pk_bf16_f32 v152, v152, v153\n"
  "v_cvt_pk_bf16_f32 v153, v154, v155\n"
  "v_cvt_pk_bf16_f32 v154, v156, v157\n"
  "v_cvt_pk_bf16_f32 v155, v158, v159\n"
  "ds_write_b64 v2, v[152:153] offset:2368\n"
  "ds_write_b64 v2, v[154:155] offset:2400\n"
  "s_waitcnt lgkmcnt(0)\n"
  "ds_read_b128 v[160:163], v3 offset:0\n"
  "ds_read_b128 v[164:167], v3 offset:1152\n"
  "ds_read_b128 v[168:171], v3 offset:2304\n"
  "ds_read_b128 v[172:175], v3 offset:3456\n"
  "s_waitcnt lgkmcnt(3)\n"
  "buffer_store_dwordx4 v[160:163], v19, s[76:79], s68 offen nt\n"
  "s_add_u32 s68, s68, 16384\n"
  "s_waitcnt lgkmcnt(2)\n"
  "buffer_store_dwordx4 v[164:167], v19, s[76:79], s68 offen nt\n"
  "s_add_u32 s68, s68, 16384\n"
  "s_waitcnt lgkmcnt(1)\n"
  "buffer_store_dwordx4 v[168:171], v19, s[76:79], s68 offen nt\n"
  "s_add_u32 s68, s68, 16384\n"
  "s_waitcnt lgkmcnt(0)\n"
  "buffer_store_dwordx4 v[172:175], v19, s[76:79], s68 offen nt\n"
  "s_cmp_lg_u32 s99, 0\n"
  "s_cbranch_scc1 K2_NOBUMP_%=\n"
  "s_add_u32 s28, s96, 21299200\n"
  "s_addc_u32 s29, s97, 0\n"
  "v_lshrrev_b32 v232, 2, v28\n"
  "v_lshlrev_b32 v232, 6, v232\n"
  "v_and_b32 v233, 3, v28\n"
  "v_lshl_or_b32 v232, v233, 3, v232\n"
  "v_add_u32 v233, 1024, v232\n"
  "v_add_u32 v234, 4096, v232\n"
  "v_add_u32 v235, 5120, v232\n"
  "s_mov_b32 s60, 0x20000\n"
  "K2_ALL_%=:\n"
  "global_load_dwordx2 v[236:237], v232, s[28:29] sc1\n"
  "global_load_dwordx2 v[238:239], v233, s[28:29] sc1\n"
  "global_load_dwordx2 v[240:241], v234, s[28:29] sc1\n"
  "global_load_dwordx2 v[242:243], v235, s[28:29] sc1\n"
  "s_waitcnt vmcnt(0)\n"
  "s_mov_b64 s[30:31], 0\n"
  "v_cmp_ne_u64 vcc, s[10:11], v[236:237]\n"
  "s_nop 0\n"
  "s_or_b64 s[30:31], s[30:31], vcc\n"
  "v_cmp_ne_u64 vcc, s[10:11], v[238:239]\n"
  "s_nop 0\n"
  "s_or_b64 s[30:31], s[30:31], vcc\n"
  "v_cmp_ne_u64 vcc, s[10:11], v[240:241]\n"
  "s_nop 0\n"
  "s_or_b64 s[30:31], s[30:31], vcc\n"
  "v_cmp_ne_u64 vcc, s[10:11], v[242:243]\n"
  "s_nop 0\n"
  "s_or_b64 s[30:31], s[30:31], vcc\n"
  "s_sub_u32 s60, s60, 1\n"
  "s_cbranch_scc1 K2_NOBUMP_%=\n"
  "s_cmp_lg_u64 s[30:31], 0\n"
  "s_cbranch_scc1 K2_ALL_%=\n"
  "global_store_dwordx2 v18, v[30:31], s[14:15] sc1\n"
  "K2_NOBUMP_%=:\n"
  "s_branch K2_CODE_END\n"
  "K2_FBH_%=:\n"
  "s_mov_b64 s[24:25], vcc\n"
  "v_add_u32 v214, 4096, v215\n"
  "s_mov_b32 s60, 0x40000\n"
  "K2_SPH2_%=:\n"
  "global_load_dwordx2 v[16:17], v214, s[8:9] sc1\n"
  "s_waitcnt vmcnt(0)\n"
  "v_cmp_ne_u64 vcc, s[10:11], v[16:17]\n"
  "s_sub_u32 s60, s60, 1\n"
  "s_cbranch_scc1 K2_OKH2_%=\n"
  "s_and_b32 s66, vcc_lo, 15\n"
  "s_cbranch_scc0 K2_OKH2_%=\n"
  "s_sleep 1\n"
  "s_branch K2_SPH2_%=\n"
  "K2_OKH2_%=:\n"
  "s_mov_b64 vcc, s[24:25]\n"
  "s_branch K2_OKH_%=\n"
  "K2_FBG_%=:\n"
  "s_mov_b32 s60, 0x40000\n"
  "K2_SPG2_%=:\n"
  "global_load_dwordx2 v[16:17], v13, s[12:13] sc1\n"
  "s_waitcnt vmcnt(0)\n"
  "v_cmp_ne_u64 vcc, s[10:11], v[16:17]\n"
  "s_sub_u32 s60, s60, 1\n"
  "s_cbranch_scc1 K2_OKG2_%=\n"
  "s_and_b32 s66, vcc_lo, 65535\n"
  "s_cbranch_scc0 K2_OKG2_%=\n"
  "s_sleep 1\n"
  "s_branch K2_SPG2_%=\n"
  "K2_OKG2_%=:\n"
  "s_branch K2_OKG_%=\n"
  "K2_BQ_%=:\n"
  "s_add_u32 s28, s96, 21299200\n"
  "s_addc_u32 s29, s97, 0\n"
  "v_lshrrev_b32 v232, 2, v28\n"
  "v_lshlrev_b32 v232, 6, v232\n"
  "v_and_b32 v233, 3, v28\n"
  "v_lshl_or_b32 v232, v233, 3, v232\n"
  "v_add_u32 v233, 1024, v232\n"
  "v_add_u32 v234, 4096, v232\n"
  "v_add_u32 v235, 5120, v232\n"
  "global_load_dwordx2 v[236:237], v232, s[28:29] sc1\n"
  "global_load_dwordx2 v[238:239], v233, s[28:29] sc1\n"
  "global_load_dwordx2 v[240:241], v234, s[28:29] sc1\n"
  "global_load_dwordx2 v[242:243], v235, s[28:29] sc1\n"
  "s_branch K2_BQR_%=\n"
  "K2_BT_%=:\n"
  "s_mov_b64 s[30:31], 0\n"
  "v_cmp_ne_u64 vcc, s[10:11], v[236:237]\n"
  "s_nop 0\n"
  "s_or_b64 s[30:31], s[30:31], vcc\n"
  "v_cmp_ne_u64 vcc, s[10:11], v[238:239]\n"
  "s_nop 0\n"
  "s_or_b64 s[30:31], s[30:31], vcc\n"
  "v_cmp_ne_u64 vcc, s[10:11], v[240:241]\n"
  "s_nop 0\n"
  "s_or_b64 s[30:31], s[30:31], vcc\n"
  "v_cmp_ne_u64 vcc, s[10:11], v[242:243]\n"
  "s_nop 0\n"
  "s_or_b64 s[30:31], s[30:31], vcc\n"
  "s_cbranch_scc1 K2_BTR_%=\n"
  "global_store_dwordx2 v18, v[30:31], s[14:15] sc1\n"
  "s_mov_b32 s99, 1\n"
  "s_branch K2_BTR_%=\n"
  "K2_SLOW_%=:\n"
  "s_waitcnt vmcnt(0)\n"
  "global_store_dwordx2 v18, v[30:31], s[80:81] offset:128 sc1\n"
  "K2_POLLA_%=:\n"
  "global_load_dwordx2 v[16:17], v29, s[82:83] sc1\n"
  "s_waitcnt vmcnt(0)\n"
  "v_cmp_ne_u64 vcc, s[10:11], v[16:17]\n"
  "s_sub_u32 s60, s60, 1\n"
  "s_cbranch_scc1 K2_GOTA_%=\n"
  "s_and_b32 s66, vcc_lo, 3\n"
  "s_cbranch_scc0 K2_GOTA_%=\n"
  "s_sleep 1\n"
  "s_branch K2_POLLA_%=\n"
  "K2_GOTA_%=:\n"
  "buffer_load_dwordx4 v[160:163], v0, s[72:75], s70 offen offset:0 sc1\n"
  "buffer_load_dwordx4 v[164:167], v0, s[72:75], s70 offen offset:1024 sc1\n"
  "buffer_load_dwordx4 v[168:171], v0, s[72:75], s70 offen offset:2048 sc1\n"
  "buffer_load_dwordx4 v[172:175], v0, s[72:75], s70 offen offset:3072 sc1\n"
  "buffer_load_dwordx4 v[176:179], v0, s[72:75], s71 offen offset:0 sc1\n"
  "buffer_load_dwordx4 v[180:183], v0, s[72:75], s71 offen offset:1024 sc1\n"
  "buffer_load_dwordx4 v[184:187], v0, s[72:75], s71 offen offset:2048 sc1\n"
  "buffer_load_dwordx4 v[188:191], v0, s[72:75], s71 offen offset:3072 sc1\n"
  "s_branch K2_JOIN_%=\n"
  "K2_CODE_END:\n"
    : "+{v0}"(tid) : "{s[36:37]}"(karg), "{s38}"(b), "{s39}"(lds) : "memory", "scc", "vcc",
      "s0", "s1", "s2", "s3", "s4", "s5", "s6", "s7", "s8", "s9", "s10", "s11", "s12", "s13", "s14", "s15", "s16", "s17", "s18", "s19", "s20", "s21", "s22", "s23", "s24", "s25", "s26", "s27", "s28", "s29", "s30", "s31", "s33", "s34", "s35", "s40", "s41", "s42", "s43", "s44", "s45", "s46", "s47", "s48", "s49", "s50", "s51", "s52", "s53", "s54", "s55", "s56", "s57", "s58", "s59", "s60", "s61", "s62", "s63", "s64", "s65", "s66", "s67", "s68", "s69", "s70", "s71", "s72", "s73", "s74", "s75", "s76", "s77", "s78", "s79", "s80", "s81", "s82", "s83", "s84", "s85", "s86", "s87", "s88", "s89", "s90", "s91", "s92", "s93", "s94", "s95", "s96", "s97", "s98", "s99",
      "v1", "v2", "v3", "v4", "v5", "v6", "v7", "v8", "v9", "v10", "v11", "v12", "v13", "v14", "v15", "v16", "v17", "v18", "v19", "v20", "v21", "v22", "v23", "v24", "v25", "v26", "v27", "v28", "v29", "v30", "v31", "v32", "v33", "v34", "v35", "v36", "v37", "v38", "v39", "v40", "v41", "v42", "v43", "v44", "v45", "v46", "v47", "v48", "v49", "v50", "v51", "v52", "v53", "v54", "v55", "v56", "v57", "v58", "v59", "v60", "v61", "v62", "v63", "v64", "v65", "v66", "v67", "v68", "v69", "v70", "v71", "v72", "v73", "v74", "v75", "v76", "v77", "v78", "v79", "v80", "v81", "v82", "v83", "v84", "v85", "v86", "v87", "v88", "v89", "v90", "v91", "v92", "v93", "v94", "v95", "v96", "v97", "v98", "v99", "v100", "v101", "v102", "v103", "v104", "v105", "v106", "v107", "v108", "v109", "v110", "v111", "v112", "v113", "v114", "v115", "v116", "v117", "v118", "v119", "v120", "v121", "v122", "v123", "v124", "v125", "v126", "v127", "v128", "v129", "v130", "v131", "v132", "v133", "v134", "v135", "v136", "v137", "v138", "v139", "v140", "v141", "v142", "v143", "v144", "v145", "v146", "v147", "v148", "v149", "v150", "v151", "v152", "v153", "v154", "v155", "v156", "v157", "v158", "v159", "v160", "v161", "v162", "v163", "v164", "v165", "v166", "v167", "v168", "v169", "v170", "v171", "v172", "v173", "v174", "v175", "v176", "v177", "v178", "v179", "v180", "v181", "v182", "v183", "v184", "v185", "v186", "v187", "v188", "v189", "v190", "v191", "v192", "v193", "v194", "v195", "v196", "v197", "v198", "v199", "v200", "v201", "v202", "v203", "v204", "v205", "v206", "v207", "v208", "v209", "v210", "v211", "v212", "v213", "v214", "v215", "v216", "v217", "v218", "v219", "v220", "v221", "v222", "v223", "v224", "v225", "v226", "v227", "v228", "v229", "v230", "v231", "v232", "v233", "v234", "v235", "v236", "v237", "v238", "v239", "v240", "v241", "v242", "v243", "v244", "v245", "v246", "v247", "v248", "v249", "v250", "v251", "v252", "v253", "v254", "v255",
      "a0", "a1", "a2", "a3", "a4", "a5", "a6", "a7", "a8", "a9", "a10", "a11", "a12", "a13", "a14", "a15", "a16", "a17", "a18", "a19", "a20", "a21", "a22", "a23", "a24", "a25", "a26", "a27", "a28", "a29", "a30", "a31", "a32", "a33", "a34", "a35", "a36", "a37", "a38", "a39", "a40", "a41", "a42", "a43", "a44", "a45", "a46", "a47", "a48", "a49", "a50", "a51", "a52", "a53", "a54", "a55", "a56", "a57", "a58", "a59", "a60", "a61", "a62", "a63", "a64", "a65", "a66", "a67", "a68", "a69", "a70", "a71", "a72", "a73", "a74", "a75", "a76", "a77", "a78", "a79", "a80", "a81", "a82", "a83", "a84", "a85", "a86", "a87", "a88", "a89", "a90", "a91", "a92", "a93", "a94", "a95", "a96", "a97", "a98", "a99", "a100", "a101", "a102", "a103", "a104", "a105", "a106", "a107", "a108", "a109", "a110", "a111", "a112", "a113", "a114", "a115", "a116", "a117", "a118", "a119", "a120", "a121", "a122", "a123", "a124", "a125", "a126", "a127", "a128", "a129", "a130", "a131", "a132", "a133", "a134", "a135", "a136", "a137", "a138", "a139", "a140", "a141", "a142", "a143", "a144", "a145", "a146", "a147", "a148", "a149", "a150", "a151", "a152", "a153", "a154", "a155", "a156", "a157", "a158", "a159", "a160", "a161", "a162", "a163", "a164", "a165", "a166", "a167", "a168", "a169", "a170", "a171", "a172", "a173", "a174", "a175", "a176", "a177", "a178", "a179", "a180", "a181", "a182", "a183", "a184", "a185", "a186", "a187", "a188", "a189", "a190", "a191", "a192", "a193", "a194", "a195", "a196", "a197", "a198", "a199", "a200", "a201", "a202", "a203", "a204", "a205", "a206", "a207", "a208", "a209", "a210", "a211", "a212", "a213", "a214", "a215", "a216", "a217", "a218", "a219", "a220", "a221", "a222", "a223", "a224", "a225", "a226", "a227", "a228", "a229", "a230", "a231", "a232", "a233", "a234", "a235", "a236", "a237", "a238", "a239", "a240", "a241", "a242", "a243");
}

extern "C" void kernel_launch(void* const* d_in, const int* in_sizes, int n_in, void* d_out, int out_size, void* d_ws, size_t ws_size, hipStream_t stream) {
  const u16* x = (const u16*)d_in[0]; const u16* wqkv = (const u16*)d_in[1]; const u16* wo = (const u16*)d_in[2];
  const u16* w1 = (const u16*)d_in[4]; const u16* w2 = (const u16*)d_in[5]; const u16* gw = (const u16*)d_in[6];
  char* ws = (char*)d_ws;
  gated_mix_kernel<<<256, 256, 0, stream>>>(x, wqkv, w1, gw, ws, wo, w2, wo, w2, ws, (u16*)d_out);
}
```
